```python
import torch

def _softmax(x, axis=-1):
    return torch.softmax(x, dim=axis)
def _gelu(x):
    return torch.nn.functional.gelu(x, approximate="tanh")
def _silu(x):
    return torch.nn.functional.silu(x)
def _sigmoid(x):
    return torch.sigmoid(x)
def _layernorm(x, axis=-1, eps=1e-5):
    m = x.mean(dim=axis, keepdim=True)
    v = x.var(dim=axis, keepdim=True, unbiased=False)
    return (x - m) / torch.sqrt(v + eps)
def _scatter_add(tgt, idx, src, axis=0):
    return tgt.clone().index_add_(axis, idx, src)
def _segment_sum(src, seg, n):
    out = torch.zeros((n,) + src.shape[1:], dtype=src.dtype)
    return out.index_add_(0, seg, src)
def _pad(x, pw):
    flat = tuple(v for pair in reversed(pw) for v in pair)
    return torch.nn.functional.pad(x, flat)

kernel_name = 'gated_mix__ssm_conv_h3__glu_gated_glu__prompt'

def setup_inputs():

    _w = (lambda a: (lambda t: t.to(torch.bfloat16) if t.is_floating_point() else t)(torch.as_tensor(a)))
    def _A_setup():
        B, S, D = 1, 2048, 640
        ks = 4
        return {
            "q": _w((lambda *s: torch.randn(*s, dtype=torch.bfloat16) * 0.05)(B, S, D)),
            "k": _w((lambda *s: torch.randn(*s, dtype=torch.bfloat16) * 0.4)(B, S, D)),
            "v": _w((lambda *s: torch.randn(*s, dtype=torch.bfloat16) * 0.4)(B, S, D)),
            "shift_k": _w((lambda *s: torch.randn(*s, dtype=torch.bfloat16) * 0.4)(D, ks) * 0.5),
            "ssm_k": _w((lambda *s: torch.randn(*s, dtype=torch.bfloat16) * 1.6)(D, S) * (1.0 / S ** 0.5)),
            "ssm_D": _w((lambda *s: torch.randn(*s, dtype=torch.bfloat16) * 0.4)(D)),
        }
    _A_in = _A_setup()

    def _B_setup():
        B, S, D, F = 1, 2048, 640, 1536
        return {
            "x": _w((lambda *s: torch.randn(*s, dtype=torch.bfloat16) * 0.05)(B, S, D)),
            "w_gate": _w((lambda *s: torch.randn(*s, dtype=torch.bfloat16) * 0.4)(D, F)),
            "w_up": _w((lambda *s: torch.randn(*s, dtype=torch.bfloat16) * 0.05)(D, F)),
            "w_down": _w((lambda *s: torch.randn(*s, dtype=torch.bfloat16) * 0.05)(F, D)),
        }
    _B_in = _B_setup()

    return {
        "A_q": _A_in["q"],
        "A_k": _A_in["k"],
        "A_v": _A_in["v"],
        "A_shift_k": _A_in["shift_k"],
        "A_ssm_k": _A_in["ssm_k"],
        "A_ssm_D": _A_in["ssm_D"],
        "B_x": _B_in["x"],
        "B_w_gate": _B_in["w_gate"],
        "B_w_up": _B_in["w_up"],
        "B_w_down": _B_in["w_down"],
        "gate_w": _w((lambda *s: torch.randn(*s, dtype=torch.bfloat16) * 0.05)(640) * 16.0),
    }

def reference(A_q, A_k, A_v, A_shift_k, A_ssm_k, A_ssm_D, B_x, B_w_gate, B_w_up, B_w_down, gate_w):
    def _A_ref(q, k, v, shift_k, ssm_k, ssm_D):
        B, S, D = q.shape
        ks = shift_k.shape[-1]
        k_pad = _pad(k, ((0, 0), (ks - 1, 0), (0, 0)))
        k_shift = torch.zeros_like(k)
        for t in range(ks):
            k_shift = k_shift + k_pad[:, t:t + S, :] * shift_k[:, ks - 1 - t][None, None, :]
        kv = k_shift * v
        L = 2 * S
        kv_pad = _pad(kv, ((0, 0), (0, S), (0, 0)))
        kf_pad = _pad(ssm_k, ((0, 0), (0, S)))
        kv_f = (lambda x, axes=(-2,-1): torch.fft.rfft2(x.float(), dim=axes))(kv_pad[:, None, :, :], axes=(1, 2))
        kf_f = (lambda x, axes=(-2,-1): torch.fft.rfft2(x.float(), dim=axes))(kf_pad[:, None, :], axes=(1, 2))
        kf_f = (lambda x, *d: x.permute(*d))(kf_f, 1, 2, 0)
        y_f = kv_f * kf_f[None, :, :, :]
        y = (lambda x, s, axes=(-2,-1): torch.fft.irfft2(x, s=s, dim=axes).to(torch.bfloat16))(y_f, (1, L), axes=(1, 2))[:, 0, :S, :]
        y = y + kv * ssm_D[None, None, :]
        return q * y

    def _B_ref(x, w_gate, w_up, w_down):
        g = torch.einsum("bsd,df->bsf", x, w_gate)
        u = torch.einsum("bsd,df->bsf", x, w_up)
        h = _sigmoid(g) * u
        return torch.einsum("bsf,fd->bsd", h, w_down)

    x = A_q
    g = _sigmoid((x * gate_w).sum(-1, keepdims=True))
    a = _A_ref(x, A_k, A_v, A_shift_k, A_ssm_k, A_ssm_D)
    b = _B_ref(x, B_w_gate, B_w_up, B_w_down)
    return g * a + (1 - g) * b
```

```cpp
#include <hip/hip_runtime.h>
#include <hip/hip_bf16.h>

namespace {

typedef unsigned short u16;
typedef __bf16 bf16x8 __attribute__((ext_vector_type(8)));
typedef __bf16 bf16x2 __attribute__((ext_vector_type(2)));
typedef short s16x4 __attribute__((ext_vector_type(4)));
typedef short s16x8 __attribute__((ext_vector_type(8)));
typedef float f32x2 __attribute__((ext_vector_type(2)));
typedef float f32x4 __attribute__((ext_vector_type(4)));
typedef unsigned u32x2 __attribute__((ext_vector_type(2)));
typedef unsigned u32x4 __attribute__((ext_vector_type(4)));
#define LAS __attribute__((address_space(3)))
#define GAS __attribute__((address_space(1)))
typedef LAS char* lptr;

constexpr int S = 2048, D = 640, F = 1536;

constexpr size_t WS_HT = 0;
constexpr size_t WS_WT = WS_HT + (size_t)S * F * 2;
constexpr size_t WS_KVR = WS_WT + (size_t)F * D * 2;
constexpr size_t WS_Y2T = WS_KVR + (size_t)S * D * 2;
constexpr size_t WS_GATE = WS_Y2T + (size_t)S * D * 2;
constexpr size_t WS_CNT = WS_GATE + (size_t)S * 4;

#define MFMA16(a, b, c) __builtin_amdgcn_mfma_f32_16x16x32_bf16((a), (b), (c), 0, 0, 0)
#define WAIT_VM(n) asm volatile("s_waitcnt vmcnt(" #n ")" ::: "memory")
#define WAIT_LGKM0() asm volatile("s_waitcnt lgkmcnt(0)" ::: "memory")
#define BARRIER() do { asm volatile("" ::: "memory"); __builtin_amdgcn_s_barrier(); asm volatile("" ::: "memory"); } while (0)

__device__ __forceinline__ unsigned pack_bf16(float lo, float hi) {
  f32x2 v = {lo, hi};
  return __builtin_bit_cast(unsigned, __builtin_convertvector(v, bf16x2));
}
__device__ __forceinline__ float bf_lo(unsigned w) { return __builtin_bit_cast(float, w << 16); }
__device__ __forceinline__ float bf_hi(unsigned w) { return __builtin_bit_cast(float, w & 0xffff0000u); }
__device__ __forceinline__ float sigmoidf_(float x) {
  return __builtin_amdgcn_rcpf(1.0f + __builtin_amdgcn_exp2f(x * -1.4426950408889634f));
}
__device__ __forceinline__ __amdgpu_buffer_rsrc_t make_rsrc(const void* p, unsigned bytes) {
  return __builtin_amdgcn_make_buffer_rsrc(const_cast<void*>(p), 0, bytes, 0x00020000);
}
#define FENCE() __builtin_amdgcn_sched_barrier(0)
__device__ __forceinline__ void dma16(__amdgpu_buffer_rsrc_t r, unsigned dst, unsigned voff, unsigned soff) {
  asm volatile("s_mov_b32 m0, %0\n\ts_nop 0\n\tbuffer_load_dwordx4 %1, %2, %3 offen lds"
               :: "s"(dst), "v"(voff), "s"(r), "s"(soff) : "memory");
}
__device__ __forceinline__ unsigned lds_addr(lptr p) { return (unsigned)(size_t)p; }
__device__ __forceinline__ s16x4 lds_tr(lptr p) { return __builtin_amdgcn_ds_read_tr16_b64_v4i16((LAS s16x4*)p); }
__device__ __forceinline__ bf16x8 join(s16x4 a, s16x4 b) {
  return __builtin_bit_cast(bf16x8, __builtin_shufflevector(a, b, 0, 1, 2, 3, 4, 5, 6, 7));
}
__device__ __forceinline__ bf16x8 lds_frag(lptr p) { return *(LAS bf16x8*)p; }

constexpr int K1_STAGE = 40960, K1_XB = 0, K1_GB = 16384, K1_UB = 28672;
constexpr int K1_T = 3 * K1_STAGE;
constexpr int K1_WD = K1_T + 10240;
constexpr int K1_LDS = K1_WD + 8192;

struct K1Args {
  const u16* x; const u16* k; const u16* v; const u16* shk; const u16* wg; const u16* wu; const u16* wd; const u16* gw;
  char* ws;
};

struct Frags { bf16x8 x[4]; s16x4 g[3][2]; s16x4 u[3][2]; };

__global__ __launch_bounds__(256, 1) void k1_gemm1(K1Args a) {
  extern __shared__ __attribute__((aligned(16))) char smem[];
  lptr lds = (lptr)smem;
  const int tid = threadIdx.x, lane = tid & 63;
  const int wave = __builtin_amdgcn_readfirstlane(tid >> 6);
  const int bid = blockIdx.x;
  const int xcd = bid & 7, j = bid >> 3;
  const int mi = 8 * (xcd & 1) + (j & 7), fi = 4 * (xcd >> 1) + (j >> 3);

  const __amdgpu_buffer_rsrc_t rx = make_rsrc(a.x, S * D * 2);
  const __amdgpu_buffer_rsrc_t rg = make_rsrc(a.wg, D * F * 2);
  const __amdgpu_buffer_rsrc_t ru = make_rsrc(a.wu, D * F * 2);

  unsigned vx[4], vw[3];
#pragma unroll
  for (int i = 0; i < 4; ++i) {
    const int row = 8 * (4 * wave + i) + (lane >> 3), ch = (lane & 7) ^ ((row >> 1) & 7);
    vx[i] = (unsigned)((128 * mi + row) * (D * 2) + ch * 16);
  }
#pragma unroll
  for (int i = 0; i < 3; ++i) {
    const int slot = 64 * (3 * wave + i) + lane, k = slot / 12, chp = slot - 12 * k, ch = chp ^ (2 * ((k >> 3) & 1));
    vw[i] = (unsigned)(k * (F * 2) + fi * 192 + ch * 16);
  }
  const unsigned lds0 = lds_addr(lds);
  auto stage_piece = [&](int i, int s, int buf) {
    const unsigned b = lds0 + buf * K1_STAGE;
    if (i < 4) dma16(rx, b + K1_XB + (4 * wave + i) * 1024, vx[i], s * 128);
    else if (i < 7) dma16(rg, b + K1_GB + (3 * wave + i - 4) * 1024, vw[i - 4], s * (64 * F * 2));
    else dma16(ru, b + K1_UB + (3 * wave + i - 7) * 1024, vw[i - 7], s * (64 * F * 2));
  };
  auto stage = [&](int s, int buf) {
#pragma unroll
    for (int i = 0; i < 10; ++i) stage_piece(i, s, buf);
  };
  stage(0, 0);
  stage(1, 1);
  stage(2, 2);

  const int wb = bid / 5, wdq = bid - 5 * wb;
  if (bid < 240) {
    const __amdgpu_buffer_rsrc_t rd = make_rsrc(a.wd, F * D * 2);
#pragma unroll
    for (int i = 0; i < 2; ++i) {
      const int slot = 64 * (2 * wave + i) + lane, row = slot >> 4, ch = slot & 15;
      dma16(rd, lds0 + K1_WD + (2 * wave + i) * 1024, (unsigned)((32 * wb + row) * (D * 2) + wdq * 256 + ch * 16), 0);
    }
  }

  {
    const int r0 = 8 * bid + 2 * wave;
    float s0 = 0.f, s1 = 0.f;
    if (lane < 40) {
      const u32x4* gw = (const u32x4*)a.gw;
      const u32x4* x0 = (const u32x4*)(a.x + (size_t)r0 * D);
      const u32x4* x1 = (const u32x4*)(a.x + (size_t)(r0 + 1) * D);
#pragma unroll
      for (int i = 0; i < 2; ++i) {
        const u32x4 w = gw[lane + 40 * i], p = x0[lane + 40 * i], q = x1[lane + 40 * i];
#pragma unroll
        for (int e = 0; e < 4; ++e) {
          s0 += bf_lo(p[e]) * bf_lo(w[e]) + bf_hi(p[e]) * bf_hi(w[e]);
          s1 += bf_lo(q[e]) * bf_lo(w[e]) + bf_hi(q[e]) * bf_hi(w[e]);
        }
      }
    }
#pragma unroll
    for (int m = 32; m >= 1; m >>= 1) { s0 += __shfl_xor(s0, m); s1 += __shfl_xor(s1, m); }
    if (lane == 0) {
      float* gate = (float*)(a.ws + WS_GATE);
      gate[r0] = sigmoidf_(s0);
      gate[r0 + 1] = sigmoidf_(s1);
    }
    if (bid == 0 && tid < 8) ((unsigned*)(a.ws + WS_CNT))[tid * 64] = 0u;
  }

  const int tsi = bid >> 3, tdi = bid & 7;
  if (tid < 160) {
    const int dc = tid % 10, sb = tid / 10;
    const int s0 = 64 * tsi + 4 * sb, d0 = 80 * tdi + 8 * dc;
    u32x4 kr[7], vr[4], wk[4];
#pragma unroll
    for (int r = 0; r < 7; ++r) {
      const int s = s0 - 3 + r;
      kr[r] = (s >= 0) ? *(const u32x4*)(a.k + (size_t)s * D + d0) : (u32x4){0u, 0u, 0u, 0u};
    }
#pragma unroll
    for (int r = 0; r < 4; ++r) vr[r] = *(const u32x4*)(a.v + (size_t)(s0 + r) * D + d0);
#pragma unroll
    for (int i = 0; i < 4; ++i) wk[i] = *(const u32x4*)(a.shk + (size_t)d0 * 4 + i * 8);
#pragma unroll
    for (int c = 0; c < 8; ++c) {
      const unsigned wa = wk[c >> 1][2 * (c & 1)], wb2 = wk[c >> 1][2 * (c & 1) + 1];
      const float w0 = bf_lo(wa), w1 = bf_hi(wa), w2 = bf_lo(wb2), w3 = bf_hi(wb2);
      float kk[7], o[4];
#pragma unroll
      for (int r = 0; r < 7; ++r) kk[r] = (c & 1) ? bf_hi(kr[r][c >> 1]) : bf_lo(kr[r][c >> 1]);
#pragma unroll
      for (int r = 0; r < 4; ++r) {
        const float ks = kk[r] * w3 + kk[r + 1] * w2 + kk[r + 2] * w1 + kk[r + 3] * w0;
        o[r] = ks * ((c & 1) ? bf_hi(vr[r][c >> 1]) : bf_lo(vr[r][c >> 1]));
      }
      const u32x2 pk = {pack_bf16(o[3], o[2]), pack_bf16(o[1], o[0])};
      const int sg = 15 - sb;
      *(LAS u32x2*)(lds + K1_T + (8 * dc + c) * 128 + (((sg >> 1) ^ (dc & 7)) << 4) + ((sg & 1) << 3)) = pk;
    }
  }
  WAIT_VM(0);
  WAIT_LGKM0();
  BARRIER();
  {
    u16* kvr = (u16*)(a.ws + WS_KVR);
#pragma unroll
    for (int i = 0; i < 3; ++i) {
      const int id = tid + 256 * i;
      if (id < 640) {
        const int dl = id >> 3, jj = id & 7;
        const u32x4 val = *(LAS u32x4*)(lds + K1_T + dl * 128 + ((jj ^ ((dl >> 3) & 7)) << 4));
        *(u32x4*)(kvr + (size_t)(80 * tdi + dl) * S + (1984 - 64 * tsi) + 8 * jj) = val;
      }
    }
    if (bid < 240) {
      const int g = lane >> 4, q = (lane & 15) >> 2, p = lane & 3;
#pragma unroll
      for (int i = 0; i < 2; ++i) {
        const int dt = 2 * wave + i;
        lptr b = lds + K1_WD + (4 * g + q) * 256 + 32 * dt + 8 * p;
        const bf16x8 fr = join(lds_tr(b), lds_tr(b + 16 * 256));
        *(bf16x8*)(a.ws + WS_WT + ((size_t)(8 * wdq + dt) * 48 + wb) * 1024 + lane * 16) = fr;
      }
    }
  }

  const int ws_ = wave & 1, wf = wave >> 1;
  const int n = lane & 15, g = lane >> 4, q = n >> 2, p = lane & 3;
  const int xb0 = K1_XB + (64 * ws_ + n) * 128 + ((g ^ (n >> 1)) << 4);
  int wb_[3];
#pragma unroll
  for (int c = 0; c < 3; ++c) wb_[c] = K1_GB + (8 * g + q) * 192 + 8 * p + 32 * ((3 * wf + c) ^ (g & 1));

  auto read_piece = [&](Frags& f, int i, lptr b, int kk) {
    if (i < 4) {
      f.x[i] = lds_frag(b + (xb0 ^ (kk << 6)) + i * 2048);
    } else {
      const int c = (i - 4) >> 2, u = ((i - 4) >> 1) & 1, t = (i - 4) & 1;
      const s16x4 v = lds_tr(b + wb_[c] + kk * (32 * 192) + u * (K1_UB - K1_GB) + t * (4 * 192));
      if (u == 0) f.g[c][t] = v; else f.u[c][t] = v;
    }
  };
  f32x4 ag[4][3], au[4][3];
#pragma unroll
  for (int t = 0; t < 4; ++t)
#pragma unroll
    for (int c = 0; c < 3; ++c) { ag[t][c] = (f32x4){0.f, 0.f, 0.f, 0.f}; au[t][c] = (f32x4){0.f, 0.f, 0.f, 0.f}; }
  auto mma_piece = [&](const Frags& f, int i) {
    const int c = i >> 3, t = (i >> 1) & 3;
    if ((i & 1) == 0) ag[t][c] = MFMA16(join(f.g[c][0], f.g[c][1]), f.x[t], ag[t][c]);
    else au[t][c] = MFMA16(join(f.u[c][0], f.u[c][1]), f.x[t], au[t][c]);
  };
  Frags f0, f1;
#pragma unroll
  for (int i = 0; i < 16; ++i) read_piece(f0, i, lds, 0);
  int buf = 0;
#pragma unroll 1
  for (int s = 0; s < 10; ++s) {
    const int nbuf = (buf == 2) ? 0 : buf + 1;
    lptr cb = lds + buf * K1_STAGE, nb = lds + nbuf * K1_STAGE;
    FENCE();
#pragma unroll
    for (int i = 0; i < 8; ++i) {
      mma_piece(f0, 3 * i); FENCE();
      read_piece(f1, 2 * i, cb, 1); FENCE();
      mma_piece(f0, 3 * i + 1); FENCE();
      read_piece(f1, 2 * i + 1, cb, 1); FENCE();
      mma_piece(f0, 3 * i + 2); FENCE();
    }
    WAIT_LGKM0();
    if (s < 8) WAIT_VM(10); else WAIT_VM(0);
    BARRIER();
    FENCE();
    if (s < 7) {
#pragma unroll
      for (int i = 0; i < 8; ++i) {
        mma_piece(f1, 3 * i); FENCE();
        read_piece(f0, 2 * i, nb, 0); FENCE();
        stage_piece(i, s + 3, buf); FENCE();
        mma_piece(f1, 3 * i + 1); FENCE();
        read_piece(f0, 2 * i + 1, nb, 0); FENCE();
        mma_piece(f1, 3 * i + 2); FENCE();
        if (i < 2) { stage_piece(8 + i, s + 3, buf); FENCE(); }
      }
    } else {
#pragma unroll
      for (int i = 0; i < 8; ++i) {
        mma_piece(f1, 3 * i); FENCE();
        read_piece(f0, 2 * i, nb, 0); FENCE();
        mma_piece(f1, 3 * i + 1); FENCE();
        read_piece(f0, 2 * i + 1, nb, 0); FENCE();
        mma_piece(f1, 3 * i + 2); FENCE();
      }
    }
    buf = nbuf;
  }

  {
    char* ht = a.ws + WS_HT;
#pragma unroll
    for (int t = 0; t < 4; ++t) {
      u32x2 pk[3];
#pragma unroll
      for (int c = 0; c < 3; ++c) {
        float h[4];
#pragma unroll
        for (int r = 0; r < 4; ++r) h[r] = sigmoidf_(ag[t][c][r]) * au[t][c][r];
        pk[c] = (u32x2){pack_bf16(h[0], h[1]), pack_bf16(h[2], h[3])};
      }
      char* base = ht + ((size_t)(8 * mi + 4 * ws_ + t) * 48 + 3 * fi) * 1024 + lane * 16;
      if (wf == 0) {
        *(u32x4*)(base) = (u32x4){pk[0][0], pk[0][1], pk[1][0], pk[1][1]};
        *(u32x2*)(base + 1024) = pk[2];
      } else {
        *(u32x2*)(base + 1024 + 8) = pk[0];
        *(u32x4*)(base + 2048) = (u32x4){pk[1][0], pk[1][1], pk[2][0], pk[2][1]};
      }
    }
  }
}

constexpr int K2_CW = 21248;
constexpr int K2_UR = 4 * 4160;
constexpr int K2_RED = 3 * K2_CW;
constexpr int K2_Y = K2_RED + 4 * 15 * 1024;
constexpr int K2_SYNC = K2_Y + 4 * 2560;
constexpr int K2_LDS = K2_SYNC + 16;

struct K2Args {
  const u16* q; const u16* ssmk; const u16* ssmd;
  char* ws; u16* out;
};

__device__ constexpr int tri_start(int t) { return 8 * t - (t * (t - 1)) / 2; }
__device__ constexpr int tri_row(int p) {
  return p < 8 ? 0 : p < 15 ? 1 : p < 21 ? 2 : p < 26 ? 3 : p < 30 ? 4 : p < 33 ? 5 : p < 35 ? 6 : 7;
}
__device__ __forceinline__ void conv_wave(const K2Args& a, lptr lds, int ch, int lane) {
  const __amdgpu_buffer_rsrc_t rk = make_rsrc(a.ssmk + (size_t)ch * S, S * 2);
  const __amdgpu_buffer_rsrc_t ru = make_rsrc(a.ws + WS_KVR + (size_t)ch * S * 2, S * 2);
  lptr ur = lds + K2_UR;
#pragma unroll
  for (int i = 0; i < 4; ++i)
    dma16(ru, lds_addr(ur) + i * 1024, (unsigned)(i * 1024 + lane * 16), 0);
  if (lane < 32) *(LAS u32x4*)(ur + 4096 + lane * 16) = (u32x4){0u, 0u, 0u, 0u};
#pragma unroll
  for (int it = 0; it < 5; ++it) {
    const int c = lane + 64 * it;
    if (c < 260) {
      const unsigned off = (unsigned)(16 * c - 32);
      const u32x4 d0 = __builtin_bit_cast(u32x4, __builtin_amdgcn_raw_buffer_load_b128(rk, off, 0, 0));
      const u32x2 d1 = __builtin_bit_cast(u32x2, __builtin_amdgcn_raw_buffer_load_b64(rk, off + 16, 0, 0));
      const unsigned e0 = __builtin_amdgcn_alignbit(d0[1], d0[0], 16), e1 = __builtin_amdgcn_alignbit(d0[2], d0[1], 16),
                     e2 = __builtin_amdgcn_alignbit(d0[3], d0[2], 16), e3 = __builtin_amdgcn_alignbit(d1[0], d0[3], 16),
                     e4 = __builtin_amdgcn_alignbit(d1[1], d1[0], 16);
      lptr w = lds + 16 * c;
      *(LAS u32x4*)(w) = d0;
      *(LAS u32x4*)(w + 4160) = (u32x4){e0, e1, e2, e3};
      *(LAS u32x4*)(w + 2 * 4160) = (u32x4){d0[1], d0[2], d0[3], d1[0]};
      *(LAS u32x4*)(w + 3 * 4160) = (u32x4){e1, e2, e3, e4};
    }
  }
  const float dcoef = bf_lo((unsigned)a.ssmd[ch]);
  WAIT_VM(0);
  WAIT_LGKM0();

  const int m = lane & 15, g = lane >> 4;
  const int rho = (m + 1) & 3;
  lptr abase = lds + rho * 4160 + 2 * (8 * g + m - 31 - rho);
  lptr bbase = ur + 2 * (2000 - 16 * m + 8 * g) - 7 * 512;
  f32x4 acc[8];
#pragma unroll
  for (int t = 0; t < 8; ++t) acc[t] = (f32x4){0.f, 0.f, 0.f, 0.f};
  struct CF { s16x4 a[8][2]; bf16x8 b[8]; };
  auto cread = [&](CF& f, int p, int i) {
    if (p < 16) f.a[p >> 1][p & 1] = *(LAS s16x4*)(abase + 64 * i + 512 * (p >> 1) + 8 * (p & 1));
    else f.b[p - 16] = lds_frag(bbase + 64 * i + 512 * (7 - (p - 16)));
  };
  auto cmma = [&](const CF& f, int p) {
    const int t = tri_row(p), u = p - tri_start(t);
    acc[t + u] = MFMA16(join(f.a[t][0], f.a[t][1]), f.b[u], acc[t + u]);
  };
  CF c0, c1;
#pragma unroll
  for (int p = 0; p < 24; ++p) cread(c0, p, 1);
#pragma unroll 1
  for (int i = 1; i <= 8; i += 2) {
    FENCE();
#pragma unroll
    for (int p = 0; p < 12; ++p) {
      cmma(c0, 3 * p); FENCE();
      cread(c1, 2 * p, i + 1); FENCE();
      cmma(c0, 3 * p + 1); FENCE();
      cread(c1, 2 * p + 1, i + 1); FENCE();
      cmma(c0, 3 * p + 2); FENCE();
    }
    const int inext = (i + 2 <= 8) ? i + 2 : 8;
#pragma unroll
    for (int p = 0; p < 12; ++p) {
      cmma(c1, 3 * p); FENCE();
      cread(c0, 2 * p, inext); FENCE();
      cmma(c1, 3 * p + 1); FENCE();
      cread(c0, 2 * p + 1, inext); FENCE();
      cmma(c1, 3 * p + 2); FENCE();
    }
  }
  GAS unsigned long long* y2 = (GAS unsigned long long*)(a.ws + WS_Y2T + (size_t)ch * S * 2);
#pragma unroll
  for (int t = 0; t < 8; ++t) {
    const int s = 256 * t + 16 * m + 4 * g;
    const u32x2 kv = *(LAS u32x2*)(ur + 2 * (2044 - s));
    const float y0 = acc[t][0] + bf_hi(kv[1]) * dcoef, y1 = acc[t][1] + bf_lo(kv[1]) * dcoef;
    const float y2v = acc[t][2] + bf_hi(kv[0]) * dcoef, y3 = acc[t][3] + bf_lo(kv[0]) * dcoef;
    const unsigned long long pk = (unsigned long long)pack_bf16(y0, y1) | ((unsigned long long)pack_bf16(y2v, y3) << 32);
    __hip_atomic_store(y2 + (s >> 2), pk, __ATOMIC_RELAXED, __HIP_MEMORY_SCOPE_AGENT);
  }
  WAIT_VM(0);
  if (lane == 0)
    __hip_atomic_fetch_add((GAS unsigned*)(a.ws + WS_CNT) + (ch / 80) * 64, 1u, __ATOMIC_RELAXED, __HIP_MEMORY_SCOPE_AGENT);
}

__device__ __forceinline__ void lds_rendezvous(LAS unsigned* word, unsigned target, int lane) {
  WAIT_LGKM0();
  if (lane == 0) __hip_atomic_fetch_add(word, 1u, __ATOMIC_RELAXED, __HIP_MEMORY_SCOPE_WORKGROUP);
  for (unsigned spins = 0; spins < (1u << 22); ++spins) {
    const unsigned v = __builtin_amdgcn_readfirstlane(__hip_atomic_load(word, __ATOMIC_RELAXED, __HIP_MEMORY_SCOPE_WORKGROUP));
    if (v >= target) break;
    __builtin_amdgcn_s_sleep(1);
  }
  asm volatile("" ::: "memory");
}

__global__ __launch_bounds__(448, 1) void k2_gemm2_conv(K2Args a) {
  extern __shared__ __attribute__((aligned(16))) char smem[];
  lptr lds = (lptr)smem;
  const int tid = threadIdx.x, lane = tid & 63;
  const int wave = __builtin_amdgcn_readfirstlane(tid >> 6);
  const int bid = blockIdx.x;
  if (tid == 0) *(LAS unsigned*)(lds + K2_SYNC) = 0u;
  WAIT_LGKM0();
  BARRIER();

  if (wave >= 4) {
    const int cw = wave - 4;
    const int c0 = (5 * bid) >> 1, cnt = 2 + (bid & 1);
    if (cw < cnt) conv_wave(a, lds + cw * K2_CW, c0 + cw, lane);
    return;
  }

  const int xcd = bid & 7, j = bid >> 3;
  const int si = 8 * (xcd & 3) + (j & 7), di = 4 * (xcd >> 2) + (j >> 3);
  const int n = lane & 15, g = lane >> 4;
  const __amdgpu_buffer_rsrc_t rh = make_rsrc(a.ws + WS_HT, S * F * 2);
  const __amdgpu_buffer_rsrc_t rw = make_rsrc(a.ws + WS_WT, F * D * 2);
  const unsigned voff = lane * 16;
  const int hbase = (4 * si * 48 + 12 * wave) * 1024, wbase = (5 * di * 48 + 12 * wave) * 1024;

  const int srow = 64 * si + 16 * wave + n;
  u32x2 qv[5];
#pragma unroll
  for (int c = 0; c < 5; ++c) qv[c] = *(const u32x2*)(a.q + (size_t)srow * D + 80 * di + 16 * c + 4 * g);
  const float gate = ((const float*)(a.ws + WS_GATE))[srow];

  struct Step { bf16x8 h[4]; bf16x8 w[5]; };
  auto load_piece = [&](Step& st, int i, int b) {
    if (i < 4) st.h[i] = __builtin_bit_cast(bf16x8, __builtin_amdgcn_raw_buffer_load_b128(rh, voff, hbase + (i * 48 + b) * 1024, 0));
    else st.w[i - 4] = __builtin_bit_cast(bf16x8, __builtin_amdgcn_raw_buffer_load_b128(rw, voff, wbase + ((i - 4) * 48 + b) * 1024, 0));
  };
  f32x4 acc[4][5];
#pragma unroll
  for (int t = 0; t < 4; ++t)
#pragma unroll
    for (int c = 0; c < 5; ++c) acc[t][c] = (f32x4){0.f, 0.f, 0.f, 0.f};
  auto step = [&](const Step& cur, Step& nxt, int bn, bool ld) {
#pragma unroll
    for (int i = 0; i < 10; ++i) {
      acc[(2 * i) & 3][(2 * i) >> 2] = MFMA16(cur.w[(2 * i) >> 2], cur.h[(2 * i) & 3], acc[(2 * i) & 3][(2 * i) >> 2]); FENCE();
      if (ld && i < 9) { load_piece(nxt, i, bn); FENCE(); }
      acc[(2 * i + 1) & 3][(2 * i + 1) >> 2] = MFMA16(cur.w[(2 * i + 1) >> 2], cur.h[(2 * i + 1) & 3], acc[(2 * i + 1) & 3][(2 * i + 1) >> 2]); FENCE();
    }
  };
  Step s0, s1, s2;
#pragma unroll
  for (int i = 0; i < 9; ++i) load_piece(s0, i, 0);
#pragma unroll
  for (int i = 0; i < 9; ++i) load_piece(s1, i, 1);
  FENCE();
#pragma unroll 1
  for (int b = 0; b < 9; b += 3) {
    step(s0, s2, b + 2, true);
    step(s1, s0, b + 3, true);
    step(s2, s1, b + 4, true);
  }
  step(s0, s2, 11, true);
  step(s1, s0, 0, false);
  step(s2, s0, 0, false);

  f32x4 b[5];
  {
    lptr red = lds + K2_RED + lane * 16;
#pragma unroll
    for (int t = 0; t < 4; ++t) {
      if (t != wave) {
#pragma unroll
        for (int c = 0; c < 5; ++c) *(LAS f32x4*)(red + ((t * 3 + ((wave - t - 1) & 3)) * 5 + c) * 1024) = acc[t][c];
      } else {
#pragma unroll
        for (int c = 0; c < 5; ++c) b[c] = acc[t][c];
      }
    }
    lds_rendezvous((LAS unsigned*)(lds + K2_SYNC), 4u, lane);
#pragma unroll
    for (int w = 0; w < 3; ++w)
#pragma unroll
      for (int c = 0; c < 5; ++c) b[c] += *(LAS f32x4*)(red + ((wave * 3 + w) * 5 + c) * 1024);
  }

  {
    GAS unsigned* cnt = (GAS unsigned*)(a.ws + WS_CNT) + di * 64;
    for (unsigned spins = 0; spins < (1u << 20); ++spins) {
      const unsigned v = __builtin_amdgcn_readfirstlane(__hip_atomic_load(cnt, __ATOMIC_RELAXED, __HIP_MEMORY_SCOPE_AGENT));
      if (v >= 80u) break;
      __builtin_amdgcn_s_sleep(2);
    }
    __builtin_amdgcn_fence(__ATOMIC_ACQUIRE, "agent");
    WAIT_VM(0);
  }
  lptr yb = lds + K2_Y + wave * 2560;
  {
    const __amdgpu_buffer_rsrc_t ry = make_rsrc(a.ws + WS_Y2T, S * D * 2);
#pragma unroll
    for (int i = 0; i < 3; ++i) {
      const int slot = lane + 64 * i;
      if (slot < 160)
        dma16(ry, lds_addr(yb) + i * 1024,
              (unsigned)((80 * di + (slot >> 1)) * (S * 2) + (64 * si + 16 * wave) * 2 + (slot & 1) * 16), 0);
    }
    WAIT_VM(0);
  }
  {
    const int q = n >> 2, p = lane & 3;
#pragma unroll
    for (int c = 0; c < 5; ++c) {
      const s16x4 yv = __builtin_amdgcn_ds_read_tr16_b64_v4i16((LAS s16x4*)(yb + (16 * c + 4 * g + q) * 32 + 8 * p));
      const u32x2 y = __builtin_bit_cast(u32x2, yv);
      const float o0 = gate * (bf_lo(qv[c][0]) * bf_lo(y[0])) + (1.f - gate) * b[c][0];
      const float o1 = gate * (bf_hi(qv[c][0]) * bf_hi(y[0])) + (1.f - gate) * b[c][1];
      const float o2 = gate * (bf_lo(qv[c][1]) * bf_lo(y[1])) + (1.f - gate) * b[c][2];
      const float o3 = gate * (bf_hi(qv[c][1]) * bf_hi(y[1])) + (1.f - gate) * b[c][3];
      *(u32x2*)(a.out + (size_t)srow * D + 80 * di + 16 * c + 4 * g) = (u32x2){pack_bf16(o0, o1), pack_bf16(o2, o3)};
    }
  }
}

}

extern "C" void kernel_launch(void* const* d_in, const int* in_sizes, int n_in, void* d_out, int out_size, void* d_ws,
                              size_t ws_size, hipStream_t stream) {
  (void)in_sizes; (void)n_in; (void)out_size; (void)ws_size;
  K1Args a1;
  a1.x = (const u16*)d_in[0];
  a1.k = (const u16*)d_in[1];
  a1.v = (const u16*)d_in[2];
  a1.shk = (const u16*)d_in[3];
  a1.wg = (const u16*)d_in[7];
  a1.wu = (const u16*)d_in[8];
  a1.wd = (const u16*)d_in[9];
  a1.gw = (const u16*)d_in[10];
  a1.ws = (char*)d_ws;
  K2Args a2;
  a2.q = (const u16*)d_in[0];
  a2.ssmk = (const u16*)d_in[4];
  a2.ssmd = (const u16*)d_in[5];
  a2.ws = (char*)d_ws;
  a2.out = (u16*)d_out;
  (void)hipFuncSetAttribute((const void*)k1_gemm1, hipFuncAttributeMaxDynamicSharedMemorySize, K1_LDS);
  (void)hipFuncSetAttribute((const void*)k2_gemm2_conv, hipFuncAttributeMaxDynamicSharedMemorySize, K2_LDS);
  k1_gemm1<<<256, 256, K1_LDS, stream>>>(a1);
  k2_gemm2_conv<<<256, 448, K2_LDS, stream>>>(a2);
}
```

```cpp
#include <hip/hip_runtime.h>
#include <hip/hip_bf16.h>

namespace {

typedef unsigned short u16;
typedef __bf16 bf16x8 __attribute__((ext_vector_type(8)));
typedef __bf16 bf16x2 __attribute__((ext_vector_type(2)));
typedef short s16x4 __attribute__((ext_vector_type(4)));
typedef float f32x2 __attribute__((ext_vector_type(2)));
typedef float f32x4 __attribute__((ext_vector_type(4)));
typedef unsigned u32x2 __attribute__((ext_vector_type(2)));
typedef unsigned u32x4 __attribute__((ext_vector_type(4)));
#define LAS __attribute__((address_space(3)))
#define GAS __attribute__((address_space(1)))
typedef LAS char* lptr;

constexpr int S = 2048, D = 640, F = 1536;
constexpr int SC1 = 16;

constexpr size_t WS_HT = 0;
constexpr size_t WS_WT = WS_HT + (size_t)S * F * 2;
constexpr size_t WS_KVR = WS_WT + (size_t)F * D * 2;
constexpr size_t WS_Y2T = WS_KVR + (size_t)S * D * 2;
constexpr size_t WS_GATE = WS_Y2T + (size_t)S * D * 2;
constexpr size_t WS_FLAGKV = WS_GATE + (size_t)S * 4;
constexpr size_t WS_FLAGW = WS_FLAGKV + 1024;
constexpr size_t WS_FLAGH = WS_FLAGW + 1280 + 256;
constexpr size_t WS_FLAGY = WS_FLAGH + 1024;
constexpr size_t WS_EPOCH = WS_FLAGY + 1024;
constexpr size_t WS_END = WS_EPOCH + 256;

constexpr int STAGE = 40960, XB = 0, GB = 16384, UB = 28672;
constexpr int L_AUX = 3 * STAGE;
constexpr int L_T = L_AUX, L_WD = L_AUX + 10240;
constexpr int UR_BYTES = 4608;
constexpr int L_Y = L_AUX + 18432;
constexpr int L_SYNC = L_Y + 10240;
constexpr int L_DUMMY = L_SYNC + 128;
constexpr int L_END = L_DUMMY + 256;
constexpr int KC_COPY = 4160, KC_BYTES = 4 * KC_COPY;
enum { SY_LANDED = 0, SY_CONSUMED = 4, SY_KV = 8, SY_SIDE = 9, SY_HDONE = 10, SY_RED = 11, SY_CONV = 12, SY_AREADY = 13,
       SY_HREADY = 14, SY_YREADY = 15, SY_TAG = 16, SY_TAGOK = 17, SY_WREADY = 18 };

#define STAMP(i) do {} while (0)
#define STAMPC(i) do {} while (0)
#define STAMPI(i) do {} while (0)
#define STAMP_INIT() do {} while (0)
#define STAMP_DUMP() do {} while (0)
constexpr int LDS_BYTES = L_END;

#define MFMA16(a, b, c) __builtin_amdgcn_mfma_f32_16x16x32_bf16((a), (b), (c), 0, 0, 0)
#define WAIT_VM(n) asm volatile("s_waitcnt vmcnt(" #n ")" ::: "memory")
#define WAIT_LGKM0() asm volatile("s_waitcnt lgkmcnt(0)" ::: "memory")
#define BARRIER() do { asm volatile("" ::: "memory"); __builtin_amdgcn_s_barrier(); asm volatile("" ::: "memory"); } while (0)
#define FENCE() __builtin_amdgcn_sched_barrier(0)
constexpr unsigned SPIN_LDS = 1u << 22, SPIN_MEM = 1u << 16;

__device__ __forceinline__ unsigned pack_bf16(float lo, float hi) {
  f32x2 v = {lo, hi};
  return __builtin_bit_cast(unsigned, __builtin_convertvector(v, bf16x2));
}
__device__ __forceinline__ float bf_lo(unsigned w) { return __builtin_bit_cast(float, w << 16); }
__device__ __forceinline__ float bf_hi(unsigned w) { return __builtin_bit_cast(float, w & 0xffff0000u); }
__device__ __forceinline__ float sigmoidf_(float x) {
  return __builtin_amdgcn_rcpf(1.0f + __builtin_amdgcn_exp2f(x * -1.4426950408889634f));
}
__device__ __forceinline__ __amdgpu_buffer_rsrc_t make_rsrc(const void* p, unsigned bytes) {
  return __builtin_amdgcn_make_buffer_rsrc(const_cast<void*>(p), 0, bytes, 0x00020000);
}
__device__ __forceinline__ unsigned lds_addr(lptr p) { return (unsigned)(size_t)p; }
__device__ __forceinline__ void dma16(__amdgpu_buffer_rsrc_t r, unsigned dst, unsigned voff, unsigned soff) {
  asm volatile("s_mov_b32 m0, %0\n\ts_nop 0\n\tbuffer_load_dwordx4 %1, %2, %3 offen lds"
               :: "s"(dst), "v"(voff), "s"(r), "s"(soff) : "memory");
}
template <int N>
__device__ __forceinline__ void dma16xN(__amdgpu_buffer_rsrc_t r, unsigned dst, const unsigned* v, unsigned soff) {
  static_assert(N >= 1 && N <= 4, "12-bit instruction offset");
  if constexpr (N == 1)
    asm volatile("s_mov_b32 m0, %0\n\ts_nop 0\n\tbuffer_load_dwordx4 %1, %2, %3 offen lds"
                 :: "s"(dst), "v"(v[0]), "s"(r), "s"(soff) : "memory");
  else if constexpr (N == 2)
    asm volatile("s_mov_b32 m0, %0\n\ts_nop 0\n\tbuffer_load_dwordx4 %1, %3, %4 offen lds\n\t"
                 "buffer_load_dwordx4 %2, %3, %4 offen offset:1024 lds"
                 :: "s"(dst), "v"(v[0]), "v"(v[1]), "s"(r), "s"(soff) : "memory");
  else if constexpr (N == 3)
    asm volatile("s_mov_b32 m0, %0\n\ts_nop 0\n\tbuffer_load_dwordx4 %1, %4, %5 offen lds\n\t"
                 "buffer_load_dwordx4 %2, %4, %5 offen offset:1024 lds\n\tbuffer_load_dwordx4 %3, %4, %5 offen offset:2048 lds"
                 :: "s"(dst), "v"(v[0]), "v"(v[1]), "v"(v[2]), "s"(r), "s"(soff) : "memory");
  else
    asm volatile("s_mov_b32 m0, %0\n\ts_nop 0\n\tbuffer_load_dwordx4 %1, %5, %6 offen lds\n\t"
                 "buffer_load_dwordx4 %2, %5, %6 offen offset:1024 lds\n\tbuffer_load_dwordx4 %3, %5, %6 offen offset:2048 lds\n\t"
                 "buffer_load_dwordx4 %4, %5, %6 offen offset:3072 lds"
                 :: "s"(dst), "v"(v[0]), "v"(v[1]), "v"(v[2]), "v"(v[3]), "s"(r), "s"(soff) : "memory");
}
__device__ __forceinline__ void touch(const void* p, unsigned dummy_lds) {
  asm volatile("s_mov_b32 m0, %0\n\ts_nop 0\n\tglobal_load_lds_dword %1, off" :: "s"(dummy_lds), "v"(p) : "memory");
}
__device__ __forceinline__ s16x4 lds_tr(lptr p) { return __builtin_amdgcn_ds_read_tr16_b64_v4i16((LAS s16x4*)p); }
__device__ __forceinline__ bf16x8 join(s16x4 a, s16x4 b) {
  return __builtin_bit_cast(bf16x8, __builtin_shufflevector(a, b, 0, 1, 2, 3, 4, 5, 6, 7));
}
__device__ __forceinline__ bf16x8 lds_frag(lptr p) { return *(LAS bf16x8*)p; }
__device__ __forceinline__ u32x4 ld128(__amdgpu_buffer_rsrc_t r, unsigned voff, unsigned soff, int aux) {
  return aux ? __builtin_bit_cast(u32x4, __builtin_amdgcn_raw_buffer_load_b128(r, voff, soff, SC1))
             : __builtin_bit_cast(u32x4, __builtin_amdgcn_raw_buffer_load_b128(r, voff, soff, 0));
}

__device__ __forceinline__ LAS unsigned* sy(lptr lds, int i) { return (LAS unsigned*)(lds + L_SYNC) + i; }
__device__ __forceinline__ unsigned sy_get(lptr lds, int i) {
  return __builtin_amdgcn_readfirstlane(__hip_atomic_load(sy(lds, i), __ATOMIC_RELAXED, __HIP_MEMORY_SCOPE_WORKGROUP));
}
__device__ __forceinline__ void wake() { asm volatile("s_wakeup" ::: "memory"); }
__device__ __forceinline__ void sy_set(lptr lds, int i, unsigned v, int lane) {
  if (lane == 0) __hip_atomic_store(sy(lds, i), v, __ATOMIC_RELAXED, __HIP_MEMORY_SCOPE_WORKGROUP);
  asm volatile("s_waitcnt lgkmcnt(0)\n\ts_wakeup" ::: "memory");
}
#define TOUCH_H 0
#define TOUCH1 0
#define FILT_ITER 4
#define W7_DELAY 0
#define POLL_AFTER 7
#define LOADER_PRIO 0
#define SLEEP_LDS 4
__device__ __forceinline__ void sy_post(unsigned addr, unsigned v) {
  asm volatile("s_mov_b64 exec, 1\n\tds_write_b32 %0, %1\n\ts_mov_b64 exec, -1" :: "v"(addr), "v"(v) : "memory");
}
__device__ __forceinline__ void sy_wait(lptr lds, int i, unsigned v) {
#pragma unroll 1
  for (unsigned n = 0; n < SPIN_LDS; ++n) {
    if (sy_get(lds, i) >= v) break;
    __builtin_amdgcn_s_sleep(SLEEP_LDS);
  }
  asm volatile("" ::: "memory");
}
__device__ __forceinline__ unsigned sy_arrive(lptr lds, int i, int lane) {
  WAIT_LGKM0();
  unsigned old = 0;
  if (lane == 0) old = __hip_atomic_fetch_add(sy(lds, i), 1u, __ATOMIC_RELAXED, __HIP_MEMORY_SCOPE_WORKGROUP);
  old = __builtin_amdgcn_readfirstlane(old);
  wake();
  return old;
}
__device__ __forceinline__ unsigned min4(u32x4 v) { return min(min(v[0], v[1]), min(v[2], v[3])); }
__device__ __forceinline__ u32x4 sy_get4(lptr lds, int i) { return *(volatile LAS u32x4*)(lds + L_SYNC + 4 * i); }
__device__ __forceinline__ u32x2 sy_get2(lptr lds, int i) { return *(volatile LAS u32x2*)(lds + L_SYNC + 4 * i); }
__device__ __forceinline__ void sy_wait4(lptr lds, int i, unsigned v) {
#pragma unroll 1
  for (unsigned n = 0; n < SPIN_LDS; ++n) {
    if ((unsigned)__builtin_amdgcn_readfirstlane(min4(sy_get4(lds, i))) >= v) break;
    __builtin_amdgcn_s_sleep(SLEEP_LDS);
  }
  asm volatile("" ::: "memory");
}
#define LOADER_SLEEP 1
__device__ __forceinline__ void loader_wait(lptr lds, unsigned v) {
#pragma unroll 1
  for (unsigned n = 0; n < SPIN_LDS; ++n) {
    if ((unsigned)__builtin_amdgcn_readfirstlane(min4(sy_get4(lds, SY_CONSUMED))) >= v) break;
    if (LOADER_SLEEP) __builtin_amdgcn_s_sleep(LOADER_SLEEP);
  }
  asm volatile("" ::: "memory");
}
__device__ __forceinline__ void sy_wait4m(lptr lds, unsigned v) {
#pragma unroll 1
  for (unsigned n = 0; n < SPIN_LDS; ++n) {
    const u32x4 l = sy_get4(lds, SY_LANDED);
    if ((unsigned)__builtin_amdgcn_readfirstlane(min(min(l[0], l[1]), l[2])) >= v) break;
    __builtin_amdgcn_s_sleep(SLEEP_LDS);
  }
  asm volatile("" ::: "memory");
}
__device__ __forceinline__ unsigned get_tag(lptr lds) {
  sy_wait(lds, SY_TAGOK, 1u);
  return sy_get(lds, SY_TAG);
}
__device__ __forceinline__ void flag_store(char* ws, size_t off, unsigned tag) {
  __hip_atomic_store((GAS unsigned*)(ws + off), tag, __ATOMIC_RELAXED, __HIP_MEMORY_SCOPE_AGENT);
}
#define SIDE_PUB_ITER 3
#define POLL_SLEEP 32
__device__ __forceinline__ void publish_side(char* ws, int bid, unsigned tag, int lane) {
  if (lane == 0) flag_store(ws, WS_FLAGKV + 4 * (32 * (bid & 7) + (bid >> 3)), tag);
  if (lane == 1 && bid < 240) flag_store(ws, WS_FLAGW + 4 * (64 * (bid % 5) + bid / 5), tag);
}
__device__ __forceinline__ void flag_wait(char* ws, size_t off, bool on, unsigned tag) {
#pragma unroll 1
  for (unsigned n = 0; n < SPIN_MEM; ++n) {
    const unsigned v = __hip_atomic_load((GAS unsigned*)(ws + off), __ATOMIC_RELAXED, __HIP_MEMORY_SCOPE_AGENT);
    if (__all(!on || v == tag)) break;
    __builtin_amdgcn_s_sleep(POLL_SLEEP);
  }
  asm volatile("" ::: "memory");
}

struct Args {
  const u16* x; const u16* k; const u16* v; const u16* shk; const u16* ssmk; const u16* ssmd;
  const u16* wg; const u16* wu; const u16* wd; const u16* gw;
  char* ws; u16* out;
};

struct Frags { bf16x8 x[4]; s16x4 g[3][2]; s16x4 u[3][2]; };

__device__ constexpr int tri_start(int t) { return 8 * t - (t * (t - 1)) / 2; }
__device__ constexpr int tri_row(int p) {
  return p < 8 ? 0 : p < 15 ? 1 : p < 21 ? 2 : p < 26 ? 3 : p < 30 ? 4 : p < 33 ? 5 : p < 35 ? 6 : 7;
}

template <int N> __device__ __forceinline__ void wait_vm() { asm volatile("s_waitcnt vmcnt(%0)" ::"n"(N) : "memory"); }

struct Filter { unsigned d[5][6]; unsigned dbits; };
__device__ __forceinline__ void load_filter(const Args& a, Filter& f, int ch, int lane) {
  const __amdgpu_buffer_rsrc_t rk = make_rsrc(a.ssmk + (size_t)ch * S, S * 2);
#pragma unroll
  for (int it = 0; it < 5; ++it) {
    const unsigned off = (unsigned)(16 * (lane + 64 * it) - 32);
    const u32x4 v0 = __builtin_bit_cast(u32x4, __builtin_amdgcn_raw_buffer_load_b128(rk, off, 0, 0));
    const u32x2 v1 = __builtin_bit_cast(u32x2, __builtin_amdgcn_raw_buffer_load_b64(rk, off + 16, 0, 0));
    f.d[it][0] = v0[0]; f.d[it][1] = v0[1]; f.d[it][2] = v0[2]; f.d[it][3] = v0[3]; f.d[it][4] = v1[0]; f.d[it][5] = v1[1];
  }
  f.dbits = a.ssmd[ch];
}

template <int NX>
__device__ __forceinline__ void loader(const Args& a, lptr lds, int lw, int x0, int mi, int fi, int xcd, int j, Filter& flt, int ch, int lane) {
  const unsigned lds0 = lds_addr(lds);
  const __amdgpu_buffer_rsrc_t rx = make_rsrc(a.x, S * D * 2);
  const __amdgpu_buffer_rsrc_t rg = make_rsrc(a.wg, D * F * 2);
  const __amdgpu_buffer_rsrc_t ru = make_rsrc(a.wu, D * F * 2);
  unsigned vx[NX], vw[4];
#pragma unroll
  for (int i = 0; i < NX; ++i) {
    const int row = 8 * (x0 + i) + (lane >> 3), c = (lane & 7) ^ ((row >> 1) & 7);
    vx[i] = (unsigned)((128 * mi + row) * (D * 2) + c * 16 - 1024 * (i & 3));
  }
#pragma unroll
  for (int i = 0; i < 4; ++i) {
    const int slot = 64 * (4 * lw + i) + lane, k = slot / 12, chp = slot - 12 * k, c = chp ^ (2 * ((k >> 3) & 1));
    vw[i] = (unsigned)(k * (F * 2) + fi * 192 + c * 16 - 1024 * i);
  }
  const unsigned land_addr = lds0 + L_SYNC + 4 * (SY_LANDED + lw);
  auto part_a = [&](int s) {
    const unsigned b = lds0 + (s % 3) * STAGE;
    dma16xN<4>(rg, b + GB + 4 * lw * 1024, vw, s * (64 * F * 2));
    dma16xN<4>(ru, b + UB + 4 * lw * 1024, vw, s * (64 * F * 2));
  };
  auto part_b = [&](int s) {
    const unsigned b = lds0 + (s % 3) * STAGE;
    dma16xN<4>(rx, b + XB + x0 * 1024, vx, s * 128);
    dma16xN<NX - 4>(rx, b + XB + (x0 + 4) * 1024, vx + 4, s * 128);
  };
  const int tl = 19 * lw + lane, L = 56 * j + ((lane < 19 && tl < 56) ? tl : 0);
  const char* tbase; int tstep;
  if (L < 1024) { tbase = (const char*)a.x + (size_t)(1024 * (xcd & 1) + L) * (D * 2); tstep = 128; }
  else {
    const int l2 = L - 1024, mat = l2 / 384, k = (l2 - 384 * mat) / 6, c = l2 % 6;
    tbase = (const char*)(mat ? a.wu : a.wg) + (size_t)k * (F * 2) + 768 * (xcd >> 1) + 128 * c; tstep = 64 * F * 2;
  }
  constexpr int H = 8;
  __builtin_amdgcn_s_setprio(LOADER_PRIO);
  part_a(0); part_b(0);
  part_a(1);
  STAMP(1);
  wait_vm<H>();
  sy_post(land_addr, 1u);
  WAIT_LGKM0();
  wake();
  STAMP(2);
  part_b(1);
  part_a(2);
  wait_vm<H>();
  sy_post(land_addr, 2u);
  part_b(2);
  constexpr int T0 = TOUCH1 == 0 ? 0 : TOUCH1 == 1 ? 2 : 7, T1 = TOUCH1 == 1 ? 1 : 0;
#pragma unroll
  for (int t = 0; t < T0; ++t) touch(tbase + (size_t)(3 + t) * tstep, lds0 + L_DUMMY);
#pragma unroll
  for (int s = 0; s < 7; ++s) {
    __builtin_amdgcn_s_setprio(0);
    loader_wait(lds, s + 1);
    __builtin_amdgcn_s_setprio(LOADER_PRIO);
    STAMPI(8 + 2 * s);
    part_a(s + 3);
    if (s == 0) wait_vm<H + T0>(); else if (s == FILT_ITER + 1) wait_vm<H + 11 + T1>(); else if (s <= 5) wait_vm<H + T1>(); else wait_vm<H>();
    STAMPI(9 + 2 * s);
    sy_post(land_addr, s + 3);
    part_b(s + 3);
    if (T1 && s <= 4) touch(tbase + (size_t)(s + 5) * tstep, lds0 + L_DUMMY);
    if (s == FILT_ITER) { FENCE(); load_filter(a, flt, ch, lane); FENCE(); asm volatile("" ::: "memory"); }
  }
  WAIT_VM(0);
  sy_post(land_addr, 10u);
  __builtin_amdgcn_s_setprio(0);
}

struct KvTask { u32x4 kr[7], vr[4], wk[4]; };
__device__ __forceinline__ void kv_load(const Args& a, KvTask& t, int task, int tsi, int tdi) {
  const int dc = task % 10, sb = task / 10;
  const int s0 = 64 * tsi + 4 * sb, d0 = 80 * tdi + 8 * dc;
#pragma unroll
  for (int r = 0; r < 7; ++r) {
    const int s = s0 - 3 + r;
    t.kr[r] = *(const u32x4*)(a.k + (size_t)(s < 0 ? 0 : s) * D + d0);
  }
#pragma unroll
  for (int r = 0; r < 4; ++r) t.vr[r] = *(const u32x4*)(a.v + (size_t)(s0 + r) * D + d0);
#pragma unroll
  for (int i = 0; i < 4; ++i) t.wk[i] = *(const u32x4*)(a.shk + (size_t)d0 * 4 + i * 8);
}
__device__ __forceinline__ float dot2(unsigned x, unsigned y, float c) {
  return __builtin_amdgcn_fdot2_f32_bf16(__builtin_bit_cast(bf16x2, x), __builtin_bit_cast(bf16x2, y), c, false);
}
__device__ __forceinline__ float dot2z(unsigned x, unsigned y) {
  float r;
  asm("v_dot2_f32_bf16 %0, %1, %2, 0" : "=v"(r) : "v"(x), "v"(y));
  return r;
}
__device__ __forceinline__ void kv_compute(KvTask& t, lptr lds, int task, int tsi) {
  const int dc = task % 10, sb = task / 10;
  if (tsi == 0 && sb == 0) {
#pragma unroll
    for (int r = 0; r < 3; ++r) t.kr[r] = (u32x4){0u, 0u, 0u, 0u};
  }
  const int sg = 15 - sb;
  lptr dst = lds + L_T + 8 * dc * 128 + (((sg >> 1) ^ (dc & 7)) << 4) + ((sg & 1) << 3);
#pragma unroll
  for (int c = 0; c < 8; ++c) {
    const unsigned w01 = t.wk[c >> 1][2 * (c & 1)], w23 = t.wk[c >> 1][2 * (c & 1) + 1];
    const unsigned sel = (c & 1) ? 0x03020706u : 0x01000504u;
    unsigned pr[6];
#pragma unroll
    for (int i = 0; i < 6; ++i) pr[i] = __builtin_amdgcn_perm(t.kr[i + 1][c >> 1], t.kr[i][c >> 1], sel);
    float o[4];
#pragma unroll
    for (int r = 0; r < 4; ++r) {
      const float ks = dot2(pr[r + 2], w01, dot2z(pr[r], w23));
      o[r] = ks * ((c & 1) ? bf_hi(t.vr[r][c >> 1]) : bf_lo(t.vr[r][c >> 1]));
    }
    *(LAS u32x2*)(dst + c * 128) = (u32x2){pack_bf16(o[3], o[2]), pack_bf16(o[1], o[0])};
  }
}

__device__ __forceinline__ void conv_wave(const Args& a, lptr lds, lptr kc, lptr ur, const Filter& flt, int ch, int nconv, int lane) {
#pragma unroll
  for (int it = 0; it < 5; ++it) {
    const int c = lane + 64 * it;
    if (c < 260) {
      const unsigned a0 = flt.d[it][0], a1 = flt.d[it][1], a2 = flt.d[it][2], a3 = flt.d[it][3], a4 = flt.d[it][4], a5 = flt.d[it][5];
      const unsigned e0 = __builtin_amdgcn_alignbit(a1, a0, 16), e1 = __builtin_amdgcn_alignbit(a2, a1, 16),
                     e2 = __builtin_amdgcn_alignbit(a3, a2, 16), e3 = __builtin_amdgcn_alignbit(a4, a3, 16),
                     e4 = __builtin_amdgcn_alignbit(a5, a4, 16);
      lptr w = kc + 16 * c;
      *(LAS u32x4*)(w) = (u32x4){a0, a1, a2, a3};
      *(LAS u32x4*)(w + KC_COPY) = (u32x4){e0, e1, e2, e3};
      *(LAS u32x4*)(w + 2 * KC_COPY) = (u32x4){a1, a2, a3, a4};
      *(LAS u32x4*)(w + 3 * KC_COPY) = (u32x4){e1, e2, e3, e4};
    }
  }
  const float dcoef = bf_lo(flt.dbits);
  WAIT_LGKM0();
  sy_wait(lds, SY_AREADY, 1u);
  STAMP(4);
  STAMPC(28);

  const int m = lane & 15, g = lane >> 4;
  const int rho = (m + 1) & 3;
  lptr abase = kc + rho * KC_COPY + 2 * (8 * g + m - 31 - rho);
  lptr bbase = ur + 2 * (2000 - 16 * m + 8 * g) - 7 * 512;
  f32x4 acc[8];
#pragma unroll
  for (int t = 0; t < 8; ++t) acc[t] = (f32x4){0.f, 0.f, 0.f, 0.f};
  struct CF { s16x4 a[8][2]; bf16x8 b[8]; };
  auto cread = [&](CF& f, int p, int i) {
    if (p < 16) f.a[p >> 1][p & 1] = *(LAS s16x4*)(abase + 64 * i + 512 * (p >> 1) + 8 * (p & 1));
    else f.b[p - 16] = lds_frag(bbase + 64 * i + 512 * (7 - (p - 16)));
  };
  auto cmma = [&](const CF& f, int p) {
    const int t = tri_row(p), u = p - tri_start(t);
    acc[t + u] = MFMA16(join(f.a[t][0], f.a[t][1]), f.b[u], acc[t + u]);
  };
  auto residue = [&](const CF& cur, CF& nxt, int inext) {
#pragma unroll
    for (int p = 0; p < 36; ++p) {
      cmma(cur, p); FENCE();
      if (p < 24) { cread(nxt, p, inext); FENCE(); }
    }
  };
  CF c0, c1;
#pragma unroll
  for (int p = 0; p < 24; ++p) cread(c0, p, 1);
#pragma unroll 1
  for (int i = 1; i <= 8; i += 2) {
    FENCE();
    residue(c0, c1, i + 1);
    residue(c1, c0, (i + 2 <= 8) ? i + 2 : 8);
  }
  STAMP(5);
  STAMPC(29);
  const __amdgpu_buffer_rsrc_t ry = make_rsrc(a.ws + WS_Y2T + (size_t)ch * S * 2, S * 2);
#pragma unroll
  for (int t = 0; t < 8; t += 2) {
    unsigned pk[2][2];
#pragma unroll
    for (int e = 0; e < 2; ++e) {
      const int s = 256 * (t + e) + 16 * m + 4 * g;
      const u32x2 kv = *(LAS u32x2*)(ur + 2 * (2044 - s));
      pk[e][0] = pack_bf16(acc[t + e][0] + bf_hi(kv[1]) * dcoef, acc[t + e][1] + bf_lo(kv[1]) * dcoef);
      pk[e][1] = pack_bf16(acc[t + e][2] + bf_hi(kv[0]) * dcoef, acc[t + e][3] + bf_lo(kv[0]) * dcoef);
    }
    const auto r0 = __builtin_amdgcn_permlane16_swap(pk[0][0], pk[1][0], false, false);
    const auto r1 = __builtin_amdgcn_permlane16_swap(pk[0][1], pk[1][1], false, false);
    const u32x4 o = {r0[0], r1[0], r0[1], r1[1]};
    const unsigned off = 2 * (256 * (t + (g & 1)) + 16 * m + 8 * (g >> 1));
    __builtin_amdgcn_raw_buffer_store_b128(o, ry, off, 0, SC1);
  }
  STAMP(6);
  WAIT_VM(0);
  STAMP(7);
  if (sy_arrive(lds, SY_CONV, lane) == (unsigned)(nconv - 1)) {
    const unsigned tag = get_tag(lds);
    if (lane == 0) flag_store(a.ws, WS_FLAGY + 4 * blockIdx.x, tag);
  }
}

__global__ __launch_bounds__(512, 2) void fused_kernel(Args a) {
  extern __shared__ __attribute__((aligned(16))) char smem[];
  lptr lds = (lptr)smem;
  const int tid = threadIdx.x, lane = tid & 63;
  const int wave = __builtin_amdgcn_readfirstlane(tid >> 6);
  const int bid = blockIdx.x;
  const int xcd = bid & 7, j = bid >> 3;
  const int mi = 8 * (xcd & 1) + (j & 7), fi = 4 * (xcd >> 1) + (j >> 3);
  const int si = 8 * (xcd & 3) + (j & 7), di = 4 * (xcd >> 2) + (j >> 3);
  const int c0 = (5 * bid) >> 1, nconv = 2 + (bid & 1);
  const unsigned lds0 = lds_addr(lds);
  STAMP_INIT();
  STAMP(0);

  if (wave == 7) {
    if (lane < 32) *sy(lds, lane) = 0u;
    WAIT_LGKM0();
  }
  BARRIER();

  if (wave == 7) {
    const int wb = bid / 5, wdq = bid - 5 * wb;
    const unsigned epoch = __hip_atomic_load((GAS unsigned*)(a.ws + WS_EPOCH), __ATOMIC_RELAXED, __HIP_MEMORY_SCOPE_AGENT);
    sy_set(lds, SY_TAG, epoch + 1u, lane);
    WAIT_LGKM0();
    sy_set(lds, SY_TAGOK, 1u, lane);
    const unsigned tag = epoch + 1u;
    STAMP(1);
    if (W7_DELAY) sy_wait4m(lds, 1u);
    {
      const char* kf = (const char*)(a.ssmk + (size_t)c0 * S);
      touch(kf + lane * 128, lds0 + L_DUMMY);
      touch(kf + (64 + (lane & 31)) * 128, lds0 + L_DUMMY);
    }
    if (bid < 240) {
      const __amdgpu_buffer_rsrc_t rd = make_rsrc(a.wd, F * D * 2);
#pragma unroll
      for (int i = 0; i < 8; ++i) {
        const int slot = 64 * i + lane, row = slot >> 4, ch = slot & 15;
        dma16(rd, lds0 + L_WD + i * 1024, (unsigned)((32 * wb + row) * (D * 2) + wdq * 256 + ch * 16), 0);
      }
    }
    const int r0 = 128 * mi + 8 * fi, gr = lane >> 4, gq = lane & 15;
    u32x4 gx[2][5], gw[5];
#pragma unroll
    for (int i = 0; i < 5; ++i) {
      gw[i] = ((const u32x4*)a.gw)[gq + 16 * i];
      gx[0][i] = ((const u32x4*)(a.x + (size_t)(r0 + gr) * D))[gq + 16 * i];
      gx[1][i] = ((const u32x4*)(a.x + (size_t)(r0 + 4 + gr) * D))[gq + 16 * i];
    }
    FENCE();
#pragma unroll
    for (int pass = 0; pass < 2; ++pass) {
      float sum = 0.f;
#pragma unroll
      for (int i = 0; i < 5; ++i)
#pragma unroll
        for (int e = 0; e < 4; ++e) sum = dot2(gx[pass][i][e], gw[i][e], sum);
#pragma unroll
      for (int m = 8; m >= 1; m >>= 1) sum += __shfl_xor(sum, m);
      if (gq == 0)
        __hip_atomic_store((GAS unsigned*)(a.ws + WS_GATE) + r0 + 4 * pass + gr, __builtin_bit_cast(unsigned, sigmoidf_(sum)),
                           __ATOMIC_RELAXED, __HIP_MEMORY_SCOPE_AGENT);
    }
    WAIT_VM(2);
    if (bid < 240) {
      const __amdgpu_buffer_rsrc_t rwt = make_rsrc(a.ws + WS_WT, F * D * 2);
      const int g = lane >> 4, q = (lane & 15) >> 2, p = lane & 3;
#pragma unroll
      for (int dt = 0; dt < 8; ++dt) {
        lptr b = lds + L_WD + (4 * g + q) * 256 + 32 * dt + 8 * p;
        const bf16x8 fr = join(lds_tr(b), lds_tr(b + 16 * 256));
        __builtin_amdgcn_raw_buffer_store_b128(__builtin_bit_cast(u32x4, fr), rwt,
                                               (unsigned)(((8 * wdq + dt) * 48 + wb) * 1024 + lane * 16), 0, SC1);
      }
    }
    WAIT_VM(0);
    if (sy_arrive(lds, SY_SIDE, lane) == 4u) publish_side(a.ws, bid, tag, lane);
    STAMP(3);

    sy_wait(lds, SY_SIDE, 5u);
    sy_wait4(lds, SY_CONSUMED, POLL_AFTER);
    {
      const int dq0 = 2 * (xcd >> 2), l48 = lane < 48 ? lane : 0;
#pragma unroll
      for (int q3 = 0; q3 < 3; ++q3) flag_wait(a.ws, WS_FLAGW + 4 * (64 * (dq0 + q3) + l48), true, tag);
      sy_set(lds, SY_WREADY, 1u, lane);
    }
    {
      const int sub = lane / 20, L = 20 * j + (lane - 20 * sub), fr = L >> 3;
      const char* base = a.ws + WS_WT + ((size_t)(20 * (xcd >> 2) + (fr >> 2)) * 48 + 12 * (fr & 3) + (sub < 3 ? sub : 0)) * 1024 + (L & 7) * 128;
#pragma unroll
      for (int t = 0; t < 12; t += 3) touch(base + t * 1024, lds0 + L_DUMMY);
    }
    flag_wait(a.ws, WS_FLAGKV + 4 * (32 * (bid >> 5) + (lane & 31)), true, tag);
    STAMP(8);
    {
      u32x4 uv[3][4];
#pragma unroll
      for (int cw = 0; cw < 3; ++cw) {
        const __amdgpu_buffer_rsrc_t ru = make_rsrc(a.ws + WS_KVR + (size_t)(c0 + (cw < nconv ? cw : 0)) * S * 2, S * 2);
#pragma unroll
        for (int i = 0; i < 4; ++i) uv[cw][i] = ld128(ru, (unsigned)(i * 1024 + lane * 16), 0, SC1);
      }
#pragma unroll
      for (int cw = 0; cw < 3; ++cw) {
        lptr ur = lds + L_AUX + cw * UR_BYTES;
#pragma unroll
        for (int i = 0; i < 4; ++i) *(LAS u32x4*)(ur + i * 1024 + lane * 16) = uv[cw][i];
        if (lane < 32) *(LAS u32x4*)(ur + 4096 + lane * 16) = (u32x4){0u, 0u, 0u, 0u};
      }
      WAIT_LGKM0();
      sy_set(lds, SY_AREADY, 1u, lane);
    }
    STAMP(9);
    flag_wait(a.ws, WS_FLAGH + 4 * (16 * (si >> 1) + (lane & 15)), true, tag);
    sy_set(lds, SY_HREADY, 1u, lane);
    STAMP(10);
    if (TOUCH_H) {
      const char* base = a.ws + WS_HT + ((size_t)(4 * si + (j >> 3)) * 48 + 12 * ((lane >> 3) & 3) + (lane >> 5)) * 1024 + (lane & 7) * 128;
#pragma unroll
      for (int t = 2; t < 12; t += 2) touch(base + t * 1024, lds0 + L_DUMMY);
    }
    flag_wait(a.ws, WS_FLAGY + 4 * (32 * di + (lane & 31)), true, tag);
    STAMP(11);
    {
      const __amdgpu_buffer_rsrc_t ry = make_rsrc(a.ws + WS_Y2T, S * D * 2);
      u32x4 yv[10];
#pragma unroll
      for (int i = 0; i < 10; ++i) {
        const int id = lane + 64 * i;
        yv[i] = ld128(ry, (unsigned)((80 * di + (id >> 3)) * (S * 2) + 64 * si * 2 + (id & 7) * 16), 0, SC1);
      }
#pragma unroll
      for (int i = 0; i < 10; ++i) *(LAS u32x4*)(lds + L_Y + (lane + 64 * i) * 16) = yv[i];
      WAIT_LGKM0();
      sy_set(lds, SY_YREADY, 1u, lane);
    }
    STAMP(12);
    if (bid == 0) {
#pragma unroll
      for (int i = 0; i < 4; ++i) flag_wait(a.ws, WS_FLAGKV + 4 * (lane + 64 * i), true, tag);
      if (lane == 0) flag_store(a.ws, WS_EPOCH, tag);
    }
    WAIT_VM(0);
    STAMP_DUMP();
    return;
  }

  if (wave >= 4) {
    const int cw = wave - 4;
    const bool active = cw < nconv;
    const int ch = c0 + (active ? cw : 0);
    Filter flt;
    if (cw == 0) loader<6>(a, lds, 0, 0, mi, fi, xcd, j, flt, ch, lane);
    else loader<5>(a, lds, cw, 1 + 5 * cw, mi, fi, xcd, j, flt, ch, lane);
    STAMP(3);
    if (active) {
      sy_wait4(lds, SY_CONSUMED, cw == 2 ? 9u : 8u);
      conv_wave(a, lds, lds + (cw == 2 ? 2 * STAGE : STAGE + cw * KC_BYTES), lds + L_AUX + cw * UR_BYTES, flt, ch, nconv, lane);
    }
    STAMP_DUMP();
    return;
  }

  f32x4 ag[4][3], au[4][3];
  {
    const int tsi = bid >> 3, tdi = bid & 7;
    const int task = 40 * wave + (lane < 40 ? lane : 0);
    KvTask t0;
    kv_load(a, t0, task, tsi, tdi);
    FENCE();
#pragma unroll
    for (int t = 0; t < 4; ++t)
#pragma unroll
      for (int c = 0; c < 3; ++c) {
        ag[t][c] = (f32x4){0.f, 0.f, 0.f, 0.f}; au[t][c] = (f32x4){0.f, 0.f, 0.f, 0.f};
        asm volatile("" : "+v"(ag[t][c]), "+v"(au[t][c]));
      }
    FENCE();
    STAMP(1);
    if (lane < 40) kv_compute(t0, lds, task, tsi);
    sy_arrive(lds, SY_KV, lane);
    sy_wait(lds, SY_KV, 4u);
    STAMP(2);
    const __amdgpu_buffer_rsrc_t rkv = make_rsrc(a.ws + WS_KVR, S * D * 2);
#pragma unroll
    for (int i = 0; i < 3; ++i) {
      const int id = tid + 256 * i;
      if (id < 640) {
        const int dl = id >> 3, jj = id & 7;
        const u32x4 val = *(LAS u32x4*)(lds + L_T + dl * 128 + ((jj ^ ((dl >> 3) & 7)) << 4));
        __builtin_amdgcn_raw_buffer_store_b128(val, rkv, (unsigned)(((80 * tdi + dl) * S + (1984 - 64 * tsi) + 8 * jj) * 2), 0, SC1);
      }
    }
  }
  const int n = lane & 15, g = lane >> 4;
  {
    const int ws_ = wave & 1, wf = wave >> 1;
    const int q = n >> 2, p = lane & 3;
    const int xb0 = XB + (64 * ws_ + n) * 128 + ((g ^ (n >> 1)) << 4);
    int wb_[3];
#pragma unroll
    for (int c = 0; c < 3; ++c) wb_[c] = GB + (8 * g + q) * 192 + 8 * p + 32 * ((3 * wf + c) ^ (g & 1));
    auto read_piece = [&](Frags& f, int i, lptr b, int kk) {
      if (i < 4) {
        f.x[i] = lds_frag(b + (xb0 ^ (kk << 6)) + i * 2048);
      } else {
        const int c = (i - 4) >> 2, u = ((i - 4) >> 1) & 1, t = (i - 4) & 1;
        const s16x4 v = lds_tr(b + wb_[c] + kk * (32 * 192) + u * (UB - GB) + t * (4 * 192));
        if (u == 0) f.g[c][t] = v; else f.u[c][t] = v;
      }
    };
    auto mma_piece = [&](const Frags& f, int i) {
      const int c = i >> 3, t = (i >> 1) & 3;
      if ((i & 1) == 0) ag[t][c] = MFMA16(join(f.g[c][0], f.g[c][1]), f.x[t], ag[t][c]);
      else au[t][c] = MFMA16(join(f.u[c][0], f.u[c][1]), f.x[t], au[t][c]);
    };
    auto half = [&](const Frags& cur, Frags& nxt, lptr nb, int nkk, bool rd, auto&& hook) {
#pragma unroll
      for (int i = 0; i < 24; ++i) {
        mma_piece(cur, i); FENCE();
        if (rd && i < 16) { read_piece(nxt, i, nb, nkk); FENCE(); }
        hook(i);
      }
    };
    auto nohook = [](int) {};
    const unsigned cons_addr = lds0 + L_SYNC + 4 * (SY_CONSUMED + wave);
    {
#pragma unroll 1
      for (unsigned it = 0; it < SPIN_LDS; ++it) {
        const u32x4 l = sy_get4(lds, SY_LANDED);
        if ((unsigned)__builtin_amdgcn_readfirstlane(min(min(l[0], l[1]), l[2])) >= 1u) break;
        __builtin_amdgcn_s_sleep(2);
      }
      asm volatile("" ::: "memory");
    }
    STAMP(3);
    STAMPC(28);
    Frags f0, f1;
#pragma unroll
    for (int i = 0; i < 16; ++i) read_piece(f0, i, lds, 0);
    FENCE();
    STAMPC(27);
    int buf = 0;
#pragma unroll 1
    for (int s = 0; s < 9; ++s) {
      const int nbuf = (buf == 2) ? 0 : buf + 1;
      FENCE();
      u32x4 l;
      unsigned seen = 0;
      half(f0, f1, lds + buf * STAGE, 1, true, [&](int i) {
        if (i == 16) { l = sy_get4(lds, SY_LANDED); FENCE(); }
        if (i == 21) {
          WAIT_LGKM0();
          seen = __builtin_amdgcn_readfirstlane(min(min(l[0], l[1]), l[2]));
          sy_post(cons_addr, s + 1);
          FENCE();
        }
        if (i == 23) { wake(); FENCE(); }
      });
      STAMPI(13 + s);
      if (seen < (unsigned)(s + 2)) {
#pragma unroll 1
        for (unsigned it = 0; it < SPIN_LDS; ++it) {
          l = sy_get4(lds, SY_LANDED);
          if ((unsigned)__builtin_amdgcn_readfirstlane(min(min(l[0], l[1]), l[2])) >= (unsigned)(s + 2)) break;
        }
      }
      if (s == SIDE_PUB_ITER) {
        WAIT_VM(0);
        if (sy_arrive(lds, SY_SIDE, lane) == 4u) {
          publish_side(a.ws, bid, get_tag(lds), lane);
        }
      }
      asm volatile("" ::: "memory");
      FENCE();
      half(f1, f0, lds + nbuf * STAGE, 0, true, nohook);
      buf = nbuf;
    }
    half(f0, f1, lds + buf * STAGE, 1, true, [&](int i) {
      if (i == 21) { WAIT_LGKM0(); sy_post(cons_addr, 10u); FENCE(); }
      if (i == 23) { wake(); FENCE(); }
    });
    half(f1, f0, lds, 0, false, nohook);
    STAMP(4);
    STAMPC(29);

    const __amdgpu_buffer_rsrc_t rhs = make_rsrc(a.ws + WS_HT, S * F * 2);
#pragma unroll
    for (int t = 0; t < 4; ++t) {
      u32x2 pk[3];
#pragma unroll
      for (int c = 0; c < 3; ++c) {
        float h[4];
#pragma unroll
        for (int r = 0; r < 4; ++r) h[r] = sigmoidf_(ag[t][c][r]) * au[t][c][r];
        pk[c] = (u32x2){pack_bf16(h[0], h[1]), pack_bf16(h[2], h[3])};
      }
      const unsigned base = (unsigned)(((8 * mi + 4 * ws_ + t) * 48 + 3 * fi) * 1024 + lane * 16);
      if (wf == 0) {
        __builtin_amdgcn_raw_buffer_store_b128((u32x4){pk[0][0], pk[0][1], pk[1][0], pk[1][1]}, rhs, base, 0, SC1);
        __builtin_amdgcn_raw_buffer_store_b64(pk[2], rhs, base + 1024, 0, SC1);
      } else {
        __builtin_amdgcn_raw_buffer_store_b64(pk[0], rhs, base + 1024 + 8, 0, SC1);
        __builtin_amdgcn_raw_buffer_store_b128((u32x4){pk[1][0], pk[1][1], pk[2][0], pk[2][1]}, rhs, base + 2048, 0, SC1);
      }
    }
  }
  STAMP(5);

  const __amdgpu_buffer_rsrc_t rh = make_rsrc(a.ws + WS_HT, S * F * 2);
  const __amdgpu_buffer_rsrc_t rw = make_rsrc(a.ws + WS_WT, F * D * 2);
  const unsigned voff = lane * 16;
  const int hbase = (4 * si * 48 + 12 * wave) * 1024, wbase = (5 * di * 48 + 12 * wave) * 1024;
  struct WS5 { bf16x8 f[5]; };
  struct HS4 { bf16x8 f[4]; };
  WS5 W[3];
  HS4 H[4];
  const int rot = 3 * (j >> 3);
  auto kstep = [&](int b) { const int t = b + rot; return (t >= 12 ? t - 12 : t) * 1024; };
  auto load_w = [&](WS5& w, int i, int b) { w.f[i] = __builtin_bit_cast(bf16x8, ld128(rw, voff, wbase + i * 48 * 1024 + kstep(b), SC1)); };
  auto load_h = [&](HS4& h, int i, int b) { h.f[i] = __builtin_bit_cast(bf16x8, ld128(rh, voff, hbase + i * 48 * 1024 + kstep(b), SC1)); };
  const int srow = 64 * si + 16 * wave + n;
  u32x2 qv[5];
  FENCE();
  sy_wait(lds, SY_WREADY, 1u);
#pragma unroll
  for (int b = 0; b < 3; ++b)
#pragma unroll
    for (int i = 0; i < 5; ++i) load_w(W[b], i, b);
#pragma unroll
  for (int c = 0; c < 5; ++c) qv[c] = *(const u32x2*)(a.x + (size_t)srow * D + 80 * di + 16 * c + 4 * g);
  FENCE();
  WAIT_VM(20);
  if (sy_arrive(lds, SY_HDONE, lane) == 3u) {
    const unsigned tag = get_tag(lds);
    if (lane == 0) flag_store(a.ws, WS_FLAGH + 4 * (16 * mi + fi), tag);
  }
  f32x4 acc[4][5];
#pragma unroll
  for (int t = 0; t < 4; ++t)
#pragma unroll
    for (int c = 0; c < 5; ++c) { acc[t][c] = (f32x4){0.f, 0.f, 0.f, 0.f}; asm volatile("" : "+v"(acc[t][c])); }
  STAMP(6);
  sy_wait(lds, SY_HREADY, 1u);
  STAMP(7);
#pragma unroll
  for (int b = 0; b < 3; ++b)
#pragma unroll
    for (int i = 0; i < 4; ++i) load_h(H[b], i, b);
  const float gate = __builtin_bit_cast(float, __hip_atomic_load((GAS unsigned*)(a.ws + WS_GATE) + srow, __ATOMIC_RELAXED, __HIP_MEMORY_SCOPE_AGENT));
  FENCE();
#pragma unroll
  for (int b = 0; b < 12; ++b) {
#pragma unroll
    for (int i = 0; i < 20; ++i) {
      acc[i & 3][i >> 2] = MFMA16(W[b % 3].f[i >> 2], H[b & 3].f[i & 3], acc[i & 3][i >> 2]);
      FENCE();
      if (i < 4 && b + 3 < 12) { load_h(H[(b + 3) & 3], i, b + 3); FENCE(); }
      if (i >= 4 && i < 9 && b >= 1 && b + 2 < 12) { load_w(W[(b + 2) % 3], i - 4, b + 2); FENCE(); }
    }
  }
  STAMP(8);

  f32x4 bsum[5];
  {
    auto red = [&](int r) { return lds + (r < 40 ? r * 1024 : 2 * STAGE + KC_BYTES + (r - 40) * 1024) + lane * 16; };
    sy_wait4(lds, SY_CONSUMED, 10u);
#pragma unroll
    for (int t = 0; t < 4; ++t) {
      if (t != wave) {
#pragma unroll
        for (int c = 0; c < 5; ++c) *(LAS f32x4*)red((t * 3 + ((wave - t - 1) & 3)) * 5 + c) = acc[t][c];
      } else {
#pragma unroll
        for (int c = 0; c < 5; ++c) bsum[c] = acc[t][c];
      }
    }
    sy_arrive(lds, SY_RED, lane);
    sy_wait(lds, SY_RED, 4u);
#pragma unroll
    for (int w = 0; w < 3; ++w)
#pragma unroll
      for (int c = 0; c < 5; ++c) bsum[c] += *(LAS f32x4*)red((wave * 3 + w) * 5 + c);
  }
  STAMP(9);
  sy_wait(lds, SY_YREADY, 1u);
  STAMP(10);
  {
    const int q = n >> 2, p = lane & 3;
#pragma unroll
    for (int c = 0; c < 5; ++c) {
      const s16x4 yv = lds_tr(lds + L_Y + (16 * c + 4 * g + q) * 128 + 32 * wave + 8 * p);
      const u32x2 y = __builtin_bit_cast(u32x2, yv);
      const float o0 = gate * (bf_lo(qv[c][0]) * bf_lo(y[0])) + (1.f - gate) * bsum[c][0];
      const float o1 = gate * (bf_hi(qv[c][0]) * bf_hi(y[0])) + (1.f - gate) * bsum[c][1];
      const float o2 = gate * (bf_lo(qv[c][1]) * bf_lo(y[1])) + (1.f - gate) * bsum[c][2];
      const float o3 = gate * (bf_hi(qv[c][1]) * bf_hi(y[1])) + (1.f - gate) * bsum[c][3];
      *(u32x2*)(a.out + (size_t)srow * D + 80 * di + 16 * c + 4 * g) = (u32x2){pack_bf16(o0, o1), pack_bf16(o2, o3)};
    }
  }
  STAMP(11);
}

}

extern "C" void kernel_launch(void* const* d_in, const int* in_sizes, int n_in, void* d_out, int out_size, void* d_ws,
                              size_t ws_size, hipStream_t stream) {
  (void)in_sizes; (void)n_in; (void)out_size; (void)ws_size;
  Args a;
  a.x = (const u16*)d_in[0];
  a.k = (const u16*)d_in[1];
  a.v = (const u16*)d_in[2];
  a.shk = (const u16*)d_in[3];
  a.ssmk = (const u16*)d_in[4];
  a.ssmd = (const u16*)d_in[5];
  a.wg = (const u16*)d_in[7];
  a.wu = (const u16*)d_in[8];
  a.wd = (const u16*)d_in[9];
  a.gw = (const u16*)d_in[10];
  a.ws = (char*)d_ws;
  a.out = (u16*)d_out;
  (void)hipFuncSetAttribute((const void*)fused_kernel, hipFuncAttributeMaxDynamicSharedMemorySize, LDS_BYTES);
  fused_kernel<<<256, 512, LDS_BYTES, stream>>>(a);
}
```

```cpp
#include <hip/hip_runtime.h>
#include <hip/hip_bf16.h>

namespace {

typedef unsigned short u16;
typedef __bf16 bf16x8 __attribute__((ext_vector_type(8)));
typedef __bf16 bf16x2 __attribute__((ext_vector_type(2)));
typedef short s16x4 __attribute__((ext_vector_type(4)));
typedef float f32x2 __attribute__((ext_vector_type(2)));
typedef float f32x4 __attribute__((ext_vector_type(4)));
typedef unsigned u32x2 __attribute__((ext_vector_type(2)));
typedef unsigned u32x4 __attribute__((ext_vector_type(4)));
#define LAS __attribute__((address_space(3)))
#define GAS __attribute__((address_space(1)))
typedef LAS char* lptr;

constexpr int S = 2048, D = 640, F = 1536;
constexpr int SC1 = 16;

constexpr size_t WS_HT = 0;
constexpr size_t WS_KVR = WS_HT + (size_t)S * F * 2;
constexpr size_t WS_Y2T = WS_KVR + (size_t)S * D * 2;
constexpr size_t WS_GATE = WS_Y2T + (size_t)S * D * 2;
constexpr size_t WS_FLAGKV = WS_GATE + (size_t)S * 4;
constexpr size_t WS_FLAGH = WS_FLAGKV + 1024;
constexpr size_t WS_FLAGY = WS_FLAGH + 1024;
constexpr size_t WS_EPOCH = WS_FLAGY + 1024;
constexpr size_t WS_END = WS_EPOCH + 256;

constexpr int STAGE = 40960, XB = 0, GB = 16384, UB = 28672;
constexpr int L_AUX = 3 * STAGE;
constexpr int L_T = L_AUX, UR_BYTES = 4608;
constexpr int L_FREE = L_AUX + 3 * UR_BYTES;
constexpr int L_SYNC = L_FREE + 20480 + 2 * 2560;
constexpr int L_DUMMY = L_SYNC + 128;
constexpr int L_END = L_DUMMY + 256;
constexpr int KC_COPY = 4160, KC_BYTES = 4 * KC_COPY;
constexpr int WSLOT = 5120;
__device__ constexpr int wring_off(int w) { return w == 0 ? 0 : w == 1 ? 20480 : w == 2 ? 2 * STAGE + KC_BYTES : L_FREE; }
__device__ constexpr int ystrip_off(int w) { return w < 2 ? STAGE + 2 * KC_BYTES + w * 2560 : L_FREE + 20480 + (w - 2) * 2560; }
static_assert(2 * STAGE + KC_BYTES + 20480 <= 3 * STAGE && STAGE + 2 * KC_BYTES + 2 * 2560 <= 2 * STAGE, "second-phase LDS map");
enum { SY_LANDED = 0, SY_CONSUMED = 4, SY_KV = 8, SY_SIDE = 9, SY_HDONE = 10, SY_RED = 11, SY_CONV = 12, SY_AREADY = 13,
       SY_HREADY = 14, SY_YREADY = 15, SY_TAG = 16, SY_TAGOK = 17, SY_GSTEP = 18, SY_GATE = 19, SY_EPI = 20, SY_WPRE = 21 };

#define STAMP(i) do {} while (0)
#define STAMPC(i) do {} while (0)
#define STAMPI(i) do {} while (0)
#define STAMP_INIT() do {} while (0)
#define STAMP_DUMP() do {} while (0)
constexpr int LDS_BYTES = L_END;

#define MFMA16(a, b, c) __builtin_amdgcn_mfma_f32_16x16x32_bf16((a), (b), (c), 0, 0, 0)
#define WAIT_VM(n) asm volatile("s_waitcnt vmcnt(" #n ")" ::: "memory")
#define WAIT_LGKM0() asm volatile("s_waitcnt lgkmcnt(0)" ::: "memory")
#define BARRIER() do { asm volatile("" ::: "memory"); __builtin_amdgcn_s_barrier(); asm volatile("" ::: "memory"); } while (0)
#define FENCE() __builtin_amdgcn_sched_barrier(0)
constexpr unsigned SPIN_LDS = 1u << 22, SPIN_MEM = 1u << 16;

__device__ __forceinline__ unsigned pack_bf16(float lo, float hi) {
  f32x2 v = {lo, hi};
  return __builtin_bit_cast(unsigned, __builtin_convertvector(v, bf16x2));
}
__device__ __forceinline__ float bf_lo(unsigned w) { return __builtin_bit_cast(float, w << 16); }
__device__ __forceinline__ float bf_hi(unsigned w) { return __builtin_bit_cast(float, w & 0xffff0000u); }
__device__ __forceinline__ float sigmoidf_(float x) {
  return __builtin_amdgcn_rcpf(1.0f + __builtin_amdgcn_exp2f(x * -1.4426950408889634f));
}
__device__ __forceinline__ __amdgpu_buffer_rsrc_t make_rsrc(const void* p, unsigned bytes) {
  return __builtin_amdgcn_make_buffer_rsrc(const_cast<void*>(p), 0, bytes, 0x00020000);
}
__device__ __forceinline__ unsigned lds_addr(lptr p) { return (unsigned)(size_t)p; }
__device__ __forceinline__ void dma16(__amdgpu_buffer_rsrc_t r, unsigned dst, unsigned voff, unsigned soff) {
  asm volatile("s_mov_b32 m0, %0\n\ts_nop 0\n\tbuffer_load_dwordx4 %1, %2, %3 offen lds"
               :: "s"(dst), "v"(voff), "s"(r), "s"(soff) : "memory");
}
template <int N>
__device__ __forceinline__ void dma16xN(__amdgpu_buffer_rsrc_t r, unsigned dst, const unsigned* v, unsigned soff) {
  static_assert(N >= 1 && N <= 4, "12-bit instruction offset");
  if constexpr (N == 1)
    asm volatile("s_mov_b32 m0, %0\n\ts_nop 0\n\tbuffer_load_dwordx4 %1, %2, %3 offen lds"
                 :: "s"(dst), "v"(v[0]), "s"(r), "s"(soff) : "memory");
  else if constexpr (N == 2)
    asm volatile("s_mov_b32 m0, %0\n\ts_nop 0\n\tbuffer_load_dwordx4 %1, %3, %4 offen lds\n\t"
                 "buffer_load_dwordx4 %2, %3, %4 offen offset:1024 lds"
                 :: "s"(dst), "v"(v[0]), "v"(v[1]), "s"(r), "s"(soff) : "memory");
  else if constexpr (N == 3)
    asm volatile("s_mov_b32 m0, %0\n\ts_nop 0\n\tbuffer_load_dwordx4 %1, %4, %5 offen lds\n\t"
                 "buffer_load_dwordx4 %2, %4, %5 offen offset:1024 lds\n\tbuffer_load_dwordx4 %3, %4, %5 offen offset:2048 lds"
                 :: "s"(dst), "v"(v[0]), "v"(v[1]), "v"(v[2]), "s"(r), "s"(soff) : "memory");
  else
    asm volatile("s_mov_b32 m0, %0\n\ts_nop 0\n\tbuffer_load_dwordx4 %1, %5, %6 offen lds\n\t"
                 "buffer_load_dwordx4 %2, %5, %6 offen offset:1024 lds\n\tbuffer_load_dwordx4 %3, %5, %6 offen offset:2048 lds\n\t"
                 "buffer_load_dwordx4 %4, %5, %6 offen offset:3072 lds"
                 :: "s"(dst), "v"(v[0]), "v"(v[1]), "v"(v[2]), "v"(v[3]), "s"(r), "s"(soff) : "memory");
}
__device__ __forceinline__ void touch(const void* p, unsigned dummy_lds) {
  asm volatile("s_mov_b32 m0, %0\n\ts_nop 0\n\tglobal_load_lds_dword %1, off" :: "s"(dummy_lds), "v"(p) : "memory");
}
__device__ __forceinline__ s16x4 lds_tr(lptr p) { return __builtin_amdgcn_ds_read_tr16_b64_v4i16((LAS s16x4*)p); }
__device__ __forceinline__ bf16x8 join(s16x4 a, s16x4 b) {
  return __builtin_bit_cast(bf16x8, __builtin_shufflevector(a, b, 0, 1, 2, 3, 4, 5, 6, 7));
}
__device__ __forceinline__ bf16x8 lds_frag(lptr p) { return *(LAS bf16x8*)p; }
__device__ __forceinline__ u32x4 ld128(__amdgpu_buffer_rsrc_t r, unsigned voff, unsigned soff, int aux) {
  return aux ? __builtin_bit_cast(u32x4, __builtin_amdgcn_raw_buffer_load_b128(r, voff, soff, SC1))
             : __builtin_bit_cast(u32x4, __builtin_amdgcn_raw_buffer_load_b128(r, voff, soff, 0));
}

__device__ __forceinline__ LAS unsigned* sy(lptr lds, int i) { return (LAS unsigned*)(lds + L_SYNC) + i; }
__device__ __forceinline__ unsigned sy_get(lptr lds, int i) {
  return __builtin_amdgcn_readfirstlane(__hip_atomic_load(sy(lds, i), __ATOMIC_RELAXED, __HIP_MEMORY_SCOPE_WORKGROUP));
}
__device__ __forceinline__ void wake() { asm volatile("s_wakeup" ::: "memory"); }
__device__ __forceinline__ void sy_set(lptr lds, int i, unsigned v, int lane) {
  if (lane == 0) __hip_atomic_store(sy(lds, i), v, __ATOMIC_RELAXED, __HIP_MEMORY_SCOPE_WORKGROUP);
  asm volatile("s_waitcnt lgkmcnt(0)\n\ts_wakeup" ::: "memory");
}
#define TOUCH_H 0
#define LOADER_POST_FIRST 1
#define HPOLL_SLEEP 4
#define YPOLL_SLEEP 8
#define YPOLL_STEP 8
#ifndef M_PRIO
#define M_PRIO 2
#endif
#define G_PRIO 0
#define ROT1 0
#define DMA_SPACE 0
#define C_PRIO 2
#define LAND_RD 18
#define TOUCH_DELAY 8
#define TOUCH1 0
#define FILT_ITER 6
#define W7_DELAY 0
#define POLL_AFTER 8
#define LOADER_PRIO 0
#define SLEEP_LDS 4
__device__ __forceinline__ void sy_post(unsigned addr, unsigned v) {
  asm volatile("s_mov_b64 exec, 1\n\tds_write_b32 %0, %1\n\ts_mov_b64 exec, -1" :: "v"(addr), "v"(v) : "memory");
}
__device__ __forceinline__ void sy_wait(lptr lds, int i, unsigned v) {
#pragma unroll 1
  for (unsigned n = 0; n < SPIN_LDS; ++n) {
    if (sy_get(lds, i) >= v) break;
    __builtin_amdgcn_s_sleep(SLEEP_LDS);
  }
  asm volatile("" ::: "memory");
}
__device__ __forceinline__ unsigned sy_arrive(lptr lds, int i, int lane) {
  WAIT_LGKM0();
  unsigned old = 0;
  if (lane == 0) old = __hip_atomic_fetch_add(sy(lds, i), 1u, __ATOMIC_RELAXED, __HIP_MEMORY_SCOPE_WORKGROUP);
  old = __builtin_amdgcn_readfirstlane(old);
  wake();
  return old;
}
__device__ __forceinline__ unsigned min4(u32x4 v) { return min(min(v[0], v[1]), min(v[2], v[3])); }
__device__ __forceinline__ u32x4 sy_get4(lptr lds, int i) { return *(volatile LAS u32x4*)(lds + L_SYNC + 4 * i); }
__device__ __forceinline__ u32x2 sy_get2(lptr lds, int i) { return *(volatile LAS u32x2*)(lds + L_SYNC + 4 * i); }
__device__ __forceinline__ void sy_wait4(lptr lds, int i, unsigned v) {
#pragma unroll 1
  for (unsigned n = 0; n < SPIN_LDS; ++n) {
    if ((unsigned)__builtin_amdgcn_readfirstlane(min4(sy_get4(lds, i))) >= v) break;
    __builtin_amdgcn_s_sleep(SLEEP_LDS);
  }
  asm volatile("" ::: "memory");
}
#define LOADER_SLEEP 1
__device__ __forceinline__ void loader_wait(lptr lds, unsigned v) {
#pragma unroll 1
  for (unsigned n = 0; n < SPIN_LDS; ++n) {
    if ((unsigned)__builtin_amdgcn_readfirstlane(min4(sy_get4(lds, SY_CONSUMED))) >= v) break;
    if (LOADER_SLEEP) __builtin_amdgcn_s_sleep(LOADER_SLEEP);
  }
  asm volatile("" ::: "memory");
}
__device__ __forceinline__ void sy_wait4m(lptr lds, unsigned v) {
#pragma unroll 1
  for (unsigned n = 0; n < SPIN_LDS; ++n) {
    const u32x4 l = sy_get4(lds, SY_LANDED);
    if ((unsigned)__builtin_amdgcn_readfirstlane(min(min(l[0], l[1]), l[2])) >= v) break;
    __builtin_amdgcn_s_sleep(SLEEP_LDS);
  }
  asm volatile("" ::: "memory");
}
__device__ __forceinline__ unsigned get_tag(lptr lds) {
  sy_wait(lds, SY_TAGOK, 1u);
  return sy_get(lds, SY_TAG);
}
__device__ __forceinline__ void flag_store(char* ws, size_t off, unsigned tag) {
  __hip_atomic_store((GAS unsigned*)(ws + off), tag, __ATOMIC_RELAXED, __HIP_MEMORY_SCOPE_AGENT);
}
#define SIDE_PUB_ITER 3
#define POLL_SLEEP 32
template <int SLEEP = POLL_SLEEP>
__device__ __forceinline__ void flag_wait(char* ws, size_t off, bool on, unsigned tag) {
#pragma unroll 1
  for (unsigned n = 0; n < SPIN_MEM; ++n) {
    const unsigned v = __hip_atomic_load((GAS unsigned*)(ws + off), __ATOMIC_RELAXED, __HIP_MEMORY_SCOPE_AGENT);
    if (__all(!on || v == tag)) break;
    __builtin_amdgcn_s_sleep(SLEEP);
  }
  asm volatile("" ::: "memory");
}

struct Args {
  const u16* x; const u16* k; const u16* v; const u16* shk; const u16* ssmk; const u16* ssmd;
  const u16* wg; const u16* wu; const u16* wd; const u16* gw;
  char* ws; u16* out;
};

struct Frags { bf16x8 x[4]; s16x4 g[3][2]; s16x4 u[3][2]; };

__device__ constexpr int tri_start(int t) { return 8 * t - (t * (t - 1)) / 2; }
__device__ constexpr int tri_row(int p) {
  return p < 8 ? 0 : p < 15 ? 1 : p < 21 ? 2 : p < 26 ? 3 : p < 30 ? 4 : p < 33 ? 5 : p < 35 ? 6 : 7;
}

template <int N> __device__ __forceinline__ void wait_vm() { asm volatile("s_waitcnt vmcnt(%0)" ::"n"(N) : "memory"); }

struct Filter { unsigned d[5][6]; unsigned dbits; };
__device__ __forceinline__ void load_filter(const Args& a, Filter& f, int ch, int lane) {
  const __amdgpu_buffer_rsrc_t rk = make_rsrc(a.ssmk + (size_t)ch * S, S * 2);
#pragma unroll
  for (int it = 0; it < 5; ++it) {
    const unsigned off = (unsigned)(16 * (lane + 64 * it) - 32);
    const u32x4 v0 = __builtin_bit_cast(u32x4, __builtin_amdgcn_raw_buffer_load_b128(rk, off, 0, 0));
    const u32x2 v1 = __builtin_bit_cast(u32x2, __builtin_amdgcn_raw_buffer_load_b64(rk, off + 16, 0, 0));
    f.d[it][0] = v0[0]; f.d[it][1] = v0[1]; f.d[it][2] = v0[2]; f.d[it][3] = v0[3]; f.d[it][4] = v1[0]; f.d[it][5] = v1[1];
  }
  f.dbits = a.ssmd[ch];
}

template <int NX>
__device__ __forceinline__ void loader(const Args& a, lptr lds, int lw, int x0, int mi, int fi, int rot, Filter& flt, int ch, int lane) {
  const unsigned lds0 = lds_addr(lds);
  const __amdgpu_buffer_rsrc_t rx = make_rsrc(a.x, S * D * 2);
  const __amdgpu_buffer_rsrc_t rg = make_rsrc(a.wg, D * F * 2);
  const __amdgpu_buffer_rsrc_t ru = make_rsrc(a.wu, D * F * 2);
  unsigned vx[NX], vw[4];
#pragma unroll
  for (int i = 0; i < NX; ++i) {
    const int row = 8 * (x0 + i) + (lane >> 3), c = (lane & 7) ^ ((row >> 1) & 7);
    vx[i] = (unsigned)((128 * mi + row) * (D * 2) + c * 16 - 1024 * (i & 3));
  }
#pragma unroll
  for (int i = 0; i < 4; ++i) {
    const int slot = 64 * (4 * lw + i) + lane, k = slot / 12, chp = slot - 12 * k, c = chp ^ (2 * ((k >> 3) & 1));
    vw[i] = (unsigned)(k * (F * 2) + fi * 192 + c * 16 - 1024 * i);
  }
  const unsigned land_addr = lds0 + L_SYNC + 4 * (SY_LANDED + lw);
  auto slice = [&](int s) { const int k = s + rot; return k >= 10 ? k - 10 : k; };
  auto part_a = [&](int s) {
    const unsigned b = lds0 + (s % 3) * STAGE, so = (unsigned)slice(s) * (64 * F * 2);
    dma16xN<4>(rg, b + GB + 4 * lw * 1024, vw, so);
    if (DMA_SPACE) __builtin_amdgcn_s_sleep(DMA_SPACE);
    dma16xN<4>(ru, b + UB + 4 * lw * 1024, vw, so);
    if (DMA_SPACE) __builtin_amdgcn_s_sleep(DMA_SPACE);
  };
  auto part_b = [&](int s) {
    const unsigned b = lds0 + (s % 3) * STAGE, so = (unsigned)slice(s) * 128;
    dma16xN<4>(rx, b + XB + x0 * 1024, vx, so);
    if (DMA_SPACE) __builtin_amdgcn_s_sleep(DMA_SPACE);
    dma16xN<NX - 4>(rx, b + XB + (x0 + 4) * 1024, vx + 4, so);
  };
  constexpr int H = 8, P = NX + 8;
  __builtin_amdgcn_s_setprio(LOADER_PRIO);
  part_a(0); part_b(0);
  part_a(1); part_b(1);
  STAMP(1);
  wait_vm<P>();
  sy_post(land_addr, 1u);
  WAIT_LGKM0();
  wake();
  STAMP(2);
  part_a(2);
  wait_vm<H>();
  sy_post(land_addr, 2u);
  part_b(2);
#pragma unroll
  for (int s = 0; s < 7; ++s) {
    __builtin_amdgcn_s_setprio(0);
    loader_wait(lds, s + 1);
    __builtin_amdgcn_s_setprio(LOADER_PRIO);
    STAMPI(8 + 2 * s);
    if (s == FILT_ITER + 1) wait_vm<11>(); else wait_vm<0>();
    STAMPI(9 + 2 * s);
    sy_post(land_addr, s + 3);
    part_a(s + 3);
    part_b(s + 3);
    if (s == FILT_ITER) { FENCE(); load_filter(a, flt, ch, lane); FENCE(); asm volatile("" ::: "memory"); }
  }
  WAIT_VM(0);
  sy_post(land_addr, 10u);
  __builtin_amdgcn_s_setprio(0);
}

struct KvTask { u32x4 kr[7], vr[4], wk[4]; };
__device__ __forceinline__ void kv_load(const Args& a, KvTask& t, int task, int tsi, int tdi) {
  const int dc = task % 10, sb = task / 10;
  const int s0 = 64 * tsi + 4 * sb, d0 = 80 * tdi + 8 * dc;
#pragma unroll
  for (int r = 0; r < 7; ++r) {
    const int s = s0 - 3 + r;
    t.kr[r] = *(const u32x4*)(a.k + (size_t)(s < 0 ? 0 : s) * D + d0);
  }
#pragma unroll
  for (int r = 0; r < 4; ++r) t.vr[r] = *(const u32x4*)(a.v + (size_t)(s0 + r) * D + d0);
#pragma unroll
  for (int i = 0; i < 4; ++i) t.wk[i] = *(const u32x4*)(a.shk + (size_t)d0 * 4 + i * 8);
}
__device__ __forceinline__ float dot2(unsigned x, unsigned y, float c) {
  return __builtin_amdgcn_fdot2_f32_bf16(__builtin_bit_cast(bf16x2, x), __builtin_bit_cast(bf16x2, y), c, false);
}
__device__ __forceinline__ float dot2z(unsigned x, unsigned y) {
  float r;
  asm("v_dot2_f32_bf16 %0, %1, %2, 0" : "=v"(r) : "v"(x), "v"(y));
  return r;
}
__device__ __forceinline__ void kv_compute(KvTask& t, lptr lds, int task, int tsi) {
  const int dc = task % 10, sb = task / 10;
  if (tsi == 0 && sb == 0) {
#pragma unroll
    for (int r = 0; r < 3; ++r) t.kr[r] = (u32x4){0u, 0u, 0u, 0u};
  }
  const int sg = 15 - sb;
  lptr dst = lds + L_T + 8 * dc * 128 + (((sg >> 1) ^ (dc & 7)) << 4) + ((sg & 1) << 3);
#pragma unroll
  for (int c = 0; c < 8; ++c) {
    const unsigned w01 = t.wk[c >> 1][2 * (c & 1)], w23 = t.wk[c >> 1][2 * (c & 1) + 1];
    const unsigned sel = (c & 1) ? 0x03020706u : 0x01000504u;
    unsigned pr[6];
#pragma unroll
    for (int i = 0; i < 6; ++i) pr[i] = __builtin_amdgcn_perm(t.kr[i + 1][c >> 1], t.kr[i][c >> 1], sel);
    float o[4];
#pragma unroll
    for (int r = 0; r < 4; ++r) {
      const float ks = dot2(pr[r + 2], w01, dot2z(pr[r], w23));
      o[r] = ks * ((c & 1) ? bf_hi(t.vr[r][c >> 1]) : bf_lo(t.vr[r][c >> 1]));
    }
    *(LAS u32x2*)(dst + c * 128) = (u32x2){pack_bf16(o[3], o[2]), pack_bf16(o[1], o[0])};
  }
}

__device__ __forceinline__ void conv_wave(const Args& a, lptr lds, lptr kc, lptr ur, const Filter& flt, int ch, int nconv, int lane) {
#pragma unroll
  for (int it = 0; it < 5; ++it) {
    const int c = lane + 64 * it;
    if (c < 260) {
      const unsigned a0 = flt.d[it][0], a1 = flt.d[it][1], a2 = flt.d[it][2], a3 = flt.d[it][3], a4 = flt.d[it][4], a5 = flt.d[it][5];
      const unsigned e0 = __builtin_amdgcn_alignbit(a1, a0, 16), e1 = __builtin_amdgcn_alignbit(a2, a1, 16),
                     e2 = __builtin_amdgcn_alignbit(a3, a2, 16), e3 = __builtin_amdgcn_alignbit(a4, a3, 16),
                     e4 = __builtin_amdgcn_alignbit(a5, a4, 16);
      lptr w = kc + 16 * c;
      *(LAS u32x4*)(w) = (u32x4){a0, a1, a2, a3};
      *(LAS u32x4*)(w + KC_COPY) = (u32x4){e0, e1, e2, e3};
      *(LAS u32x4*)(w + 2 * KC_COPY) = (u32x4){a1, a2, a3, a4};
      *(LAS u32x4*)(w + 3 * KC_COPY) = (u32x4){e1, e2, e3, e4};
    }
  }
  const float dcoef = bf_lo(flt.dbits);
  WAIT_LGKM0();
  sy_wait(lds, SY_AREADY, 1u);
  sy_wait(lds, SY_EPI, 4u);
  __builtin_amdgcn_s_setprio(C_PRIO);
  STAMP(4);
  STAMPC(28);

  const int m = lane & 15, g = lane >> 4;
  const int rho = (m + 1) & 3;
  lptr abase = kc + rho * KC_COPY + 2 * (8 * g + m - 31 - rho);
  lptr bbase = ur + 2 * (2000 - 16 * m + 8 * g) - 7 * 512;
  f32x4 acc[8];
#pragma unroll
  for (int t = 0; t < 8; ++t) acc[t] = (f32x4){0.f, 0.f, 0.f, 0.f};
  struct CF { s16x4 a[8][2]; bf16x8 b[8]; };
  auto cread = [&](CF& f, int p, int i) {
    if (p < 16) f.a[p >> 1][p & 1] = *(LAS s16x4*)(abase + 64 * i + 512 * (p >> 1) + 8 * (p & 1));
    else f.b[p - 16] = lds_frag(bbase + 64 * i + 512 * (7 - (p - 16)));
  };
  auto cmma = [&](const CF& f, int p) {
    const int t = tri_row(p), u = p - tri_start(t);
    acc[t + u] = MFMA16(join(f.a[t][0], f.a[t][1]), f.b[u], acc[t + u]);
  };
  auto residue = [&](const CF& cur, CF& nxt, int inext) {
#pragma unroll
    for (int p = 0; p < 36; ++p) {
      cmma(cur, p); FENCE();
      if (p < 24) { cread(nxt, p, inext); FENCE(); }
    }
  };
  CF c0, c1;
#pragma unroll
  for (int p = 0; p < 24; ++p) cread(c0, p, 1);
#pragma unroll 1
  for (int i = 1; i <= 8; i += 2) {
    FENCE();
    residue(c0, c1, i + 1);
    residue(c1, c0, (i + 2 <= 8) ? i + 2 : 8);
  }
  STAMP(5);
  STAMPC(29);
  const __amdgpu_buffer_rsrc_t ry = make_rsrc(a.ws + WS_Y2T + (size_t)ch * S * 2, S * 2);
#pragma unroll
  for (int t = 0; t < 8; t += 2) {
    unsigned pk[2][2];
#pragma unroll
    for (int e = 0; e < 2; ++e) {
      const int s = 256 * (t + e) + 16 * m + 4 * g;
      const u32x2 kv = *(LAS u32x2*)(ur + 2 * (2044 - s));
      pk[e][0] = pack_bf16(acc[t + e][0] + bf_hi(kv[1]) * dcoef, acc[t + e][1] + bf_lo(kv[1]) * dcoef);
      pk[e][1] = pack_bf16(acc[t + e][2] + bf_hi(kv[0]) * dcoef, acc[t + e][3] + bf_lo(kv[0]) * dcoef);
    }
    const auto r0 = __builtin_amdgcn_permlane16_swap(pk[0][0], pk[1][0], false, false);
    const auto r1 = __builtin_amdgcn_permlane16_swap(pk[0][1], pk[1][1], false, false);
    const u32x4 o = {r0[0], r1[0], r0[1], r1[1]};
    const unsigned off = 2 * (256 * (t + (g & 1)) + 16 * m + 8 * (g >> 1));
    __builtin_amdgcn_raw_buffer_store_b128(o, ry, off, 0, SC1);
  }
  STAMP(6);
  WAIT_VM(0);
  STAMP(7);
  if (sy_arrive(lds, SY_CONV, lane) == (unsigned)(nconv - 1)) {
    const unsigned tag = get_tag(lds);
    if (lane == 0) flag_store(a.ws, WS_FLAGY + 4 * blockIdx.x, tag);
  }
}

__global__ __launch_bounds__(512, 2) void fused_kernel(Args a) {
  extern __shared__ __attribute__((aligned(16))) char smem[];
  lptr lds = (lptr)smem;
  const int tid = threadIdx.x, lane = tid & 63;
  const int wave = __builtin_amdgcn_readfirstlane(tid >> 6);
  const int bid = blockIdx.x;
  const int xcd = bid & 7, j = bid >> 3;
  const int mi = 8 * (xcd & 1) + (j & 7), fi = 4 * (xcd >> 1) + (j >> 3);
  const int si = 8 * (xcd & 3) + (j & 7), di = 4 * (xcd >> 2) + (j >> 3);
  const int c0 = (5 * bid) >> 1, nconv = 2 + (bid & 1);
  const unsigned lds0 = lds_addr(lds);
  STAMP_INIT();
  STAMP(0);

  if (wave == 7) {
    if (lane < 32) *sy(lds, lane) = 0u;
    WAIT_LGKM0();
  }
  BARRIER();

  if (wave == 7) {
    const unsigned epoch = __hip_atomic_load((GAS unsigned*)(a.ws + WS_EPOCH), __ATOMIC_RELAXED, __HIP_MEMORY_SCOPE_AGENT);
    {
      const int r0 = 128 * mi + 8 * fi, gr = lane >> 4, gq = lane & 15;
      u32x4 gx[2][5], gw[5];
#pragma unroll
      for (int i = 0; i < 5; ++i) {
        gw[i] = ((const u32x4*)a.gw)[gq + 16 * i];
        gx[0][i] = ((const u32x4*)(a.x + (size_t)(r0 + gr) * D))[gq + 16 * i];
        gx[1][i] = ((const u32x4*)(a.x + (size_t)(r0 + 4 + gr) * D))[gq + 16 * i];
      }
      FENCE();
      sy_set(lds, SY_TAG, epoch + 1u, lane);
      sy_set(lds, SY_TAGOK, 1u, lane);
#pragma unroll
      for (int pass = 0; pass < 2; ++pass) {
        float sum = 0.f;
#pragma unroll
        for (int i = 0; i < 5; ++i)
#pragma unroll
          for (int e = 0; e < 4; ++e) sum = dot2(gx[pass][i][e], gw[i][e], sum);
#pragma unroll
        for (int m = 8; m >= 1; m >>= 1) sum += __shfl_xor(sum, m);
        if (gq == 0)
          __hip_atomic_store((GAS unsigned*)(a.ws + WS_GATE) + r0 + 4 * pass + gr, __builtin_bit_cast(unsigned, sigmoidf_(sum)),
                             __ATOMIC_RELAXED, __HIP_MEMORY_SCOPE_AGENT);
      }
      WAIT_VM(0);
      sy_set(lds, SY_GATE, 1u, lane);
    }
    const unsigned tag = epoch + 1u;
    STAMP(1);
    if (TOUCH1) {
      __builtin_amdgcn_s_sleep(TOUCH_DELAY);
      const int L = 56 * j + (lane < 56 ? lane : 0);
      const char* tbase; int tstep;
      if (L < 1024) { tbase = (const char*)a.x + (size_t)(1024 * (xcd & 1) + L) * (D * 2); tstep = 128; }
      else {
        const int l2 = L - 1024, mat = l2 / 384, k = (l2 - 384 * mat) / 6, c = l2 % 6;
        tbase = (const char*)(mat ? a.wu : a.wg) + (size_t)k * (F * 2) + 768 * (xcd >> 1) + 128 * c; tstep = 64 * F * 2;
      }
#pragma unroll
      for (int sl = 2; sl < 10; ++sl) touch(tbase + (size_t)sl * tstep, lds0 + L_DUMMY);
    }
    sy_wait4m(lds, POLL_AFTER);
    STAMP(2);
    {
      flag_wait(a.ws, WS_FLAGKV + 4 * (32 * (bid >> 5) + (lane & 31)), true, tag);
      STAMP(8);
      u32x4 uv[3][4];
#pragma unroll
      for (int cw = 0; cw < 3; ++cw) {
        const __amdgpu_buffer_rsrc_t ru = make_rsrc(a.ws + WS_KVR + (size_t)(c0 + (cw < nconv ? cw : 0)) * S * 2, S * 2);
#pragma unroll
        for (int i = 0; i < 4; ++i) uv[cw][i] = ld128(ru, (unsigned)(i * 1024 + lane * 16), 0, SC1);
      }
      FENCE();
#pragma unroll
      for (int cw = 0; cw < 3; ++cw) {
        lptr ur = lds + L_AUX + cw * UR_BYTES;
#pragma unroll
        for (int i = 0; i < 4; ++i) *(LAS u32x4*)(ur + i * 1024 + lane * 16) = uv[cw][i];
        if (lane < 32) *(LAS u32x4*)(ur + 4096 + lane * 16) = (u32x4){0u, 0u, 0u, 0u};
      }
      WAIT_LGKM0();
      sy_set(lds, SY_AREADY, 1u, lane);
    }
    STAMP(9);
    FENCE();
    {
      const char* base = (const char*)a.wd + 640 * (xcd >> 2);
#pragma unroll
      for (int t = 0; t < 4; ++t) {
        const int id = min(64 * t + lane, 239), r = id / 5, c = id - 5 * r;
        touch(base + (size_t)(48 * j + r) * (D * 2) + 128 * c, lds0 + L_DUMMY);
      }
    }
    sy_wait(lds, SY_WPRE, 4u);
    flag_wait<HPOLL_SLEEP>(a.ws, WS_FLAGH + 4 * (16 * (si >> 1) + (lane & 15)), true, tag);
    sy_set(lds, SY_HREADY, 1u, lane);
    STAMP(10);
    sy_wait(lds, SY_CONV, nconv);
    sy_wait(lds, SY_GSTEP, 1u);
    flag_wait<YPOLL_SLEEP>(a.ws, WS_FLAGY + 4 * (32 * di + (lane & 31)), true, tag);
    STAMP(11);
    {
      const __amdgpu_buffer_rsrc_t ry = make_rsrc(a.ws + WS_Y2T, S * D * 2);
      u32x4 yv[10];
#pragma unroll
      for (int i = 0; i < 10; ++i) {
        const int id = lane + 64 * i;
        yv[i] = ld128(ry, (unsigned)((80 * di + (id >> 3)) * (S * 2) + 64 * si * 2 + (id & 7) * 16), 0, SC1);
      }
#pragma unroll
      for (int i = 0; i < 10; ++i) {
        const int id = lane + 64 * i, w = (id & 7) >> 1;
        const int so = w == 0 ? ystrip_off(0) : w == 1 ? ystrip_off(1) : w == 2 ? ystrip_off(2) : ystrip_off(3);
        *(LAS u32x4*)(lds + so + (id >> 3) * 32 + (id & 1) * 16) = yv[i];
      }
      WAIT_LGKM0();
      sy_set(lds, SY_YREADY, 1u, lane);
    }
    STAMP(12);
    if (bid == 0) {
#pragma unroll
      for (int i = 0; i < 4; ++i) flag_wait(a.ws, WS_FLAGKV + 4 * (lane + 64 * i), true, tag);
      if (lane == 0) flag_store(a.ws, WS_EPOCH, tag);
    }
    WAIT_VM(0);
    STAMP_DUMP();
    return;
  }

  if (wave >= 4) {
    const int cw = wave - 4;
    const bool active = cw < nconv;
    const int ch = c0 + (active ? cw : 0);
    Filter flt;
    const int rot1 = ROT1 ? (3 * (j >> 3) + (j & 7)) % 10 : 0;
    if (cw == 0) loader<6>(a, lds, 0, 0, mi, fi, rot1, flt, ch, lane);
    else loader<5>(a, lds, cw, 1 + 5 * cw, mi, fi, rot1, flt, ch, lane);
    STAMP(3);
    if (active) {
      sy_wait4(lds, SY_CONSUMED, cw == 2 ? 9u : 8u);
      conv_wave(a, lds, lds + (cw == 2 ? 2 * STAGE : STAGE + cw * KC_BYTES), lds + L_AUX + cw * UR_BYTES, flt, ch, nconv, lane);
    }
    STAMP_DUMP();
    return;
  }

  f32x4 ag[4][3], au[4][3];
  {
    const int tsi = bid >> 3, tdi = bid & 7;
    const int task = 40 * wave + (lane < 40 ? lane : 0);
    KvTask t0;
    kv_load(a, t0, task, tsi, tdi);
    FENCE();
#pragma unroll
    for (int t = 0; t < 4; ++t)
#pragma unroll
      for (int c = 0; c < 3; ++c) {
        ag[t][c] = (f32x4){0.f, 0.f, 0.f, 0.f}; au[t][c] = (f32x4){0.f, 0.f, 0.f, 0.f};
        asm volatile("" : "+v"(ag[t][c]), "+v"(au[t][c]));
      }
    FENCE();
    STAMP(1);
    if (lane < 40) kv_compute(t0, lds, task, tsi);
    sy_arrive(lds, SY_KV, lane);
    sy_wait(lds, SY_KV, 4u);
    STAMP(2);
    const __amdgpu_buffer_rsrc_t rkv = make_rsrc(a.ws + WS_KVR, S * D * 2);
#pragma unroll
    for (int i = 0; i < 3; ++i) {
      const int id = tid + 256 * i;
      if (id < 640) {
        const int dl = id >> 3, jj = id & 7;
        const u32x4 val = *(LAS u32x4*)(lds + L_T + dl * 128 + ((jj ^ ((dl >> 3) & 7)) << 4));
        __builtin_amdgcn_raw_buffer_store_b128(val, rkv, (unsigned)(((80 * tdi + dl) * S + (1984 - 64 * tsi) + 8 * jj) * 2), 0, SC1);
      }
    }
  }
  const int n = lane & 15, g = lane >> 4;
  {
    const int ws_ = wave & 1, wf = wave >> 1;
    const int q = n >> 2, p = lane & 3;
    const int xb0 = XB + (64 * ws_ + n) * 128 + ((g ^ (n >> 1)) << 4);
    int wb_[3];
#pragma unroll
    for (int c = 0; c < 3; ++c) wb_[c] = GB + (8 * g + q) * 192 + 8 * p + 32 * ((3 * wf + c) ^ (g & 1));
    auto read_piece = [&](Frags& f, int i, lptr b, int kk) {
      if (i < 4) {
        f.x[i] = lds_frag(b + (xb0 ^ (kk << 6)) + i * 2048);
      } else {
        const int c = (i - 4) >> 2, u = ((i - 4) >> 1) & 1, t = (i - 4) & 1;
        const s16x4 v = lds_tr(b + wb_[c] + kk * (32 * 192) + u * (UB - GB) + t * (4 * 192));
        if (u == 0) f.g[c][t] = v; else f.u[c][t] = v;
      }
    };
    auto mma_piece = [&](const Frags& f, int i) {
      const int c = i >> 3, t = (i >> 1) & 3;
      if ((i & 1) == 0) ag[t][c] = MFMA16(join(f.g[c][0], f.g[c][1]), f.x[t], ag[t][c]);
      else au[t][c] = MFMA16(join(f.u[c][0], f.u[c][1]), f.x[t], au[t][c]);
    };
    auto half = [&](const Frags& cur, Frags& nxt, lptr nb, int nkk, bool rd, auto&& hook) {
#pragma unroll
      for (int i = 0; i < 24; ++i) {
        mma_piece(cur, i); FENCE();
        if (rd && i < 16) { read_piece(nxt, i, nb, nkk); FENCE(); }
        hook(i);
      }
    };
    auto nohook = [](int) {};
    const unsigned cons_addr = lds0 + L_SYNC + 4 * (SY_CONSUMED + wave);
    {
#pragma unroll 1
      for (unsigned it = 0; it < SPIN_LDS; ++it) {
        const u32x4 l = sy_get4(lds, SY_LANDED);
        if ((unsigned)__builtin_amdgcn_readfirstlane(min(min(l[0], l[1]), l[2])) >= 1u) break;
        __builtin_amdgcn_s_sleep(2);
      }
      asm volatile("" ::: "memory");
    }
    STAMP(3);
    STAMPC(28);
    __builtin_amdgcn_s_setprio(M_PRIO);
    Frags f0, f1;
#pragma unroll
    for (int i = 0; i < 16; ++i) read_piece(f0, i, lds, 0);
    FENCE();
    STAMPC(27);
    int buf = 0;
#pragma unroll 1
    for (int s = 0; s < 9; ++s) {
      const int nbuf = (buf == 2) ? 0 : buf + 1;
      FENCE();
      u32x4 l;
      unsigned seen = 0;
      half(f0, f1, lds + buf * STAGE, 1, true, [&](int i) {
        if (i == LAND_RD) { l = sy_get4(lds, SY_LANDED); FENCE(); }
        if (i == 22) { seen = __builtin_amdgcn_readfirstlane(min(min(l[0], l[1]), l[2])); FENCE(); }
      });
      if (seen < (unsigned)(s + 2)) {
#pragma unroll 1
        for (unsigned it = 0; it < SPIN_LDS; ++it) {
          l = sy_get4(lds, SY_LANDED);
          if ((unsigned)__builtin_amdgcn_readfirstlane(min(min(l[0], l[1]), l[2])) >= (unsigned)(s + 2)) break;
        }
      }
      if (s == SIDE_PUB_ITER) {
        WAIT_VM(0);
        if (sy_arrive(lds, SY_SIDE, lane) == 3u) {
          const unsigned tag = get_tag(lds);
          if (lane == 0) flag_store(a.ws, WS_FLAGKV + 4 * (32 * (bid & 7) + (bid >> 3)), tag);
        }
      }
      asm volatile("" ::: "memory");
      FENCE();
      half(f1, f0, lds + nbuf * STAGE, 0, true, [&](int i) {
        if (i == 3) { asm volatile("s_waitcnt lgkmcnt(4)" ::: "memory"); sy_post(cons_addr, s + 1); FENCE(); }
        if (i == 5) { wake(); FENCE(); }
      });
      buf = nbuf;
    }
    half(f0, f1, lds + buf * STAGE, 1, true, nohook);
    half(f1, f0, lds, 0, false, [&](int i) {
      if (i == 3) { WAIT_LGKM0(); sy_post(cons_addr, 10u); FENCE(); }
      if (i == 5) { wake(); FENCE(); }
    });
    STAMP(4);
    STAMPC(29);

    const __amdgpu_buffer_rsrc_t rhs = make_rsrc(a.ws + WS_HT, S * F * 2);
#pragma unroll
    for (int t = 0; t < 4; ++t) {
      u32x2 pk[3];
#pragma unroll
      for (int c = 0; c < 3; ++c) {
        float h[4];
#pragma unroll
        for (int r = 0; r < 4; ++r) h[r] = sigmoidf_(ag[t][c][r]) * au[t][c][r];
        pk[c] = (u32x2){pack_bf16(h[0], h[1]), pack_bf16(h[2], h[3])};
      }
      const unsigned base = (unsigned)(((8 * mi + 4 * ws_ + t) * 48 + 3 * fi) * 1024 + lane * 16);
      if (wf == 0) {
        __builtin_amdgcn_raw_buffer_store_b128((u32x4){pk[0][0], pk[0][1], pk[1][0], pk[1][1]}, rhs, base, 0, SC1);
        __builtin_amdgcn_raw_buffer_store_b64(pk[2], rhs, base + 1024, 0, SC1);
      } else {
        __builtin_amdgcn_raw_buffer_store_b64(pk[0], rhs, base + 1024 + 8, 0, SC1);
        __builtin_amdgcn_raw_buffer_store_b128((u32x4){pk[1][0], pk[1][1], pk[2][0], pk[2][1]}, rhs, base + 2048, 0, SC1);
      }
    }
  }
  STAMP(5);

  const __amdgpu_buffer_rsrc_t rh = make_rsrc(a.ws + WS_HT, S * F * 2);
  const __amdgpu_buffer_rsrc_t rw = make_rsrc(a.wd, F * D * 2);
  const unsigned wr0 = wave == 0 ? wring_off(0) : wave == 1 ? wring_off(1) : wave == 2 ? wring_off(2) : wring_off(3);
  const int rot = 3 * (j >> 3);
  auto kstep = [&](int b) { const int t = b + rot; return 12 * wave + (t >= 12 ? t - 12 : t); };
  unsigned vwd[5];
#pragma unroll
  for (int i = 0; i < 5; ++i) {
    const int slot = 64 * i + lane, row = slot / 10, ch = slot - 10 * row;
    vwd[i] = (unsigned)(row * (D * 2) + 160 * di + ch * 16 - 1024 * (i & 3));
  }
  auto load_w = [&](int b) {
    const unsigned dst = lds0 + wr0 + (b & 3) * WSLOT, so = (unsigned)kstep(b) * (32 * D * 2);
    dma16xN<4>(rw, dst, vwd, so);
    dma16xN<1>(rw, dst + 4096, vwd + 4, so);
  };
  struct HS4 { u32x4 f[4]; };
  HS4 H[4];
  const unsigned voff = lane * 16;
  auto load_h = [&](HS4& h, int b) {
    const unsigned so = (unsigned)(4 * si * 48 + kstep(b)) * 1024;
    asm volatile("buffer_load_dwordx4 %0, %4, %5, %6 offen sc1\n\tbuffer_load_dwordx4 %1, %4, %5, %7 offen sc1\n\t"
                 "buffer_load_dwordx4 %2, %4, %5, %8 offen sc1\n\tbuffer_load_dwordx4 %3, %4, %5, %9 offen sc1"
                 : "=&v"(h.f[0]), "=&v"(h.f[1]), "=&v"(h.f[2]), "=&v"(h.f[3])
                 : "v"(voff), "s"(rh), "s"(so), "s"(so + 48 * 1024), "s"(so + 96 * 1024), "s"(so + 144 * 1024) : "memory");
  };
  const int srow = 64 * si + 16 * wave + n;
  u32x2 qv[5];
  FENCE();
  sy_arrive(lds, SY_EPI, lane);
  __builtin_amdgcn_s_setprio(G_PRIO);
  STAMP(22);
  WAIT_VM(0);
  if (sy_arrive(lds, SY_HDONE, lane) == 3u) {
    sy_wait(lds, SY_GATE, 1u);
    const unsigned tag = get_tag(lds);
    if (lane == 0) flag_store(a.ws, WS_FLAGH + 4 * (16 * mi + fi), tag);
  }
  STAMP(23);
  FENCE();
#pragma unroll
  for (int c = 0; c < 5; ++c) qv[c] = *(const u32x2*)(a.x + (size_t)srow * D + 80 * di + 16 * c + 4 * g);
  FENCE();
  sy_wait4(lds, SY_CONSUMED, 10u);
  load_w(0); load_w(1); load_w(2); load_w(3);
  f32x4 acc[4][5];
#pragma unroll
  for (int t = 0; t < 4; ++t)
#pragma unroll
    for (int c = 0; c < 5; ++c) { acc[t][c] = (f32x4){0.f, 0.f, 0.f, 0.f}; asm volatile("" : "+v"(acc[t][c])); }
  struct WF { s16x4 f[5][2]; };
  WF w0, w1;
  const lptr wrd = lds + wr0 + (4 * g + (n >> 2)) * 160 + 8 * (lane & 3);
  auto read_w = [&](WF& w, int p, int b) {
    w.f[p >> 1][p & 1] = lds_tr(wrd + (b & 3) * WSLOT + (p & 1) * (16 * 160) + (p >> 1) * 32);
  };
  STAMP(24);
  WAIT_VM(15);
#pragma unroll
  for (int p = 0; p < 10; ++p) read_w(w0, p, 0);
  WAIT_LGKM0();
  load_w(4);
  sy_arrive(lds, SY_WPRE, lane);
  STAMP(6);
  sy_wait(lds, SY_HREADY, 1u);
  STAMP(7);
  load_h(H[0], 0); load_h(H[1], 1); load_h(H[2], 2);
  const float gate = __builtin_bit_cast(float, __hip_atomic_load((GAS unsigned*)(a.ws + WS_GATE) + srow, __ATOMIC_RELAXED, __HIP_MEMORY_SCOPE_AGENT));
  FENCE();
  auto gstep = [&](const WF& cur, WF& nxt, HS4& h, HS4& hn, int b) {
    constexpr int NB[12] = {9, 9, 13, 18, 18, 18, 18, 18, 18, 13, 4, 0};
    asm volatile("s_waitcnt vmcnt(%4)" : "+v"(h.f[0]), "+v"(h.f[1]), "+v"(h.f[2]), "+v"(h.f[3]) : "n"(NB[b]) : "memory");
    FENCE();
#pragma unroll
    for (int i = 0; i < 20; ++i) {
      acc[i & 3][i >> 2] = MFMA16(join(cur.f[i >> 2][0], cur.f[i >> 2][1]), __builtin_bit_cast(bf16x8, h.f[i & 3]), acc[i & 3][i >> 2]);
      FENCE();
      if (i == 0 && b + 3 < 12) { load_h(hn, b + 3); FENCE(); }
      if (i == 16 && b >= 1 && b + 4 < 12) { load_w(b + 4); FENCE(); }
      if (i >= 6 && i < 16 && b + 1 < 12) { read_w(nxt, i - 6, b + 1); FENCE(); }
      if (b == YPOLL_STEP && i == 17 && wave == 0) { sy_post(lds0 + L_SYNC + 4 * SY_GSTEP, 1u); FENCE(); }
      if (b == YPOLL_STEP && i == 19 && wave == 0) { wake(); FENCE(); }
    }
  };
#pragma unroll
  for (int b = 0; b < 12; b += 2) {
    gstep(w0, w1, H[b & 3], H[(b + 3) & 3], b);
    if (b == 0) STAMPI(14);
    gstep(w1, w0, H[(b + 1) & 3], H[(b + 4) & 3], b + 1);
    STAMPI(15 + (b >> 1));
  }
  STAMP(8);

  f32x4 bsum[5];
  {
#pragma unroll
    for (int t = 0; t < 4; ++t) {
      if (t != wave) {
#pragma unroll
        for (int c = 0; c < 5; ++c) *(LAS f32x4*)(lds + wr0 + (((t - wave - 1) & 3) * 5 + c) * 1024 + lane * 16) = acc[t][c];
      } else {
#pragma unroll
        for (int c = 0; c < 5; ++c) bsum[c] = acc[t][c];
      }
    }
    sy_arrive(lds, SY_RED, lane);
    sy_wait(lds, SY_RED, 4u);
#pragma unroll
    for (int w = 0; w < 4; ++w) {
      if (w != wave) {
#pragma unroll
        for (int c = 0; c < 5; ++c) bsum[c] += *(LAS f32x4*)(lds + wring_off(w) + (((wave - w - 1) & 3) * 5 + c) * 1024 + lane * 16);
      }
    }
  }
  STAMP(9);
  sy_wait(lds, SY_YREADY, 1u);
  STAMP(10);
  {
    const int q = n >> 2, p = lane & 3;
    const lptr ystr = lds + (wave == 0 ? ystrip_off(0) : wave == 1 ? ystrip_off(1) : wave == 2 ? ystrip_off(2) : ystrip_off(3));
#pragma unroll
    for (int c = 0; c < 5; ++c) {
      const s16x4 yv = lds_tr(ystr + (16 * c + 4 * g + q) * 32 + 8 * p);
      const u32x2 y = __builtin_bit_cast(u32x2, yv);
      const float o0 = gate * (bf_lo(qv[c][0]) * bf_lo(y[0])) + (1.f - gate) * bsum[c][0];
      const float o1 = gate * (bf_hi(qv[c][0]) * bf_hi(y[0])) + (1.f - gate) * bsum[c][1];
      const float o2 = gate * (bf_lo(qv[c][1]) * bf_lo(y[1])) + (1.f - gate) * bsum[c][2];
      const float o3 = gate * (bf_hi(qv[c][1]) * bf_hi(y[1])) + (1.f - gate) * bsum[c][3];
      *(u32x2*)(a.out + (size_t)srow * D + 80 * di + 16 * c + 4 * g) = (u32x2){pack_bf16(o0, o1), pack_bf16(o2, o3)};
    }
  }
  STAMP(11);
}

}

extern "C" void kernel_launch(void* const* d_in, const int* in_sizes, int n_in, void* d_out, int out_size, void* d_ws,
                              size_t ws_size, hipStream_t stream) {
  (void)in_sizes; (void)n_in; (void)out_size; (void)ws_size;
  Args a;
  a.x = (const u16*)d_in[0];
  a.k = (const u16*)d_in[1];
  a.v = (const u16*)d_in[2];
  a.shk = (const u16*)d_in[3];
  a.ssmk = (const u16*)d_in[4];
  a.ssmd = (const u16*)d_in[5];
  a.wg = (const u16*)d_in[7];
  a.wu = (const u16*)d_in[8];
  a.wd = (const u16*)d_in[9];
  a.gw = (const u16*)d_in[10];
  a.ws = (char*)d_ws;
  a.out = (u16*)d_out;
  (void)hipFuncSetAttribute((const void*)fused_kernel, hipFuncAttributeMaxDynamicSharedMemorySize, LDS_BYTES);
  fused_kernel<<<256, 512, LDS_BYTES, stream>>>(a);
}
```

```cpp
#include <hip/hip_runtime.h>
#include <hip/hip_bf16.h>

namespace {

typedef unsigned short u16;
typedef __bf16 bf16x8 __attribute__((ext_vector_type(8)));
typedef __bf16 bf16x2 __attribute__((ext_vector_type(2)));
typedef short s16x4 __attribute__((ext_vector_type(4)));
typedef float f32x2 __attribute__((ext_vector_type(2)));
typedef float f32x4 __attribute__((ext_vector_type(4)));
typedef unsigned u32x2 __attribute__((ext_vector_type(2)));
typedef unsigned u32x4 __attribute__((ext_vector_type(4)));
#define LAS __attribute__((address_space(3)))
#define GAS __attribute__((address_space(1)))
typedef LAS char* lptr;

constexpr int S = 2048, D = 640, F = 1536;
constexpr int SC1 = 16;

constexpr size_t WS_HT = 0;
constexpr size_t WS_KVR = WS_HT + (size_t)S * F * 2;
constexpr size_t WS_Y2T = WS_KVR + (size_t)S * D * 2;
constexpr size_t WS_GATE = WS_Y2T + (size_t)S * D * 2;
constexpr size_t WS_FLAGKV = WS_GATE + (size_t)S * 4;
constexpr size_t WS_FLAGH = WS_FLAGKV + 1024;
constexpr size_t WS_FLAGY = WS_FLAGH + 1024;
constexpr size_t WS_EPOCH = WS_FLAGY + 1024;
constexpr size_t WS_END = WS_EPOCH + 256;

constexpr int STAGE = 40960, XB = 0, GB = 16384, UB = 28672;
constexpr int L_AUX = 3 * STAGE;
constexpr int L_T = L_AUX, UR_BYTES = 4608;
constexpr int L_FREE = L_AUX + 3 * UR_BYTES;
constexpr int L_SYNC = L_FREE + 20480 + 2 * 2560;
constexpr int L_DUMMY = L_SYNC + 128;
constexpr int L_END = L_DUMMY + 256;
constexpr int KC_COPY = 4160, KC_BYTES = 4 * KC_COPY;
constexpr int WSLOT = 5120;
__device__ constexpr int wring_off(int w) { return w == 0 ? 0 : w == 1 ? 20480 : w == 2 ? 2 * STAGE + KC_BYTES : L_FREE; }
__device__ constexpr int ystrip_off(int w) { return w < 2 ? STAGE + 2 * KC_BYTES + w * 2560 : L_FREE + 20480 + (w - 2) * 2560; }
static_assert(2 * STAGE + KC_BYTES + 20480 <= 3 * STAGE && STAGE + 2 * KC_BYTES + 2 * 2560 <= 2 * STAGE, "second-phase LDS map");
enum { SY_LANDED = 0, SY_CONSUMED = 4, SY_KV = 8, SY_SIDE = 9, SY_HDONE = 10, SY_RED = 11, SY_CONV = 12, SY_AREADY = 13,
       SY_HREADY = 14, SY_YREADY = 15, SY_TAG = 16, SY_TAGOK = 17, SY_GSTEP = 18, SY_GATE = 19, SY_EPI = 20, SY_WPRE = 21 };

#define STAMP(i) do {} while (0)
#define STAMPC(i) do {} while (0)
#define STAMPI(i) do {} while (0)
#define STAMP_INIT() do {} while (0)
#define STAMP_DUMP() do {} while (0)
constexpr int LDS_BYTES = L_END;

#define MFMA16(a, b, c) __builtin_amdgcn_mfma_f32_16x16x32_bf16((a), (b), (c), 0, 0, 0)
#define WAIT_VM(n) asm volatile("s_waitcnt vmcnt(" #n ")" ::: "memory")
#define WAIT_LGKM0() asm volatile("s_waitcnt lgkmcnt(0)" ::: "memory")
#define BARRIER() do { asm volatile("" ::: "memory"); __builtin_amdgcn_s_barrier(); asm volatile("" ::: "memory"); } while (0)
#define FENCE() __builtin_amdgcn_sched_barrier(0)
constexpr unsigned SPIN_LDS = 1u << 22, SPIN_MEM = 1u << 16;

__device__ __forceinline__ unsigned pack_bf16(float lo, float hi) {
  f32x2 v = {lo, hi};
  return __builtin_bit_cast(unsigned, __builtin_convertvector(v, bf16x2));
}
__device__ __forceinline__ float bf_lo(unsigned w) { return __builtin_bit_cast(float, w << 16); }
__device__ __forceinline__ float bf_hi(unsigned w) { return __builtin_bit_cast(float, w & 0xffff0000u); }
__device__ __forceinline__ float sigmoidf_(float x) {
  return __builtin_amdgcn_rcpf(1.0f + __builtin_amdgcn_exp2f(x * -1.4426950408889634f));
}
__device__ __forceinline__ __amdgpu_buffer_rsrc_t make_rsrc(const void* p, unsigned bytes) {
  return __builtin_amdgcn_make_buffer_rsrc(const_cast<void*>(p), 0, bytes, 0x00020000);
}
__device__ __forceinline__ unsigned lds_addr(lptr p) { return (unsigned)(size_t)p; }
__device__ __forceinline__ void dma16(__amdgpu_buffer_rsrc_t r, unsigned dst, unsigned voff, unsigned soff) {
  asm volatile("s_mov_b32 m0, %0\n\ts_nop 0\n\tbuffer_load_dwordx4 %1, %2, %3 offen lds"
               :: "s"(dst), "v"(voff), "s"(r), "s"(soff) : "memory");
}
template <int N>
__device__ __forceinline__ void dma16xN(__amdgpu_buffer_rsrc_t r, unsigned dst, const unsigned* v, unsigned soff) {
  static_assert(N >= 1 && N <= 4, "12-bit instruction offset");
  if constexpr (N == 1)
    asm volatile("s_mov_b32 m0, %0\n\ts_nop 0\n\tbuffer_load_dwordx4 %1, %2, %3 offen lds"
                 :: "s"(dst), "v"(v[0]), "s"(r), "s"(soff) : "memory");
  else if constexpr (N == 2)
    asm volatile("s_mov_b32 m0, %0\n\ts_nop 0\n\tbuffer_load_dwordx4 %1, %3, %4 offen lds\n\t"
                 "buffer_load_dwordx4 %2, %3, %4 offen offset:1024 lds"
                 :: "s"(dst), "v"(v[0]), "v"(v[1]), "s"(r), "s"(soff) : "memory");
  else if constexpr (N == 3)
    asm volatile("s_mov_b32 m0, %0\n\ts_nop 0\n\tbuffer_load_dwordx4 %1, %4, %5 offen lds\n\t"
                 "buffer_load_dwordx4 %2, %4, %5 offen offset:1024 lds\n\tbuffer_load_dwordx4 %3, %4, %5 offen offset:2048 lds"
                 :: "s"(dst), "v"(v[0]), "v"(v[1]), "v"(v[2]), "s"(r), "s"(soff) : "memory");
  else
    asm volatile("s_mov_b32 m0, %0\n\ts_nop 0\n\tbuffer_load_dwordx4 %1, %5, %6 offen lds\n\t"
                 "buffer_load_dwordx4 %2, %5, %6 offen offset:1024 lds\n\tbuffer_load_dwordx4 %3, %5, %6 offen offset:2048 lds\n\t"
                 "buffer_load_dwordx4 %4, %5, %6 offen offset:3072 lds"
                 :: "s"(dst), "v"(v[0]), "v"(v[1]), "v"(v[2]), "v"(v[3]), "s"(r), "s"(soff) : "memory");
}
__device__ __forceinline__ void touch(const void* p, unsigned dummy_lds) {
  asm volatile("s_mov_b32 m0, %0\n\ts_nop 0\n\tglobal_load_lds_dword %1, off" :: "s"(dummy_lds), "v"(p) : "memory");
}
__device__ __forceinline__ s16x4 lds_tr(lptr p) { return __builtin_amdgcn_ds_read_tr16_b64_v4i16((LAS s16x4*)p); }
__device__ __forceinline__ bf16x8 join(s16x4 a, s16x4 b) {
  return __builtin_bit_cast(bf16x8, __builtin_shufflevector(a, b, 0, 1, 2, 3, 4, 5, 6, 7));
}
__device__ __forceinline__ bf16x8 lds_frag(lptr p) { return *(LAS bf16x8*)p; }
__device__ __forceinline__ u32x4 ld128(__amdgpu_buffer_rsrc_t r, unsigned voff, unsigned soff, int aux) {
  return aux ? __builtin_bit_cast(u32x4, __builtin_amdgcn_raw_buffer_load_b128(r, voff, soff, SC1))
             : __builtin_bit_cast(u32x4, __builtin_amdgcn_raw_buffer_load_b128(r, voff, soff, 0));
}

__device__ __forceinline__ LAS unsigned* sy(lptr lds, int i) { return (LAS unsigned*)(lds + L_SYNC) + i; }
__device__ __forceinline__ unsigned sy_get(lptr lds, int i) {
  return __builtin_amdgcn_readfirstlane(__hip_atomic_load(sy(lds, i), __ATOMIC_RELAXED, __HIP_MEMORY_SCOPE_WORKGROUP));
}
__device__ __forceinline__ void wake() { asm volatile("s_wakeup" ::: "memory"); }
__device__ __forceinline__ void sy_set(lptr lds, int i, unsigned v, int lane) {
  if (lane == 0) __hip_atomic_store(sy(lds, i), v, __ATOMIC_RELAXED, __HIP_MEMORY_SCOPE_WORKGROUP);
  asm volatile("s_waitcnt lgkmcnt(0)\n\ts_wakeup" ::: "memory");
}
#define TOUCH_H 0
#define LOADER_POST_FIRST 1
#define HPOLL_SLEEP 4
#define YPOLL_SLEEP 8
#define YPOLL_STEP 8
#ifndef M_PRIO
#define M_PRIO 2
#endif
#define G_PRIO 0
#define ROT1 1
#define DMA_SPACE 0
#define C_PRIO 2
#define G_HDEPTH 3
#define OUT_AUX 0
#define H_ACQUIRE 0
#define LAND_RD 18
#define TOUCH_DELAY 8
#define TOUCH1 0
#define FILT_ITER 6
#define W7_DELAY 0
#define POLL_AFTER 8
#define LOADER_PRIO 0
#define SLEEP_LDS 4
__device__ __forceinline__ void sy_post(unsigned addr, unsigned v) {
  asm volatile("s_mov_b64 exec, 1\n\tds_write_b32 %0, %1\n\ts_mov_b64 exec, -1" :: "v"(addr), "v"(v) : "memory");
}
__device__ __forceinline__ void sy_wait(lptr lds, int i, unsigned v) {
#pragma unroll 1
  for (unsigned n = 0; n < SPIN_LDS; ++n) {
    if (sy_get(lds, i) >= v) break;
    __builtin_amdgcn_s_sleep(SLEEP_LDS);
  }
  asm volatile("" ::: "memory");
}
__device__ __forceinline__ unsigned sy_arrive(lptr lds, int i, int lane) {
  WAIT_LGKM0();
  unsigned old = 0;
  if (lane == 0) old = __hip_atomic_fetch_add(sy(lds, i), 1u, __ATOMIC_RELAXED, __HIP_MEMORY_SCOPE_WORKGROUP);
  old = __builtin_amdgcn_readfirstlane(old);
  wake();
  return old;
}
__device__ __forceinline__ unsigned min4(u32x4 v) { return min(min(v[0], v[1]), min(v[2], v[3])); }
__device__ __forceinline__ u32x4 sy_get4(lptr lds, int i) { return *(volatile LAS u32x4*)(lds + L_SYNC + 4 * i); }
__device__ __forceinline__ u32x2 sy_get2(lptr lds, int i) { return *(volatile LAS u32x2*)(lds + L_SYNC + 4 * i); }
__device__ __forceinline__ void sy_wait4(lptr lds, int i, unsigned v) {
#pragma unroll 1
  for (unsigned n = 0; n < SPIN_LDS; ++n) {
    if ((unsigned)__builtin_amdgcn_readfirstlane(min4(sy_get4(lds, i))) >= v) break;
    __builtin_amdgcn_s_sleep(SLEEP_LDS);
  }
  asm volatile("" ::: "memory");
}
#define LOADER_SLEEP 1
__device__ __forceinline__ void loader_wait(lptr lds, unsigned v) {
#pragma unroll 1
  for (unsigned n = 0; n < SPIN_LDS; ++n) {
    if ((unsigned)__builtin_amdgcn_readfirstlane(min4(sy_get4(lds, SY_CONSUMED))) >= v) break;
    if (LOADER_SLEEP) __builtin_amdgcn_s_sleep(LOADER_SLEEP);
  }
  asm volatile("" ::: "memory");
}
__device__ __forceinline__ void sy_wait4m(lptr lds, unsigned v) {
#pragma unroll 1
  for (unsigned n = 0; n < SPIN_LDS; ++n) {
    const u32x4 l = sy_get4(lds, SY_LANDED);
    if ((unsigned)__builtin_amdgcn_readfirstlane(min(min(l[0], l[1]), l[2])) >= v) break;
    __builtin_amdgcn_s_sleep(SLEEP_LDS);
  }
  asm volatile("" ::: "memory");
}
__device__ __forceinline__ unsigned get_tag(lptr lds) {
  sy_wait(lds, SY_TAGOK, 1u);
  return sy_get(lds, SY_TAG);
}
__device__ __forceinline__ void flag_store(char* ws, size_t off, unsigned tag) {
  __hip_atomic_store((GAS unsigned*)(ws + off), tag, __ATOMIC_RELAXED, __HIP_MEMORY_SCOPE_AGENT);
}
#define SIDE_PUB_ITER 3
#define POLL_SLEEP 32
template <int SLEEP = POLL_SLEEP>
__device__ __forceinline__ void flag_wait(char* ws, size_t off, bool on, unsigned tag) {
#pragma unroll 1
  for (unsigned n = 0; n < SPIN_MEM; ++n) {
    const unsigned v = __hip_atomic_load((GAS unsigned*)(ws + off), __ATOMIC_RELAXED, __HIP_MEMORY_SCOPE_AGENT);
    if (__all(!on || v == tag)) break;
    __builtin_amdgcn_s_sleep(SLEEP);
  }
  asm volatile("" ::: "memory");
}

struct Args {
  const u16* x; const u16* k; const u16* v; const u16* shk; const u16* ssmk; const u16* ssmd;
  const u16* wg; const u16* wu; const u16* wd; const u16* gw;
  char* ws; u16* out;
};

struct Frags { bf16x8 x[4]; s16x4 g[3][2]; s16x4 u[3][2]; };

__device__ constexpr int tri_start(int t) { return 8 * t - (t * (t - 1)) / 2; }
__device__ constexpr int tri_row(int p) {
  return p < 8 ? 0 : p < 15 ? 1 : p < 21 ? 2 : p < 26 ? 3 : p < 30 ? 4 : p < 33 ? 5 : p < 35 ? 6 : 7;
}

template <int N> __device__ __forceinline__ void wait_vm() { asm volatile("s_waitcnt vmcnt(%0)" ::"n"(N) : "memory"); }

struct Filter { unsigned d[5][6]; unsigned dbits; };
__device__ __forceinline__ void load_filter(const Args& a, Filter& f, int ch, int lane) {
  const __amdgpu_buffer_rsrc_t rk = make_rsrc(a.ssmk + (size_t)ch * S, S * 2);
#pragma unroll
  for (int it = 0; it < 5; ++it) {
    const unsigned off = (unsigned)(16 * (lane + 64 * it) - 32);
    const u32x4 v0 = __builtin_bit_cast(u32x4, __builtin_amdgcn_raw_buffer_load_b128(rk, off, 0, 0));
    const u32x2 v1 = __builtin_bit_cast(u32x2, __builtin_amdgcn_raw_buffer_load_b64(rk, off + 16, 0, 0));
    f.d[it][0] = v0[0]; f.d[it][1] = v0[1]; f.d[it][2] = v0[2]; f.d[it][3] = v0[3]; f.d[it][4] = v1[0]; f.d[it][5] = v1[1];
  }
  f.dbits = a.ssmd[ch];
}

template <int NX>
__device__ __forceinline__ void loader(const Args& a, lptr lds, int lw, int x0, int mi, int fi, int rot, Filter& flt, int ch, int lane) {
  const unsigned lds0 = lds_addr(lds);
  const __amdgpu_buffer_rsrc_t rx = make_rsrc(a.x, S * D * 2);
  const __amdgpu_buffer_rsrc_t rg = make_rsrc(a.wg, D * F * 2);
  const __amdgpu_buffer_rsrc_t ru = make_rsrc(a.wu, D * F * 2);
  unsigned vx[NX], vw[4];
#pragma unroll
  for (int i = 0; i < NX; ++i) {
    const int row = 8 * (x0 + i) + (lane >> 3), c = (lane & 7) ^ ((row >> 1) & 7);
    vx[i] = (unsigned)((128 * mi + row) * (D * 2) + c * 16 - 1024 * (i & 3));
  }
#pragma unroll
  for (int i = 0; i < 4; ++i) {
    const int slot = 64 * (4 * lw + i) + lane, k = slot / 12, chp = slot - 12 * k, c = chp ^ (2 * ((k >> 3) & 1));
    vw[i] = (unsigned)(k * (F * 2) + fi * 192 + c * 16 - 1024 * i);
  }
  const unsigned land_addr = lds0 + L_SYNC + 4 * (SY_LANDED + lw);
  auto slice = [&](int s) { const int k = s + rot; return k >= 10 ? k - 10 : k; };
  auto part_a = [&](int s) {
    const unsigned b = lds0 + (s % 3) * STAGE, so = (unsigned)slice(s) * (64 * F * 2);
    dma16xN<4>(rg, b + GB + 4 * lw * 1024, vw, so);
    if (DMA_SPACE) __builtin_amdgcn_s_sleep(DMA_SPACE);
    dma16xN<4>(ru, b + UB + 4 * lw * 1024, vw, so);
    if (DMA_SPACE) __builtin_amdgcn_s_sleep(DMA_SPACE);
  };
  auto part_b = [&](int s) {
    const unsigned b = lds0 + (s % 3) * STAGE, so = (unsigned)slice(s) * 128;
    dma16xN<4>(rx, b + XB + x0 * 1024, vx, so);
    if (DMA_SPACE) __builtin_amdgcn_s_sleep(DMA_SPACE);
    dma16xN<NX - 4>(rx, b + XB + (x0 + 4) * 1024, vx + 4, so);
  };
  constexpr int P = NX + 8;
  part_a(0); part_b(0);
  part_a(1); part_b(1);
  STAMP(1);
  wait_vm<P>();
  STAMP(2);
  BARRIER();
  part_a(2); part_b(2);
#pragma unroll
  for (int s = 0; s < 9; ++s) {
    if (s == 8) wait_vm<11>(); else wait_vm<P>();
    STAMPI(8 + 2 * s);
    BARRIER();
    STAMPI(9 + 2 * s);
    if (s < 7) { part_a(s + 3); part_b(s + 3); }
    if (s == 7) { FENCE(); load_filter(a, flt, ch, lane); FENCE(); asm volatile("" ::: "memory"); }
  }
}

struct KvTask { u32x4 kr[7], vr[4], wk[4]; };
__device__ __forceinline__ void kv_load(const Args& a, KvTask& t, int task, int tsi, int tdi) {
  const int dc = task % 10, sb = task / 10;
  const int s0 = 64 * tsi + 4 * sb, d0 = 80 * tdi + 8 * dc;
#pragma unroll
  for (int r = 0; r < 7; ++r) {
    const int s = s0 - 3 + r;
    t.kr[r] = *(const u32x4*)(a.k + (size_t)(s < 0 ? 0 : s) * D + d0);
  }
#pragma unroll
  for (int r = 0; r < 4; ++r) t.vr[r] = *(const u32x4*)(a.v + (size_t)(s0 + r) * D + d0);
#pragma unroll
  for (int i = 0; i < 4; ++i) t.wk[i] = *(const u32x4*)(a.shk + (size_t)d0 * 4 + i * 8);
}
__device__ __forceinline__ float dot2(unsigned x, unsigned y, float c) {
  return __builtin_amdgcn_fdot2_f32_bf16(__builtin_bit_cast(bf16x2, x), __builtin_bit_cast(bf16x2, y), c, false);
}
__device__ __forceinline__ float dot2z(unsigned x, unsigned y) {
  float r;
  asm("v_dot2_f32_bf16 %0, %1, %2, 0" : "=v"(r) : "v"(x), "v"(y));
  return r;
}
__device__ __forceinline__ void kv_compute(KvTask& t, lptr lds, int task, int tsi) {
  const int dc = task % 10, sb = task / 10;
  if (tsi == 0 && sb == 0) {
#pragma unroll
    for (int r = 0; r < 3; ++r) t.kr[r] = (u32x4){0u, 0u, 0u, 0u};
  }
  const int sg = 15 - sb;
  lptr dst = lds + L_T + 8 * dc * 128 + (((sg >> 1) ^ (dc & 7)) << 4) + ((sg & 1) << 3);
#pragma unroll
  for (int c = 0; c < 8; ++c) {
    const unsigned w01 = t.wk[c >> 1][2 * (c & 1)], w23 = t.wk[c >> 1][2 * (c & 1) + 1];
    const unsigned sel = (c & 1) ? 0x03020706u : 0x01000504u;
    unsigned pr[6];
#pragma unroll
    for (int i = 0; i < 6; ++i) pr[i] = __builtin_amdgcn_perm(t.kr[i + 1][c >> 1], t.kr[i][c >> 1], sel);
    float o[4];
#pragma unroll
    for (int r = 0; r < 4; ++r) {
      const float ks = dot2(pr[r + 2], w01, dot2z(pr[r], w23));
      o[r] = ks * ((c & 1) ? bf_hi(t.vr[r][c >> 1]) : bf_lo(t.vr[r][c >> 1]));
    }
    *(LAS u32x2*)(dst + c * 128) = (u32x2){pack_bf16(o[3], o[2]), pack_bf16(o[1], o[0])};
  }
}

__device__ __forceinline__ void conv_wave(const Args& a, lptr lds, lptr kc, lptr ur, const Filter& flt, int ch, int nconv, int lane) {
#pragma unroll
  for (int it = 0; it < 5; ++it) {
    const int c = lane + 64 * it;
    if (c < 260) {
      const unsigned a0 = flt.d[it][0], a1 = flt.d[it][1], a2 = flt.d[it][2], a3 = flt.d[it][3], a4 = flt.d[it][4], a5 = flt.d[it][5];
      const unsigned e0 = __builtin_amdgcn_alignbit(a1, a0, 16), e1 = __builtin_amdgcn_alignbit(a2, a1, 16),
                     e2 = __builtin_amdgcn_alignbit(a3, a2, 16), e3 = __builtin_amdgcn_alignbit(a4, a3, 16),
                     e4 = __builtin_amdgcn_alignbit(a5, a4, 16);
      lptr w = kc + 16 * c;
      *(LAS u32x4*)(w) = (u32x4){a0, a1, a2, a3};
      *(LAS u32x4*)(w + KC_COPY) = (u32x4){e0, e1, e2, e3};
      *(LAS u32x4*)(w + 2 * KC_COPY) = (u32x4){a1, a2, a3, a4};
      *(LAS u32x4*)(w + 3 * KC_COPY) = (u32x4){e1, e2, e3, e4};
    }
  }
  const float dcoef = bf_lo(flt.dbits);
  WAIT_LGKM0();
  sy_wait(lds, SY_AREADY, 1u);
  sy_wait(lds, SY_EPI, 4u);
  __builtin_amdgcn_s_setprio(C_PRIO);
  STAMP(4);
  STAMPC(28);

  const int m = lane & 15, g = lane >> 4;
  const int rho = (m + 1) & 3;
  lptr abase = kc + rho * KC_COPY + 2 * (8 * g + m - 31 - rho);
  lptr bbase = ur + 2 * (2000 - 16 * m + 8 * g) - 7 * 512;
  f32x4 acc[8];
#pragma unroll
  for (int t = 0; t < 8; ++t) acc[t] = (f32x4){0.f, 0.f, 0.f, 0.f};
  struct CF { s16x4 a[8][2]; bf16x8 b[8]; };
  auto cread = [&](CF& f, int p, int i) {
    if (p < 16) f.a[p >> 1][p & 1] = *(LAS s16x4*)(abase + 64 * i + 512 * (p >> 1) + 8 * (p & 1));
    else f.b[p - 16] = lds_frag(bbase + 64 * i + 512 * (7 - (p - 16)));
  };
  auto cmma = [&](const CF& f, int p) {
    const int t = tri_row(p), u = p - tri_start(t);
    acc[t + u] = MFMA16(join(f.a[t][0], f.a[t][1]), f.b[u], acc[t + u]);
  };
  auto residue = [&](const CF& cur, CF& nxt, int inext) {
#pragma unroll
    for (int p = 0; p < 36; ++p) {
      cmma(cur, p); FENCE();
      if (p < 24) { cread(nxt, p, inext); FENCE(); }
    }
  };
  CF c0, c1;
#pragma unroll
  for (int p = 0; p < 24; ++p) cread(c0, p, 1);
#pragma unroll 1
  for (int i = 1; i <= 8; i += 2) {
    FENCE();
    residue(c0, c1, i + 1);
    residue(c1, c0, (i + 2 <= 8) ? i + 2 : 8);
  }
  STAMP(5);
  STAMPC(29);
  const __amdgpu_buffer_rsrc_t ry = make_rsrc(a.ws + WS_Y2T + (size_t)ch * S * 2, S * 2);
#pragma unroll
  for (int t = 0; t < 8; t += 2) {
    unsigned pk[2][2];
#pragma unroll
    for (int e = 0; e < 2; ++e) {
      const int s = 256 * (t + e) + 16 * m + 4 * g;
      const u32x2 kv = *(LAS u32x2*)(ur + 2 * (2044 - s));
      pk[e][0] = pack_bf16(acc[t + e][0] + bf_hi(kv[1]) * dcoef, acc[t + e][1] + bf_lo(kv[1]) * dcoef);
      pk[e][1] = pack_bf16(acc[t + e][2] + bf_hi(kv[0]) * dcoef, acc[t + e][3] + bf_lo(kv[0]) * dcoef);
    }
    const auto r0 = __builtin_amdgcn_permlane16_swap(pk[0][0], pk[1][0], false, false);
    const auto r1 = __builtin_amdgcn_permlane16_swap(pk[0][1], pk[1][1], false, false);
    const u32x4 o = {r0[0], r1[0], r0[1], r1[1]};
    const unsigned off = 2 * (256 * (t + (g & 1)) + 16 * m + 8 * (g >> 1));
    __builtin_amdgcn_raw_buffer_store_b128(o, ry, off, 0, SC1);
  }
  STAMP(6);
  WAIT_VM(0);
  STAMP(7);
  if (sy_arrive(lds, SY_CONV, lane) == (unsigned)(nconv - 1)) {
    const unsigned tag = get_tag(lds);
    if (lane == 0) flag_store(a.ws, WS_FLAGY + 4 * blockIdx.x, tag);
  }
}

constexpr int HD = G_HDEPTH;
__device__ constexpr int g_younger(int b) {
  int count = 0, last = 0;
  for (int k = 0; k < HD; ++k) { count += 4; if (k == b) last = count; }
  count += 1;
  for (int s = 0; s < b; ++s) {
    if (s + HD <= 11) { count += 4; if (s + HD == b) last = count; }
    if (s == 9) count += 5;
    if (s >= 1 && s <= 7) { count += 5; if (s + 4 == b + 1) last = count; }
  }
  return count - last;
}
static_assert(G_HDEPTH != 3 || (g_younger(0) == 9 && g_younger(1) == 9 && g_younger(2) == 14 && g_younger(3) == 18 &&
              g_younger(8) == 18 && g_younger(9) == 13 && g_younger(10) == 9 && g_younger(11) == 5), "wait table");
__global__ __launch_bounds__(512, 2) void fused_kernel(Args a) {
  extern __shared__ __attribute__((aligned(16))) char smem[];
  lptr lds = (lptr)smem;
  const int tid = threadIdx.x, lane = tid & 63;
  const int wave = __builtin_amdgcn_readfirstlane(tid >> 6);
  const int bid = blockIdx.x;
  const int xcd = bid & 7, j = bid >> 3;
  const int mi = 8 * (xcd & 1) + (j & 7), fi = 4 * (xcd >> 1) + (j >> 3);
  const int si = 8 * (xcd & 3) + (j & 7), di = 4 * (xcd >> 2) + (j >> 3);
  const int c0 = (5 * bid) >> 1, nconv = 2 + (bid & 1);
  const unsigned lds0 = lds_addr(lds);
  STAMP_INIT();
  STAMP(0);

  if (wave == 7) {
    if (lane < 32) *sy(lds, lane) = 0u;
    WAIT_LGKM0();
  }
  BARRIER();

  if (wave == 7) {
    const unsigned epoch = __hip_atomic_load((GAS unsigned*)(a.ws + WS_EPOCH), __ATOMIC_RELAXED, __HIP_MEMORY_SCOPE_AGENT);
    {
      const int r0 = 128 * mi + 8 * fi, gr = lane >> 4, gq = lane & 15;
      u32x4 gx[2][5], gw[5];
#pragma unroll
      for (int i = 0; i < 5; ++i) {
        gw[i] = ((const u32x4*)a.gw)[gq + 16 * i];
        gx[0][i] = ((const u32x4*)(a.x + (size_t)(r0 + gr) * D))[gq + 16 * i];
        gx[1][i] = ((const u32x4*)(a.x + (size_t)(r0 + 4 + gr) * D))[gq + 16 * i];
      }
      FENCE();
      sy_set(lds, SY_TAG, epoch + 1u, lane);
      sy_set(lds, SY_TAGOK, 1u, lane);
      BARRIER();
#pragma unroll
      for (int pass = 0; pass < 2; ++pass) {
        float sum = 0.f;
#pragma unroll
        for (int i = 0; i < 5; ++i)
#pragma unroll
          for (int e = 0; e < 4; ++e) sum = dot2(gx[pass][i][e], gw[i][e], sum);
#pragma unroll
        for (int m = 8; m >= 1; m >>= 1) sum += __shfl_xor(sum, m);
        if (gq == 0)
          __hip_atomic_store((GAS unsigned*)(a.ws + WS_GATE) + r0 + 4 * pass + gr, __builtin_bit_cast(unsigned, sigmoidf_(sum)),
                             __ATOMIC_RELAXED, __HIP_MEMORY_SCOPE_AGENT);
      }
    }
    const unsigned tag = epoch + 1u;
    STAMP(1);
#pragma unroll 1
    for (int s = 0; s < 9; ++s) BARRIER();
    WAIT_VM(0);
    sy_set(lds, SY_GATE, 1u, lane);
    STAMP(2);
    {
      flag_wait(a.ws, WS_FLAGKV + 4 * (32 * (bid >> 5) + (lane & 31)), true, tag);
      STAMP(8);
      u32x4 uv[3][4];
#pragma unroll
      for (int cw = 0; cw < 3; ++cw) {
        const __amdgpu_buffer_rsrc_t ru = make_rsrc(a.ws + WS_KVR + (size_t)(c0 + (cw < nconv ? cw : 0)) * S * 2, S * 2);
#pragma unroll
        for (int i = 0; i < 4; ++i) uv[cw][i] = ld128(ru, (unsigned)(i * 1024 + lane * 16), 0, SC1);
      }
      FENCE();
#pragma unroll
      for (int cw = 0; cw < 3; ++cw) {
        lptr ur = lds + L_AUX + cw * UR_BYTES;
#pragma unroll
        for (int i = 0; i < 4; ++i) *(LAS u32x4*)(ur + i * 1024 + lane * 16) = uv[cw][i];
        if (lane < 32) *(LAS u32x4*)(ur + 4096 + lane * 16) = (u32x4){0u, 0u, 0u, 0u};
      }
      WAIT_LGKM0();
      sy_set(lds, SY_AREADY, 1u, lane);
    }
    STAMP(9);
    FENCE();
    {
      const char* base = (const char*)a.wd + 640 * (xcd >> 2);
#pragma unroll
      for (int t = 0; t < 4; ++t) {
        const int id = min(64 * t + lane, 239), r = id / 5, c = id - 5 * r;
        touch(base + (size_t)(48 * j + r) * (D * 2) + 128 * c, lds0 + L_DUMMY);
      }
    }
    sy_wait(lds, SY_WPRE, 4u);
    flag_wait<HPOLL_SLEEP>(a.ws, WS_FLAGH + 4 * (16 * (si >> 1) + (lane & 15)), true, tag);
    sy_set(lds, SY_HREADY, 1u, lane);
    STAMP(10);
    sy_wait(lds, SY_CONV, nconv);
    sy_wait(lds, SY_GSTEP, 1u);
    flag_wait<YPOLL_SLEEP>(a.ws, WS_FLAGY + 4 * (32 * di + (lane & 31)), true, tag);
    STAMP(11);
    {
      const __amdgpu_buffer_rsrc_t ry = make_rsrc(a.ws + WS_Y2T, S * D * 2);
      u32x4 yv[10];
#pragma unroll
      for (int i = 0; i < 10; ++i) {
        const int id = lane + 64 * i;
        yv[i] = ld128(ry, (unsigned)((80 * di + (id >> 3)) * (S * 2) + 64 * si * 2 + (id & 7) * 16), 0, SC1);
      }
#pragma unroll
      for (int i = 0; i < 10; ++i) {
        const int id = lane + 64 * i, w = (id & 7) >> 1;
        const int so = w == 0 ? ystrip_off(0) : w == 1 ? ystrip_off(1) : w == 2 ? ystrip_off(2) : ystrip_off(3);
        *(LAS u32x4*)(lds + so + (id >> 3) * 32 + (id & 1) * 16) = yv[i];
      }
      WAIT_LGKM0();
      sy_set(lds, SY_YREADY, 1u, lane);
    }
    STAMP(12);
    if (bid == 0) {
#pragma unroll
      for (int i = 0; i < 4; ++i) flag_wait(a.ws, WS_FLAGKV + 4 * (lane + 64 * i), true, tag);
      if (lane == 0) flag_store(a.ws, WS_EPOCH, tag);
    }
    WAIT_VM(0);
    STAMP_DUMP();
    return;
  }

  if (wave >= 4) {
    const int cw = wave - 4;
    const bool active = cw < nconv;
    const int ch = c0 + (active ? cw : 0);
    Filter flt;
    const int rot1 = ROT1 ? (3 * (j >> 3) + (j & 7)) % 10 : 0;
    if (cw == 0) loader<6>(a, lds, 0, 0, mi, fi, rot1, flt, ch, lane);
    else loader<5>(a, lds, cw, 1 + 5 * cw, mi, fi, rot1, flt, ch, lane);
    STAMP(3);
    if (active) {
      conv_wave(a, lds, lds + (cw == 2 ? 2 * STAGE : STAGE + cw * KC_BYTES), lds + L_AUX + cw * UR_BYTES, flt, ch, nconv, lane);
    }
    STAMP_DUMP();
    return;
  }

  f32x4 ag[4][3], au[4][3];
  {
    const int tsi = bid >> 3, tdi = bid & 7;
    const int task = 40 * wave + (lane < 40 ? lane : 0);
    KvTask t0;
    kv_load(a, t0, task, tsi, tdi);
    FENCE();
#pragma unroll
    for (int t = 0; t < 4; ++t)
#pragma unroll
      for (int c = 0; c < 3; ++c) {
        ag[t][c] = (f32x4){0.f, 0.f, 0.f, 0.f}; au[t][c] = (f32x4){0.f, 0.f, 0.f, 0.f};
        asm volatile("" : "+v"(ag[t][c]), "+v"(au[t][c]));
      }
    FENCE();
    STAMP(1);
    if (lane < 40) kv_compute(t0, lds, task, tsi);
    sy_arrive(lds, SY_KV, lane);
    sy_wait(lds, SY_KV, 4u);
    STAMP(2);
    const __amdgpu_buffer_rsrc_t rkv = make_rsrc(a.ws + WS_KVR, S * D * 2);
#pragma unroll
    for (int i = 0; i < 3; ++i) {
      const int id = tid + 256 * i;
      if (id < 640) {
        const int dl = id >> 3, jj = id & 7;
        const u32x4 val = *(LAS u32x4*)(lds + L_T + dl * 128 + ((jj ^ ((dl >> 3) & 7)) << 4));
        __builtin_amdgcn_raw_buffer_store_b128(val, rkv, (unsigned)(((80 * tdi + dl) * S + (1984 - 64 * tsi) + 8 * jj) * 2), 0, SC1);
      }
    }
  }
  const int n = lane & 15, g = lane >> 4;
  {
    const int ws_ = wave & 1, wf = wave >> 1;
    const int q = n >> 2, p = lane & 3;
    const int xb0 = XB + (64 * ws_ + n) * 128 + ((g ^ (n >> 1)) << 4);
    int wb_[3];
#pragma unroll
    for (int c = 0; c < 3; ++c) wb_[c] = GB + (8 * g + q) * 192 + 8 * p + 32 * ((3 * wf + c) ^ (g & 1));
    auto read_piece = [&](Frags& f, int i, lptr b, int kk) {
      if (i < 4) {
        f.x[i] = lds_frag(b + (xb0 ^ (kk << 6)) + i * 2048);
      } else {
        const int c = (i - 4) >> 2, u = ((i - 4) >> 1) & 1, t = (i - 4) & 1;
        const s16x4 v = lds_tr(b + wb_[c] + kk * (32 * 192) + u * (UB - GB) + t * (4 * 192));
        if (u == 0) f.g[c][t] = v; else f.u[c][t] = v;
      }
    };
    auto mma_piece = [&](const Frags& f, int i) {
      const int c = i >> 3, t = (i >> 1) & 3;
      if ((i & 1) == 0) ag[t][c] = MFMA16(join(f.g[c][0], f.g[c][1]), f.x[t], ag[t][c]);
      else au[t][c] = MFMA16(join(f.u[c][0], f.u[c][1]), f.x[t], au[t][c]);
    };
    auto half = [&](const Frags& cur, Frags& nxt, lptr nb, int nkk, bool rd, auto&& hook) {
#pragma unroll
      for (int i = 0; i < 24; ++i) {
        mma_piece(cur, i); FENCE();
        if (rd && i < 16) { read_piece(nxt, i, nb, nkk); FENCE(); }
        hook(i);
      }
    };
    auto nohook = [](int) {};
    const unsigned cons_addr = lds0 + L_SYNC + 4 * (SY_CONSUMED + wave);
    BARRIER();
    STAMP(3);
    STAMPC(28);
    __builtin_amdgcn_s_setprio(M_PRIO);
    Frags f0, f1;
#pragma unroll
    for (int i = 0; i < 16; ++i) read_piece(f0, i, lds, 0);
    FENCE();
    STAMPC(27);
    int buf = 0;
#pragma unroll 1
    for (int s = 0; s < 9; ++s) {
      const int nbuf = (buf == 2) ? 0 : buf + 1;
      FENCE();
      half(f0, f1, lds + buf * STAGE, 1, true, nohook);
      if (s == SIDE_PUB_ITER) {
        WAIT_VM(0);
        if (sy_arrive(lds, SY_SIDE, lane) == 3u) {
          const unsigned tag = get_tag(lds);
          if (lane == 0) flag_store(a.ws, WS_FLAGKV + 4 * (32 * (bid & 7) + (bid >> 3)), tag);
        }
      }
      WAIT_LGKM0();
      BARRIER();
      FENCE();
      half(f1, f0, lds + nbuf * STAGE, 0, true, nohook);
      buf = nbuf;
    }
    half(f0, f1, lds + buf * STAGE, 1, true, nohook);
    half(f1, f0, lds, 0, false, nohook);
    STAMP(4);
    STAMPC(29);

    const __amdgpu_buffer_rsrc_t rhs = make_rsrc(a.ws + WS_HT, S * F * 2);
#pragma unroll
    for (int t = 0; t < 4; ++t) {
      u32x2 pk[3];
#pragma unroll
      for (int c = 0; c < 3; ++c) {
        float h[4];
#pragma unroll
        for (int r = 0; r < 4; ++r) h[r] = sigmoidf_(ag[t][c][r]) * au[t][c][r];
        pk[c] = (u32x2){pack_bf16(h[0], h[1]), pack_bf16(h[2], h[3])};
      }
      const unsigned base = (unsigned)(((8 * mi + 4 * ws_ + t) * 48 + 3 * fi) * 1024 + lane * 16);
      if (wf == 0) {
        __builtin_amdgcn_raw_buffer_store_b128((u32x4){pk[0][0], pk[0][1], pk[1][0], pk[1][1]}, rhs, base, 0, SC1);
        __builtin_amdgcn_raw_buffer_store_b64(pk[2], rhs, base + 1024, 0, SC1);
      } else {
        __builtin_amdgcn_raw_buffer_store_b64(pk[0], rhs, base + 1024 + 8, 0, SC1);
        __builtin_amdgcn_raw_buffer_store_b128((u32x4){pk[1][0], pk[1][1], pk[2][0], pk[2][1]}, rhs, base + 2048, 0, SC1);
      }
    }
  }
  STAMP(5);

  const __amdgpu_buffer_rsrc_t rh = make_rsrc(a.ws + WS_HT, S * F * 2);
  const __amdgpu_buffer_rsrc_t rw = make_rsrc(a.wd, F * D * 2);
  const unsigned wr0 = wave == 0 ? wring_off(0) : wave == 1 ? wring_off(1) : wave == 2 ? wring_off(2) : wring_off(3);
  const int rot = 3 * (j >> 3);
  auto kstep = [&](int b) { const int t = b + rot; return 12 * wave + (t >= 12 ? t - 12 : t); };
  unsigned vwd[5];
#pragma unroll
  for (int i = 0; i < 5; ++i) {
    const int slot = 64 * i + lane, row = slot / 10, ch = slot - 10 * row;
    vwd[i] = (unsigned)(row * (D * 2) + 160 * di + ch * 16 - 1024 * (i & 3));
  }
  auto load_w = [&](int b) {
    const unsigned dst = lds0 + wr0 + (b & 3) * WSLOT, so = (unsigned)kstep(b) * (32 * D * 2);
    dma16xN<4>(rw, dst, vwd, so);
    dma16xN<1>(rw, dst + 4096, vwd + 4, so);
  };
  auto load_w1 = [&](int b, int i) {
    dma16(rw, lds0 + wr0 + (b & 3) * WSLOT + i * 1024, vwd[i] + 1024 * (i & 3), (unsigned)kstep(b) * (32 * D * 2));
  };
  struct HS4 { u32x4 f[4]; };
  HS4 H[HD + 1];
  const unsigned voff = lane * 16;
  auto load_h = [&](HS4& h, int b) {
    const unsigned so = (unsigned)(4 * si * 48 + kstep(b)) * 1024;
#define HPOL " sc1"
    asm volatile("buffer_load_dwordx4 %0, %4, %5, %6 offen" HPOL "\n\tbuffer_load_dwordx4 %1, %4, %5, %7 offen" HPOL "\n\t"
                 "buffer_load_dwordx4 %2, %4, %5, %8 offen" HPOL "\n\tbuffer_load_dwordx4 %3, %4, %5, %9 offen" HPOL
                 : "=&v"(h.f[0]), "=&v"(h.f[1]), "=&v"(h.f[2]), "=&v"(h.f[3])
                 : "v"(voff), "s"(rh), "s"(so), "s"(so + 48 * 1024), "s"(so + 96 * 1024), "s"(so + 144 * 1024) : "memory");
  };
  auto load_h1 = [&](HS4& h, int b, int i) {
    const unsigned so = (unsigned)((4 * si + i) * 48 + kstep(b)) * 1024;
    asm volatile("buffer_load_dwordx4 %0, %1, %2, %3 offen" HPOL : "=&v"(h.f[i]) : "v"(voff), "s"(rh), "s"(so) : "memory");
  };
  const int srow = 64 * si + 16 * wave + n;
  u32x2 qv[5];
  FENCE();
  __builtin_amdgcn_s_setprio(3);
  sy_arrive(lds, SY_EPI, lane);
  STAMP(22);
  WAIT_VM(0);
  if (sy_arrive(lds, SY_HDONE, lane) == 3u) {
    sy_wait(lds, SY_GATE, 1u);
    const unsigned tag = get_tag(lds);
    if (lane == 0) flag_store(a.ws, WS_FLAGH + 4 * (16 * mi + fi), tag);
  }
  STAMP(23);
  FENCE();
  sy_wait(lds, SY_EPI, 4u);
  load_w(0); load_w(1); load_w(2); load_w(3);
  f32x4 acc[4][5];
#pragma unroll
  for (int t = 0; t < 4; ++t)
#pragma unroll
    for (int c = 0; c < 5; ++c) { acc[t][c] = (f32x4){0.f, 0.f, 0.f, 0.f}; asm volatile("" : "+v"(acc[t][c])); }
  struct WF { s16x4 f[5][2]; };
  WF w0, w1;
  const lptr wrd = lds + wr0 + (4 * g + (n >> 2)) * 160 + 8 * (lane & 3);
  auto read_w = [&](WF& w, int p, int b) {
    w.f[p >> 1][p & 1] = lds_tr(wrd + (b & 3) * WSLOT + (p & 1) * (16 * 160) + (p >> 1) * 32);
  };
  STAMP(24);
  WAIT_VM(15);
#pragma unroll
  for (int p = 0; p < 10; ++p) read_w(w0, p, 0);
  WAIT_LGKM0();
  load_w(4);
  sy_arrive(lds, SY_WPRE, lane);
  __builtin_amdgcn_s_setprio(G_PRIO);
  STAMP(6);
  sy_wait(lds, SY_HREADY, 1u);
  STAMP(7);
#pragma unroll
  for (int b = 0; b < HD; ++b) load_h(H[b], b);
  const float gate = __builtin_bit_cast(float, __hip_atomic_load((GAS unsigned*)(a.ws + WS_GATE) + srow, __ATOMIC_RELAXED, __HIP_MEMORY_SCOPE_AGENT));
  FENCE();
  auto gstep = [&](const WF& cur, WF& nxt, HS4& h, HS4& hn, int b) {
    asm volatile("s_waitcnt vmcnt(%4) lgkmcnt(0)" : "+v"(h.f[0]), "+v"(h.f[1]), "+v"(h.f[2]), "+v"(h.f[3]) : "n"(g_younger(b)) : "memory");
    FENCE();
#pragma unroll
    for (int i = 0; i < 20; ++i) {
      acc[i & 3][i >> 2] = MFMA16(join(cur.f[i >> 2][0], cur.f[i >> 2][1]), __builtin_bit_cast(bf16x8, h.f[i & 3]), acc[i & 3][i >> 2]);
      FENCE();
      if (i < 8 && (i & 1) == 0 && b + HD < 12) { load_h1(hn, b + HD, i >> 1); FENCE(); }
      if (i == 7 && b == 9) {
#pragma unroll
        for (int c = 0; c < 5; ++c) qv[c] = *(const u32x2*)(a.x + (size_t)srow * D + 80 * di + 16 * c + 4 * g);
        asm volatile("" ::: "memory");
        FENCE();
      }
      if (i < 10 && b + 1 < 12) { read_w(nxt, i, b + 1); FENCE(); }
      if (i >= 10 && (i & 1) == 0 && b >= 1 && b + 4 < 12) { load_w1(b + 4, (i - 10) >> 1); FENCE(); }
      if (b == YPOLL_STEP && i == 17 && wave == 0) { sy_post(lds0 + L_SYNC + 4 * SY_GSTEP, 1u); FENCE(); }
      if (b == YPOLL_STEP && i == 19 && wave == 0) { wake(); FENCE(); }
    }
  };
#pragma unroll
  for (int b = 0; b < 12; b += 2) {
    gstep(w0, w1, H[b % (HD + 1)], H[(b + HD) % (HD + 1)], b);
    if (b == 0) STAMPI(14);
    gstep(w1, w0, H[(b + 1) % (HD + 1)], H[(b + 1 + HD) % (HD + 1)], b + 1);
    STAMPI(15 + (b >> 1));
  }
  STAMP(8);

  f32x4 bsum[5];
  {
#pragma unroll
    for (int t = 0; t < 4; ++t) {
      if (t != wave) {
#pragma unroll
        for (int c = 0; c < 5; ++c) *(LAS f32x4*)(lds + wr0 + (((t - wave - 1) & 3) * 5 + c) * 1024 + lane * 16) = acc[t][c];
      } else {
#pragma unroll
        for (int c = 0; c < 5; ++c) bsum[c] = acc[t][c];
      }
    }
    sy_arrive(lds, SY_RED, lane);
    sy_wait(lds, SY_RED, 4u);
#pragma unroll
    for (int w = 0; w < 4; ++w) {
      if (w != wave) {
#pragma unroll
        for (int c = 0; c < 5; ++c) bsum[c] += *(LAS f32x4*)(lds + wring_off(w) + (((wave - w - 1) & 3) * 5 + c) * 1024 + lane * 16);
      }
    }
  }
  STAMP(9);
  sy_wait(lds, SY_YREADY, 1u);
  STAMP(10);
  {
    const int q = n >> 2, p = lane & 3;
    const __amdgpu_buffer_rsrc_t ro = make_rsrc(a.out, S * D * 2);
    const lptr ystr = lds + (wave == 0 ? ystrip_off(0) : wave == 1 ? ystrip_off(1) : wave == 2 ? ystrip_off(2) : ystrip_off(3));
#pragma unroll
    for (int c = 0; c < 5; ++c) {
      const s16x4 yv = lds_tr(ystr + (16 * c + 4 * g + q) * 32 + 8 * p);
      const u32x2 y = __builtin_bit_cast(u32x2, yv);
      const float o0 = gate * (bf_lo(qv[c][0]) * bf_lo(y[0])) + (1.f - gate) * bsum[c][0];
      const float o1 = gate * (bf_hi(qv[c][0]) * bf_hi(y[0])) + (1.f - gate) * bsum[c][1];
      const float o2 = gate * (bf_lo(qv[c][1]) * bf_lo(y[1])) + (1.f - gate) * bsum[c][2];
      const float o3 = gate * (bf_hi(qv[c][1]) * bf_hi(y[1])) + (1.f - gate) * bsum[c][3];
      __builtin_amdgcn_raw_buffer_store_b64((u32x2){pack_bf16(o0, o1), pack_bf16(o2, o3)}, ro,
                                            (unsigned)((srow * D + 80 * di + 16 * c + 4 * g) * 2), 0, OUT_AUX);
    }
  }
  STAMP(11);
}

}

extern "C" void kernel_launch(void* const* d_in, const int* in_sizes, int n_in, void* d_out, int out_size, void* d_ws,
                              size_t ws_size, hipStream_t stream) {
  (void)in_sizes; (void)n_in; (void)out_size; (void)ws_size;
  Args a;
  a.x = (const u16*)d_in[0];
  a.k = (const u16*)d_in[1];
  a.v = (const u16*)d_in[2];
  a.shk = (const u16*)d_in[3];
  a.ssmk = (const u16*)d_in[4];
  a.ssmd = (const u16*)d_in[5];
  a.wg = (const u16*)d_in[7];
  a.wu = (const u16*)d_in[8];
  a.wd = (const u16*)d_in[9];
  a.gw = (const u16*)d_in[10];
  a.ws = (char*)d_ws;
  a.out = (u16*)d_out;
  (void)hipFuncSetAttribute((const void*)fused_kernel, hipFuncAttributeMaxDynamicSharedMemorySize, LDS_BYTES);
  fused_kernel<<<256, 512, LDS_BYTES, stream>>>(a);
}
```

```cpp
#include <hip/hip_runtime.h>
#include <hip/hip_bf16.h>

namespace {

typedef unsigned short u16;
typedef __bf16 bf16x8 __attribute__((ext_vector_type(8)));
typedef __bf16 bf16x2 __attribute__((ext_vector_type(2)));
typedef short s16x4 __attribute__((ext_vector_type(4)));
typedef float f32x2 __attribute__((ext_vector_type(2)));
typedef float f32x4 __attribute__((ext_vector_type(4)));
typedef unsigned u32x2 __attribute__((ext_vector_type(2)));
typedef unsigned u32x4 __attribute__((ext_vector_type(4)));
#define LAS __attribute__((address_space(3)))
#define GAS __attribute__((address_space(1)))
typedef LAS char* lptr;

constexpr int S = 2048, D = 640, F = 1536;
constexpr int SC1 = 16;

constexpr size_t WS_HT = 0;
constexpr size_t WS_KVR = WS_HT + (size_t)S * F * 2;
constexpr size_t WS_Y2T = WS_KVR + (size_t)S * D * 2;
constexpr size_t WS_GATE = WS_Y2T + (size_t)S * D * 2;
constexpr size_t WS_FLAGKV = WS_GATE + (size_t)S * 4;
constexpr size_t WS_FLAGH = WS_FLAGKV + 1024;
constexpr size_t WS_FLAGY = WS_FLAGH + 1024;
constexpr size_t WS_EPOCH = WS_FLAGY + 1024;
constexpr size_t WS_END = WS_EPOCH + 256;

constexpr int STAGE = 40960, XB = 0, GB = 16384, UB = 28672;
constexpr int L_AUX = 3 * STAGE;
constexpr int L_T = L_AUX, UR_BYTES = 4608;
constexpr int L_FREE = L_AUX + 3 * UR_BYTES;
constexpr int L_SYNC = L_FREE + 20480 + 2 * 2560;
constexpr int L_DUMMY = L_SYNC + 128;
constexpr int L_END = L_DUMMY + 256;
constexpr int KC_COPY = 4160, KC_BYTES = 4 * KC_COPY;
constexpr int WSLOT = 5120;
__device__ constexpr int wring_off(int w) { return w == 0 ? 0 : w == 1 ? 20480 : w == 2 ? 2 * STAGE + KC_BYTES : L_FREE; }
__device__ constexpr int ystrip_off(int w) { return w < 2 ? STAGE + 2 * KC_BYTES + w * 2560 : L_FREE + 20480 + (w - 2) * 2560; }
static_assert(2 * STAGE + KC_BYTES + 20480 <= 3 * STAGE && STAGE + 2 * KC_BYTES + 2 * 2560 <= 2 * STAGE, "second-phase LDS map");
enum { SY_LANDED = 0, SY_CONSUMED = 4, SY_KV = 8, SY_SIDE = 9, SY_HDONE = 10, SY_RED = 11, SY_CONV = 12, SY_AREADY = 13,
       SY_HREADY = 14, SY_YREADY = 15, SY_TAG = 16, SY_TAGOK = 17, SY_GSTEP = 18, SY_GATE = 19, SY_EPI = 20, SY_WPRE = 21 };

#define STAMP(i) do {} while (0)
#define STAMPC(i) do {} while (0)
#define STAMPI(i) do {} while (0)
#define STAMP_INIT() do {} while (0)
#define STAMP_DUMP() do {} while (0)
constexpr int LDS_BYTES = L_END;

#define PROBE_AT 0
#define PROBE_US 0
#define PROBE(k) do { if (PROBE_AT == (k)) { while (__builtin_amdgcn_s_memrealtime() - probe_t0 < 100ull * PROBE_US) __builtin_amdgcn_s_sleep(1); } } while (0)
#define MFMA16(a, b, c) __builtin_amdgcn_mfma_f32_16x16x32_bf16((a), (b), (c), 0, 0, 0)
#define WAIT_VM(n) asm volatile("s_waitcnt vmcnt(" #n ")" ::: "memory")
#define WAIT_LGKM0() asm volatile("s_waitcnt lgkmcnt(0)" ::: "memory")
#define BARRIER() do { asm volatile("" ::: "memory"); __builtin_amdgcn_s_barrier(); asm volatile("" ::: "memory"); } while (0)
#define FENCE() __builtin_amdgcn_sched_barrier(0)
constexpr unsigned SPIN_LDS = 1u << 22, SPIN_MEM = 1u << 16;

__device__ __forceinline__ unsigned pack_bf16(float lo, float hi) {
  f32x2 v = {lo, hi};
  return __builtin_bit_cast(unsigned, __builtin_convertvector(v, bf16x2));
}
__device__ __forceinline__ float bf_lo(unsigned w) { return __builtin_bit_cast(float, w << 16); }
__device__ __forceinline__ float bf_hi(unsigned w) { return __builtin_bit_cast(float, w & 0xffff0000u); }
__device__ __forceinline__ float sigmoidf_(float x) {
  return __builtin_amdgcn_rcpf(1.0f + __builtin_amdgcn_exp2f(x * -1.4426950408889634f));
}
__device__ __forceinline__ __amdgpu_buffer_rsrc_t make_rsrc(const void* p, unsigned bytes) {
  return __builtin_amdgcn_make_buffer_rsrc(const_cast<void*>(p), 0, bytes, 0x00020000);
}
__device__ __forceinline__ unsigned lds_addr(lptr p) { return (unsigned)(size_t)p; }
__device__ __forceinline__ void dma16(__amdgpu_buffer_rsrc_t r, unsigned dst, unsigned voff, unsigned soff) {
  asm volatile("s_mov_b32 m0, %0\n\ts_nop 0\n\tbuffer_load_dwordx4 %1, %2, %3 offen lds"
               :: "s"(dst), "v"(voff), "s"(r), "s"(soff) : "memory");
}
template <int N>
__device__ __forceinline__ void dma16xN(__amdgpu_buffer_rsrc_t r, unsigned dst, const unsigned* v, unsigned soff) {
  static_assert(N >= 1 && N <= 4, "12-bit instruction offset");
  if constexpr (N == 1)
    asm volatile("s_mov_b32 m0, %0\n\ts_nop 0\n\tbuffer_load_dwordx4 %1, %2, %3 offen lds"
                 :: "s"(dst), "v"(v[0]), "s"(r), "s"(soff) : "memory");
  else if constexpr (N == 2)
    asm volatile("s_mov_b32 m0, %0\n\ts_nop 0\n\tbuffer_load_dwordx4 %1, %3, %4 offen lds\n\t"
                 "buffer_load_dwordx4 %2, %3, %4 offen offset:1024 lds"
                 :: "s"(dst), "v"(v[0]), "v"(v[1]), "s"(r), "s"(soff) : "memory");
  else if constexpr (N == 3)
    asm volatile("s_mov_b32 m0, %0\n\ts_nop 0\n\tbuffer_load_dwordx4 %1, %4, %5 offen lds\n\t"
                 "buffer_load_dwordx4 %2, %4, %5 offen offset:1024 lds\n\tbuffer_load_dwordx4 %3, %4, %5 offen offset:2048 lds"
                 :: "s"(dst), "v"(v[0]), "v"(v[1]), "v"(v[2]), "s"(r), "s"(soff) : "memory");
  else
    asm volatile("s_mov_b32 m0, %0\n\ts_nop 0\n\tbuffer_load_dwordx4 %1, %5, %6 offen lds\n\t"
                 "buffer_load_dwordx4 %2, %5, %6 offen offset:1024 lds\n\tbuffer_load_dwordx4 %3, %5, %6 offen offset:2048 lds\n\t"
                 "buffer_load_dwordx4 %4, %5, %6 offen offset:3072 lds"
                 :: "s"(dst), "v"(v[0]), "v"(v[1]), "v"(v[2]), "v"(v[3]), "s"(r), "s"(soff) : "memory");
}
__device__ __forceinline__ void touch(const void* p, unsigned dummy_lds) {
  asm volatile("s_mov_b32 m0, %0\n\ts_nop 0\n\tglobal_load_lds_dword %1, off" :: "s"(dummy_lds), "v"(p) : "memory");
}
__device__ __forceinline__ s16x4 lds_tr(lptr p) { return __builtin_amdgcn_ds_read_tr16_b64_v4i16((LAS s16x4*)p); }
__device__ __forceinline__ bf16x8 join(s16x4 a, s16x4 b) {
  return __builtin_bit_cast(bf16x8, __builtin_shufflevector(a, b, 0, 1, 2, 3, 4, 5, 6, 7));
}
__device__ __forceinline__ bf16x8 lds_frag(lptr p) { return *(LAS bf16x8*)p; }
__device__ __forceinline__ u32x4 ld128(__amdgpu_buffer_rsrc_t r, unsigned voff, unsigned soff, int aux) {
  return aux ? __builtin_bit_cast(u32x4, __builtin_amdgcn_raw_buffer_load_b128(r, voff, soff, SC1))
             : __builtin_bit_cast(u32x4, __builtin_amdgcn_raw_buffer_load_b128(r, voff, soff, 0));
}

__device__ __forceinline__ LAS unsigned* sy(lptr lds, int i) { return (LAS unsigned*)(lds + L_SYNC) + i; }
__device__ __forceinline__ unsigned sy_get(lptr lds, int i) {
  return __builtin_amdgcn_readfirstlane(__hip_atomic_load(sy(lds, i), __ATOMIC_RELAXED, __HIP_MEMORY_SCOPE_WORKGROUP));
}
__device__ __forceinline__ void wake() { asm volatile("s_wakeup" ::: "memory"); }
__device__ __forceinline__ void sy_set(lptr lds, int i, unsigned v, int lane) {
  if (lane == 0) __hip_atomic_store(sy(lds, i), v, __ATOMIC_RELAXED, __HIP_MEMORY_SCOPE_WORKGROUP);
  asm volatile("s_waitcnt lgkmcnt(0)\n\ts_wakeup" ::: "memory");
}
#define TOUCH_H 0
#define LOADER_POST_FIRST 1
#define HPOLL_SLEEP 4
#define YPOLL_SLEEP 8
#define YPOLL_STEP 8
#ifndef M_PRIO
#define M_PRIO 2
#endif
#define G_PRIO 0
#define ROT1 1
#define DMA_SPACE 0
#define C_PRIO 2
#define G_HDEPTH 4
#define OUT_AUX 0
#define H_ACQUIRE 0
#define CODE_LINES 0
#define CODE_WAVE 7
#define LAND_RD 18
#define TOUCH_DELAY 8
#define TOUCH1 0
#define FILT_ITER 6
#define W7_DELAY 0
#define POLL_AFTER 8
#define LOADER_PRIO 0
#define SLEEP_LDS 4
__device__ __forceinline__ void sy_post(unsigned addr, unsigned v) {
  asm volatile("s_mov_b64 exec, 1\n\tds_write_b32 %0, %1\n\ts_mov_b64 exec, -1" :: "v"(addr), "v"(v) : "memory");
}
__device__ __forceinline__ void sy_wait(lptr lds, int i, unsigned v) {
#pragma unroll 1
  for (unsigned n = 0; n < SPIN_LDS; ++n) {
    if (sy_get(lds, i) >= v) break;
    __builtin_amdgcn_s_sleep(SLEEP_LDS);
  }
  asm volatile("" ::: "memory");
}
__device__ __forceinline__ unsigned sy_arrive(lptr lds, int i, int lane) {
  WAIT_LGKM0();
  unsigned old = 0;
  if (lane == 0) old = __hip_atomic_fetch_add(sy(lds, i), 1u, __ATOMIC_RELAXED, __HIP_MEMORY_SCOPE_WORKGROUP);
  old = __builtin_amdgcn_readfirstlane(old);
  wake();
  return old;
}
__device__ __forceinline__ unsigned min4(u32x4 v) { return min(min(v[0], v[1]), min(v[2], v[3])); }
__device__ __forceinline__ u32x4 sy_get4(lptr lds, int i) { return *(volatile LAS u32x4*)(lds + L_SYNC + 4 * i); }
__device__ __forceinline__ u32x2 sy_get2(lptr lds, int i) { return *(volatile LAS u32x2*)(lds + L_SYNC + 4 * i); }
__device__ __forceinline__ void sy_wait4(lptr lds, int i, unsigned v) {
#pragma unroll 1
  for (unsigned n = 0; n < SPIN_LDS; ++n) {
    if ((unsigned)__builtin_amdgcn_readfirstlane(min4(sy_get4(lds, i))) >= v) break;
    __builtin_amdgcn_s_sleep(SLEEP_LDS);
  }
  asm volatile("" ::: "memory");
}
#define LOADER_SLEEP 1
__device__ __forceinline__ void loader_wait(lptr lds, unsigned v) {
#pragma unroll 1
  for (unsigned n = 0; n < SPIN_LDS; ++n) {
    if ((unsigned)__builtin_amdgcn_readfirstlane(min4(sy_get4(lds, SY_CONSUMED))) >= v) break;
    if (LOADER_SLEEP) __builtin_amdgcn_s_sleep(LOADER_SLEEP);
  }
  asm volatile("" ::: "memory");
}
__device__ __forceinline__ void sy_wait4m(lptr lds, unsigned v) {
#pragma unroll 1
  for (unsigned n = 0; n < SPIN_LDS; ++n) {
    const u32x4 l = sy_get4(lds, SY_LANDED);
    if ((unsigned)__builtin_amdgcn_readfirstlane(min(min(l[0], l[1]), l[2])) >= v) break;
    __builtin_amdgcn_s_sleep(SLEEP_LDS);
  }
  asm volatile("" ::: "memory");
}
__device__ __forceinline__ unsigned get_tag(lptr lds) {
  sy_wait(lds, SY_TAGOK, 1u);
  return sy_get(lds, SY_TAG);
}
__device__ __forceinline__ void flag_store(char* ws, size_t off, unsigned tag) {
  __hip_atomic_store((GAS unsigned*)(ws + off), tag, __ATOMIC_RELAXED, __HIP_MEMORY_SCOPE_AGENT);
}
#define SIDE_PUB_ITER 3
#define POLL_SLEEP 32
template <int SLEEP = POLL_SLEEP>
__device__ __forceinline__ void flag_wait(char* ws, size_t off, bool on, unsigned tag) {
#pragma unroll 1
  for (unsigned n = 0; n < SPIN_MEM; ++n) {
    const unsigned v = __hip_atomic_load((GAS unsigned*)(ws + off), __ATOMIC_RELAXED, __HIP_MEMORY_SCOPE_AGENT);
    if (__all(!on || v == tag)) break;
    __builtin_amdgcn_s_sleep(SLEEP);
  }
  asm volatile("" ::: "memory");
}

struct Args {
  const u16* x; const u16* k; const u16* v; const u16* shk; const u16* ssmk; const u16* ssmd;
  const u16* wg; const u16* wu; const u16* wd; const u16* gw;
  char* ws; u16* out;
};

struct Frags { bf16x8 x[4]; s16x4 g[3][2]; s16x4 u[3][2]; };

__device__ constexpr int tri_start(int t) { return 8 * t - (t * (t - 1)) / 2; }
__device__ constexpr int tri_row(int p) {
  return p < 8 ? 0 : p < 15 ? 1 : p < 21 ? 2 : p < 26 ? 3 : p < 30 ? 4 : p < 33 ? 5 : p < 35 ? 6 : 7;
}

template <int N> __device__ __forceinline__ void wait_vm() { asm volatile("s_waitcnt vmcnt(%0)" ::"n"(N) : "memory"); }

struct Filter { unsigned d[5][6]; unsigned dbits; };
__device__ __forceinline__ void load_filter(const Args& a, Filter& f, int ch, int lane) {
  const __amdgpu_buffer_rsrc_t rk = make_rsrc(a.ssmk + (size_t)ch * S, S * 2);
#pragma unroll
  for (int it = 0; it < 5; ++it) {
    const unsigned off = (unsigned)(16 * (lane + 64 * it) - 32);
    const u32x4 v0 = __builtin_bit_cast(u32x4, __builtin_amdgcn_raw_buffer_load_b128(rk, off, 0, 0));
    const u32x2 v1 = __builtin_bit_cast(u32x2, __builtin_amdgcn_raw_buffer_load_b64(rk, off + 16, 0, 0));
    f.d[it][0] = v0[0]; f.d[it][1] = v0[1]; f.d[it][2] = v0[2]; f.d[it][3] = v0[3]; f.d[it][4] = v1[0]; f.d[it][5] = v1[1];
  }
  f.dbits = a.ssmd[ch];
}

template <int NX>
__device__ __forceinline__ void loader(const Args& a, lptr lds, int lw, int x0, int mi, int fi, int rot, Filter& flt, int ch, int lane) {
  const unsigned lds0 = lds_addr(lds);
  const __amdgpu_buffer_rsrc_t rx = make_rsrc(a.x, S * D * 2);
  const __amdgpu_buffer_rsrc_t rg = make_rsrc(a.wg, D * F * 2);
  const __amdgpu_buffer_rsrc_t ru = make_rsrc(a.wu, D * F * 2);
  unsigned vx[NX], vw[4];
#pragma unroll
  for (int i = 0; i < NX; ++i) {
    const int row = 8 * (x0 + i) + (lane >> 3), c = (lane & 7) ^ ((row >> 1) & 7);
    vx[i] = (unsigned)((128 * mi + row) * (D * 2) + c * 16 - 1024 * (i & 3));
  }
#pragma unroll
  for (int i = 0; i < 4; ++i) {
    const int slot = 64 * (4 * lw + i) + lane, k = slot / 12, chp = slot - 12 * k, c = chp ^ (2 * ((k >> 3) & 1));
    vw[i] = (unsigned)(k * (F * 2) + fi * 192 + c * 16 - 1024 * i);
  }
  const unsigned land_addr = lds0 + L_SYNC + 4 * (SY_LANDED + lw);
  auto slice = [&](int s) { const int k = s + rot; return k >= 10 ? k - 10 : k; };
  auto part_a = [&](int s) {
    const unsigned b = lds0 + (s % 3) * STAGE, so = (unsigned)slice(s) * (64 * F * 2);
    dma16xN<4>(rg, b + GB + 4 * lw * 1024, vw, so);
    if (DMA_SPACE) __builtin_amdgcn_s_sleep(DMA_SPACE);
    dma16xN<4>(ru, b + UB + 4 * lw * 1024, vw, so);
    if (DMA_SPACE) __builtin_amdgcn_s_sleep(DMA_SPACE);
  };
  auto part_b = [&](int s) {
    const unsigned b = lds0 + (s % 3) * STAGE, so = (unsigned)slice(s) * 128;
    dma16xN<4>(rx, b + XB + x0 * 1024, vx, so);
    if (DMA_SPACE) __builtin_amdgcn_s_sleep(DMA_SPACE);
    dma16xN<NX - 4>(rx, b + XB + (x0 + 4) * 1024, vx + 4, so);
  };
  constexpr int P = NX + 8;
  part_a(0); part_b(0);
  part_a(1); part_b(1);
  STAMP(1);
  wait_vm<P>();
  STAMP(2);
  BARRIER();
  part_a(2); part_b(2);
#pragma unroll
  for (int s = 0; s < 9; ++s) {
    if (s == 8) wait_vm<11>(); else wait_vm<P>();
    STAMPI(8 + 2 * s);
    BARRIER();
    STAMPI(9 + 2 * s);
    if (s < 7) { part_a(s + 3); part_b(s + 3); }
    if (s == 7) { FENCE(); load_filter(a, flt, ch, lane); FENCE(); asm volatile("" ::: "memory"); }
  }
}

struct KvTask { u32x4 kr[7], vr[4], wk[4]; };
__device__ __forceinline__ void kv_load(const Args& a, KvTask& t, int task, int tsi, int tdi) {
  const int dc = task % 10, sb = task / 10;
  const int s0 = 64 * tsi + 4 * sb, d0 = 80 * tdi + 8 * dc;
#pragma unroll
  for (int r = 0; r < 7; ++r) {
    const int s = s0 - 3 + r;
    t.kr[r] = *(const u32x4*)(a.k + (size_t)(s < 0 ? 0 : s) * D + d0);
  }
#pragma unroll
  for (int r = 0; r < 4; ++r) t.vr[r] = *(const u32x4*)(a.v + (size_t)(s0 + r) * D + d0);
#pragma unroll
  for (int i = 0; i < 4; ++i) t.wk[i] = *(const u32x4*)(a.shk + (size_t)d0 * 4 + i * 8);
}
__device__ __forceinline__ float dot2(unsigned x, unsigned y, float c) {
  return __builtin_amdgcn_fdot2_f32_bf16(__builtin_bit_cast(bf16x2, x), __builtin_bit_cast(bf16x2, y), c, false);
}
__device__ __forceinline__ float dot2z(unsigned x, unsigned y) {
  float r;
  asm("v_dot2_f32_bf16 %0, %1, %2, 0" : "=v"(r) : "v"(x), "v"(y));
  return r;
}
__device__ __forceinline__ void kv_compute(KvTask& t, lptr lds, int task, int tsi) {
  const int dc = task % 10, sb = task / 10;
  if (tsi == 0 && sb == 0) {
#pragma unroll
    for (int r = 0; r < 3; ++r) t.kr[r] = (u32x4){0u, 0u, 0u, 0u};
  }
  const int sg = 15 - sb;
  lptr dst = lds + L_T + 8 * dc * 128 + (((sg >> 1) ^ (dc & 7)) << 4) + ((sg & 1) << 3);
#pragma unroll
  for (int c = 0; c < 8; ++c) {
    const unsigned w01 = t.wk[c >> 1][2 * (c & 1)], w23 = t.wk[c >> 1][2 * (c & 1) + 1];
    const unsigned sel = (c & 1) ? 0x03020706u : 0x01000504u;
    unsigned pr[6];
#pragma unroll
    for (int i = 0; i < 6; ++i) pr[i] = __builtin_amdgcn_perm(t.kr[i + 1][c >> 1], t.kr[i][c >> 1], sel);
    float o[4];
#pragma unroll
    for (int r = 0; r < 4; ++r) {
      const float ks = dot2(pr[r + 2], w01, dot2z(pr[r], w23));
      o[r] = ks * ((c & 1) ? bf_hi(t.vr[r][c >> 1]) : bf_lo(t.vr[r][c >> 1]));
    }
    *(LAS u32x2*)(dst + c * 128) = (u32x2){pack_bf16(o[3], o[2]), pack_bf16(o[1], o[0])};
  }
}

__device__ __forceinline__ void conv_wave(const Args& a, lptr lds, lptr kc, lptr ur, const Filter& flt, int ch, int nconv, int lane) {
#pragma unroll
  for (int it = 0; it < 5; ++it) {
    const int c = lane + 64 * it;
    if (c < 260) {
      const unsigned a0 = flt.d[it][0], a1 = flt.d[it][1], a2 = flt.d[it][2], a3 = flt.d[it][3], a4 = flt.d[it][4], a5 = flt.d[it][5];
      const unsigned e0 = __builtin_amdgcn_alignbit(a1, a0, 16), e1 = __builtin_amdgcn_alignbit(a2, a1, 16),
                     e2 = __builtin_amdgcn_alignbit(a3, a2, 16), e3 = __builtin_amdgcn_alignbit(a4, a3, 16),
                     e4 = __builtin_amdgcn_alignbit(a5, a4, 16);
      lptr w = kc + 16 * c;
      *(LAS u32x4*)(w) = (u32x4){a0, a1, a2, a3};
      *(LAS u32x4*)(w + KC_COPY) = (u32x4){e0, e1, e2, e3};
      *(LAS u32x4*)(w + 2 * KC_COPY) = (u32x4){a1, a2, a3, a4};
      *(LAS u32x4*)(w + 3 * KC_COPY) = (u32x4){e1, e2, e3, e4};
    }
  }
  const float dcoef = bf_lo(flt.dbits);
  WAIT_LGKM0();
  sy_wait(lds, SY_AREADY, 1u);
  sy_wait(lds, SY_EPI, 4u);
  __builtin_amdgcn_s_setprio(C_PRIO);
  STAMP(4);
  STAMPC(28);

  const int m = lane & 15, g = lane >> 4;
  const int rho = (m + 1) & 3;
  lptr abase = kc + rho * KC_COPY + 2 * (8 * g + m - 31 - rho);
  lptr bbase = ur + 2 * (2000 - 16 * m + 8 * g) - 7 * 512;
  f32x4 acc[8];
#pragma unroll
  for (int t = 0; t < 8; ++t) acc[t] = (f32x4){0.f, 0.f, 0.f, 0.f};
  struct CF { s16x4 a[8][2]; bf16x8 b[8]; };
  auto cread = [&](CF& f, int p, int i) {
    if (p < 16) f.a[p >> 1][p & 1] = *(LAS s16x4*)(abase + 64 * i + 512 * (p >> 1) + 8 * (p & 1));
    else f.b[p - 16] = lds_frag(bbase + 64 * i + 512 * (7 - (p - 16)));
  };
  auto cmma = [&](const CF& f, int p) {
    const int t = tri_row(p), u = p - tri_start(t);
    acc[t + u] = MFMA16(join(f.a[t][0], f.a[t][1]), f.b[u], acc[t + u]);
  };
  auto residue = [&](const CF& cur, CF& nxt, int inext) {
#pragma unroll
    for (int p = 0; p < 36; ++p) {
      cmma(cur, p); FENCE();
      if (p < 24) { cread(nxt, p, inext); FENCE(); }
    }
  };
  CF c0, c1;
#pragma unroll
  for (int p = 0; p < 24; ++p) cread(c0, p, 1);
#pragma unroll 1
  for (int i = 1; i <= 8; i += 2) {
    FENCE();
    residue(c0, c1, i + 1);
    residue(c1, c0, (i + 2 <= 8) ? i + 2 : 8);
  }
  STAMP(5);
  STAMPC(29);
  const __amdgpu_buffer_rsrc_t ry = make_rsrc(a.ws + WS_Y2T + (size_t)ch * S * 2, S * 2);
#pragma unroll
  for (int t = 0; t < 8; t += 2) {
    unsigned pk[2][2];
#pragma unroll
    for (int e = 0; e < 2; ++e) {
      const int s = 256 * (t + e) + 16 * m + 4 * g;
      const u32x2 kv = *(LAS u32x2*)(ur + 2 * (2044 - s));
      pk[e][0] = pack_bf16(acc[t + e][0] + bf_hi(kv[1]) * dcoef, acc[t + e][1] + bf_lo(kv[1]) * dcoef);
      pk[e][1] = pack_bf16(acc[t + e][2] + bf_hi(kv[0]) * dcoef, acc[t + e][3] + bf_lo(kv[0]) * dcoef);
    }
    const auto r0 = __builtin_amdgcn_permlane16_swap(pk[0][0], pk[1][0], false, false);
    const auto r1 = __builtin_amdgcn_permlane16_swap(pk[0][1], pk[1][1], false, false);
    const u32x4 o = {r0[0], r1[0], r0[1], r1[1]};
    const unsigned off = 2 * (256 * (t + (g & 1)) + 16 * m + 8 * (g >> 1));
    __builtin_amdgcn_raw_buffer_store_b128(o, ry, off, 0, SC1);
  }
  STAMP(6);
  WAIT_VM(0);
  STAMP(7);
  if (sy_arrive(lds, SY_CONV, lane) == (unsigned)(nconv - 1)) {
    const unsigned tag = get_tag(lds);
    if (lane == 0) flag_store(a.ws, WS_FLAGY + 4 * blockIdx.x, tag);
  }
}

constexpr int HD = G_HDEPTH;
__device__ constexpr int g_younger(int b) {
  int count = 0, last = 0;
  for (int k = 0; k < HD; ++k) { count += 4; if (k == b) last = count; }
  count += 1;
  for (int s = 0; s < b; ++s) {
    if (s + HD <= 11) { count += 4; if (s + HD == b) last = count; }
    if (s == 9) count += 5;
    if (s >= 1 && s <= 7) { count += 5; if (s + 4 == b + 1) last = count; }
  }
  return count - last;
}
static_assert(G_HDEPTH != 3 || (g_younger(0) == 9 && g_younger(1) == 9 && g_younger(2) == 14 && g_younger(3) == 18 &&
              g_younger(8) == 18 && g_younger(9) == 13 && g_younger(10) == 9 && g_younger(11) == 5), "wait table");
__global__ __launch_bounds__(512, 2) void fused_kernel(Args a) {
  extern __shared__ __attribute__((aligned(16))) char smem[];
  lptr lds = (lptr)smem;
  const int tid = threadIdx.x, lane = tid & 63;
  const int wave = __builtin_amdgcn_readfirstlane(tid >> 6);
  const int bid = blockIdx.x;
  const int xcd = bid & 7, j = bid >> 3;
  const int mi = 8 * (xcd & 1) + (j & 7), fi = 4 * (xcd >> 1) + (j >> 3);
  const int si = 8 * (xcd & 3) + (j & 7), di = 4 * (xcd >> 2) + (j >> 3);
  const int c0 = (5 * bid) >> 1, nconv = 2 + (bid & 1);
  const unsigned lds0 = lds_addr(lds);
  STAMP_INIT();
  STAMP(0);

  if (wave == 7) {
    if (lane < 32) *sy(lds, lane) = 0u;
    WAIT_LGKM0();
  }
  BARRIER();

  if (wave == 7) {
    const unsigned epoch = __hip_atomic_load((GAS unsigned*)(a.ws + WS_EPOCH), __ATOMIC_RELAXED, __HIP_MEMORY_SCOPE_AGENT);
    {
      const int r0 = 128 * mi + 8 * fi, gr = lane >> 4, gq = lane & 15;
      u32x4 gx[2][5], gw[5];
#pragma unroll
      for (int i = 0; i < 5; ++i) {
        gw[i] = ((const u32x4*)a.gw)[gq + 16 * i];
        gx[0][i] = ((const u32x4*)(a.x + (size_t)(r0 + gr) * D))[gq + 16 * i];
        gx[1][i] = ((const u32x4*)(a.x + (size_t)(r0 + 4 + gr) * D))[gq + 16 * i];
      }
      FENCE();
      sy_set(lds, SY_TAG, epoch + 1u, lane);
      sy_set(lds, SY_TAGOK, 1u, lane);
      BARRIER();
#pragma unroll
      for (int pass = 0; pass < 2; ++pass) {
        float sum = 0.f;
#pragma unroll
        for (int i = 0; i < 5; ++i)
#pragma unroll
          for (int e = 0; e < 4; ++e) sum = dot2(gx[pass][i][e], gw[i][e], sum);
#pragma unroll
        for (int m = 8; m >= 1; m >>= 1) sum += __shfl_xor(sum, m);
        if (gq == 0)
          __hip_atomic_store((GAS unsigned*)(a.ws + WS_GATE) + r0 + 4 * pass + gr, __builtin_bit_cast(unsigned, sigmoidf_(sum)),
                             __ATOMIC_RELAXED, __HIP_MEMORY_SCOPE_AGENT);
      }
    }
    const unsigned tag = epoch + 1u;
    STAMP(1);
#pragma unroll 1
    for (int s = 0; s < 9; ++s) BARRIER();
    WAIT_VM(0);
    sy_set(lds, SY_GATE, 1u, lane);
    STAMP(2);
    {
      flag_wait(a.ws, WS_FLAGKV + 4 * (32 * (bid >> 5) + (lane & 31)), true, tag);
      STAMP(8);
      u32x4 uv[3][4];
#pragma unroll
      for (int cw = 0; cw < 3; ++cw) {
        const __amdgpu_buffer_rsrc_t ru = make_rsrc(a.ws + WS_KVR + (size_t)(c0 + (cw < nconv ? cw : 0)) * S * 2, S * 2);
#pragma unroll
        for (int i = 0; i < 4; ++i) uv[cw][i] = ld128(ru, (unsigned)(i * 1024 + lane * 16), 0, SC1);
      }
      FENCE();
#pragma unroll
      for (int cw = 0; cw < 3; ++cw) {
        lptr ur = lds + L_AUX + cw * UR_BYTES;
#pragma unroll
        for (int i = 0; i < 4; ++i) *(LAS u32x4*)(ur + i * 1024 + lane * 16) = uv[cw][i];
        if (lane < 32) *(LAS u32x4*)(ur + 4096 + lane * 16) = (u32x4){0u, 0u, 0u, 0u};
      }
      WAIT_LGKM0();
      sy_set(lds, SY_AREADY, 1u, lane);
    }
    STAMP(9);
    FENCE();
    {
      const char* base = (const char*)a.wd + 640 * (xcd >> 2);
#pragma unroll
      for (int t = 0; t < 4; ++t) {
        const int id = min(64 * t + lane, 239), r = id / 5, c = id - 5 * r;
        touch(base + (size_t)(48 * j + r) * (D * 2) + 128 * c, lds0 + L_DUMMY);
      }
    }
    sy_wait(lds, SY_WPRE, 4u);
    flag_wait<HPOLL_SLEEP>(a.ws, WS_FLAGH + 4 * (16 * (si >> 1) + (lane & 15)), true, tag);
    sy_set(lds, SY_HREADY, 1u, lane);
    STAMP(10);
    sy_wait(lds, SY_CONV, nconv);
    sy_wait(lds, SY_GSTEP, 1u);
    flag_wait<YPOLL_SLEEP>(a.ws, WS_FLAGY + 4 * (32 * di + (lane & 31)), true, tag);
    STAMP(11);
    {
      const __amdgpu_buffer_rsrc_t ry = make_rsrc(a.ws + WS_Y2T, S * D * 2);
      u32x4 yv[10];
#pragma unroll
      for (int i = 0; i < 10; ++i) {
        const int id = lane + 64 * i;
        yv[i] = ld128(ry, (unsigned)((80 * di + (id >> 3)) * (S * 2) + 64 * si * 2 + (id & 7) * 16), 0, SC1);
      }
#pragma unroll
      for (int i = 0; i < 10; ++i) {
        const int id = lane + 64 * i, w = (id & 7) >> 1;
        const int so = w == 0 ? ystrip_off(0) : w == 1 ? ystrip_off(1) : w == 2 ? ystrip_off(2) : ystrip_off(3);
        *(LAS u32x4*)(lds + so + (id >> 3) * 32 + (id & 1) * 16) = yv[i];
      }
      WAIT_LGKM0();
      sy_set(lds, SY_YREADY, 1u, lane);
    }
    STAMP(12);
    if (bid == 0) {
#pragma unroll
      for (int i = 0; i < 4; ++i) flag_wait(a.ws, WS_FLAGKV + 4 * (lane + 64 * i), true, tag);
      if (lane == 0) flag_store(a.ws, WS_EPOCH, tag);
    }
    WAIT_VM(0);
    STAMP_DUMP();
    return;
  }

  if (wave >= 4) {
    const int cw = wave - 4;
    const bool active = cw < nconv;
    const int ch = c0 + (active ? cw : 0);
    Filter flt;
    const int rot1 = ROT1 ? (3 * (j >> 3) + (j & 7)) % 10 : 0;
    if (cw == 0) loader<6>(a, lds, 0, 0, mi, fi, rot1, flt, ch, lane);
    else loader<5>(a, lds, cw, 1 + 5 * cw, mi, fi, rot1, flt, ch, lane);
    STAMP(3);
    if (active) {
      conv_wave(a, lds, lds + (cw == 2 ? 2 * STAGE : STAGE + cw * KC_BYTES), lds + L_AUX + cw * UR_BYTES, flt, ch, nconv, lane);
    }
    STAMP_DUMP();
    return;
  }

  const unsigned long long probe_t0 = PROBE_AT ? __builtin_amdgcn_s_memrealtime() : 0ull;
  (void)probe_t0;
  f32x4 ag[4][3], au[4][3];
  {
    const int tsi = bid >> 3, tdi = bid & 7;
    const int task = 40 * wave + (lane < 40 ? lane : 0);
    KvTask t0;
    kv_load(a, t0, task, tsi, tdi);
    FENCE();
#pragma unroll
    for (int t = 0; t < 4; ++t)
#pragma unroll
      for (int c = 0; c < 3; ++c) {
        ag[t][c] = (f32x4){0.f, 0.f, 0.f, 0.f}; au[t][c] = (f32x4){0.f, 0.f, 0.f, 0.f};
        asm volatile("" : "+v"(ag[t][c]), "+v"(au[t][c]));
      }
    FENCE();
    STAMP(1);
    if (lane < 40) kv_compute(t0, lds, task, tsi);
    sy_arrive(lds, SY_KV, lane);
    sy_wait(lds, SY_KV, 4u);
    STAMP(2);
    const __amdgpu_buffer_rsrc_t rkv = make_rsrc(a.ws + WS_KVR, S * D * 2);
#pragma unroll
    for (int i = 0; i < 3; ++i) {
      const int id = tid + 256 * i;
      if (id < 640) {
        const int dl = id >> 3, jj = id & 7;
        const u32x4 val = *(LAS u32x4*)(lds + L_T + dl * 128 + ((jj ^ ((dl >> 3) & 7)) << 4));
        __builtin_amdgcn_raw_buffer_store_b128(val, rkv, (unsigned)(((80 * tdi + dl) * S + (1984 - 64 * tsi) + 8 * jj) * 2), 0, SC1);
      }
    }
  }
  const int n = lane & 15, g = lane >> 4;
  {
    const int ws_ = wave & 1, wf = wave >> 1;
    const int q = n >> 2, p = lane & 3;
    const int xb0 = XB + (64 * ws_ + n) * 128 + ((g ^ (n >> 1)) << 4);
    int wb_[3];
#pragma unroll
    for (int c = 0; c < 3; ++c) wb_[c] = GB + (8 * g + q) * 192 + 8 * p + 32 * ((3 * wf + c) ^ (g & 1));
    auto read_piece = [&](Frags& f, int i, lptr b, int kk) {
      if (i < 4) {
        f.x[i] = lds_frag(b + (xb0 ^ (kk << 6)) + i * 2048);
      } else {
        const int c = (i - 4) >> 2, u = ((i - 4) >> 1) & 1, t = (i - 4) & 1;
        const s16x4 v = lds_tr(b + wb_[c] + kk * (32 * 192) + u * (UB - GB) + t * (4 * 192));
        if (u == 0) f.g[c][t] = v; else f.u[c][t] = v;
      }
    };
    auto mma_piece = [&](const Frags& f, int i) {
      const int c = i >> 3, t = (i >> 1) & 3;
      if ((i & 1) == 0) ag[t][c] = MFMA16(join(f.g[c][0], f.g[c][1]), f.x[t], ag[t][c]);
      else au[t][c] = MFMA16(join(f.u[c][0], f.u[c][1]), f.x[t], au[t][c]);
    };
    auto half = [&](const Frags& cur, Frags& nxt, lptr nb, int nkk, bool rd, auto&& hook) {
#pragma unroll
      for (int i = 0; i < 24; ++i) {
        mma_piece(cur, i); FENCE();
        if (rd && i < 16) { read_piece(nxt, i, nb, nkk); FENCE(); }
        hook(i);
      }
    };
    auto nohook = [](int) {};
    const unsigned cons_addr = lds0 + L_SYNC + 4 * (SY_CONSUMED + wave);
    BARRIER();
    PROBE(1);
    STAMP(3);
    STAMPC(28);
    __builtin_amdgcn_s_setprio(M_PRIO);
    Frags f0, f1;
#pragma unroll
    for (int i = 0; i < 16; ++i) read_piece(f0, i, lds, 0);
    FENCE();
    STAMPC(27);
    int buf = 0;
#pragma unroll 1
    for (int s = 0; s < 9; ++s) {
      const int nbuf = (buf == 2) ? 0 : buf + 1;
      FENCE();
      half(f0, f1, lds + buf * STAGE, 1, true, nohook);
      if (s == SIDE_PUB_ITER) {
        WAIT_VM(0);
        if (sy_arrive(lds, SY_SIDE, lane) == 3u) {
          const unsigned tag = get_tag(lds);
          if (lane == 0) flag_store(a.ws, WS_FLAGKV + 4 * (32 * (bid & 7) + (bid >> 3)), tag);
        }
      }
      WAIT_LGKM0();
      BARRIER();
      FENCE();
      half(f1, f0, lds + nbuf * STAGE, 0, true, nohook);
      buf = nbuf;
    }
    half(f0, f1, lds + buf * STAGE, 1, true, nohook);
    half(f1, f0, lds, 0, false, nohook);
    PROBE(2);
    STAMP(4);
    STAMPC(29);

    const __amdgpu_buffer_rsrc_t rhs = make_rsrc(a.ws + WS_HT, S * F * 2);
#pragma unroll
    for (int t = 0; t < 4; ++t) {
      u32x2 pk[3];
#pragma unroll
      for (int c = 0; c < 3; ++c) {
        float h[4];
#pragma unroll
        for (int r = 0; r < 4; ++r) h[r] = sigmoidf_(ag[t][c][r]) * au[t][c][r];
        pk[c] = (u32x2){pack_bf16(h[0], h[1]), pack_bf16(h[2], h[3])};
      }
      const unsigned base = (unsigned)(((8 * mi + 4 * ws_ + t) * 48 + 3 * fi) * 1024 + lane * 16);
      if (wf == 0) {
        __builtin_amdgcn_raw_buffer_store_b128((u32x4){pk[0][0], pk[0][1], pk[1][0], pk[1][1]}, rhs, base, 0, SC1);
        __builtin_amdgcn_raw_buffer_store_b64(pk[2], rhs, base + 1024, 0, SC1);
      } else {
        __builtin_amdgcn_raw_buffer_store_b64(pk[0], rhs, base + 1024 + 8, 0, SC1);
        __builtin_amdgcn_raw_buffer_store_b128((u32x4){pk[1][0], pk[1][1], pk[2][0], pk[2][1]}, rhs, base + 2048, 0, SC1);
      }
    }
  }
  STAMP(5);

  const __amdgpu_buffer_rsrc_t rh = make_rsrc(a.ws + WS_HT, S * F * 2);
  const __amdgpu_buffer_rsrc_t rw = make_rsrc(a.wd, F * D * 2);
  const unsigned wr0 = wave == 0 ? wring_off(0) : wave == 1 ? wring_off(1) : wave == 2 ? wring_off(2) : wring_off(3);
  const int rot = 3 * (j >> 3);
  auto kstep = [&](int b) { const int t = b + rot; return 12 * wave + (t >= 12 ? t - 12 : t); };
  unsigned vwd[5];
#pragma unroll
  for (int i = 0; i < 5; ++i) {
    const int slot = 64 * i + lane, row = slot / 10, ch = slot - 10 * row;
    vwd[i] = (unsigned)(row * (D * 2) + 160 * di + ch * 16 - 1024 * (i & 3));
  }
  auto load_w = [&](int b) {
    const unsigned dst = lds0 + wr0 + (b & 3) * WSLOT, so = (unsigned)kstep(b) * (32 * D * 2);
    dma16xN<4>(rw, dst, vwd, so);
    dma16xN<1>(rw, dst + 4096, vwd + 4, so);
  };
  auto load_w1 = [&](int b, int i) {
    dma16(rw, lds0 + wr0 + (b & 3) * WSLOT + i * 1024, vwd[i] + 1024 * (i & 3), (unsigned)kstep(b) * (32 * D * 2));
  };
  struct HS4 { u32x4 f[4]; };
  HS4 H[HD + 1];
  const unsigned voff = lane * 16;
  auto load_h = [&](HS4& h, int b) {
    const unsigned so = (unsigned)(4 * si * 48 + kstep(b)) * 1024;
#define HPOL " sc1"
    asm volatile("buffer_load_dwordx4 %0, %4, %5, %6 offen" HPOL "\n\tbuffer_load_dwordx4 %1, %4, %5, %7 offen" HPOL "\n\t"
                 "buffer_load_dwordx4 %2, %4, %5, %8 offen" HPOL "\n\tbuffer_load_dwordx4 %3, %4, %5, %9 offen" HPOL
                 : "=&v"(h.f[0]), "=&v"(h.f[1]), "=&v"(h.f[2]), "=&v"(h.f[3])
                 : "v"(voff), "s"(rh), "s"(so), "s"(so + 48 * 1024), "s"(so + 96 * 1024), "s"(so + 144 * 1024) : "memory");
  };
  auto load_h1 = [&](HS4& h, int b, int i) {
    const unsigned so = (unsigned)((4 * si + i) * 48 + kstep(b)) * 1024;
    asm volatile("buffer_load_dwordx4 %0, %1, %2, %3 offen" HPOL : "=&v"(h.f[i]) : "v"(voff), "s"(rh), "s"(so) : "memory");
  };
  const int srow = 64 * si + 16 * wave + n;
  u32x2 qv[5];
  FENCE();
  __builtin_amdgcn_s_setprio(3);
  sy_arrive(lds, SY_EPI, lane);
  STAMP(22);
  WAIT_VM(0);
  if (sy_arrive(lds, SY_HDONE, lane) == 3u) {
    sy_wait(lds, SY_GATE, 1u);
    const unsigned tag = get_tag(lds);
    if (lane == 0) flag_store(a.ws, WS_FLAGH + 4 * (16 * mi + fi), tag);
  }
  STAMP(23);
  FENCE();
  sy_wait(lds, SY_EPI, 4u);
  load_w(0); load_w(1); load_w(2); load_w(3);
  f32x4 acc[4][5];
#pragma unroll
  for (int t = 0; t < 4; ++t)
#pragma unroll
    for (int c = 0; c < 5; ++c) { acc[t][c] = (f32x4){0.f, 0.f, 0.f, 0.f}; asm volatile("" : "+v"(acc[t][c])); }
  struct WF { s16x4 f[5][2]; };
  WF w0, w1;
  const lptr wrd = lds + wr0 + (4 * g + (n >> 2)) * 160 + 8 * (lane & 3);
  auto read_w = [&](WF& w, int p, int b) {
    w.f[p >> 1][p & 1] = lds_tr(wrd + (b & 3) * WSLOT + (p & 1) * (16 * 160) + (p >> 1) * 32);
  };
  STAMP(24);
  WAIT_VM(15);
#pragma unroll
  for (int p = 0; p < 10; ++p) read_w(w0, p, 0);
  WAIT_LGKM0();
  load_w(4);
  sy_arrive(lds, SY_WPRE, lane);
  __builtin_amdgcn_s_setprio(G_PRIO);
  STAMP(6);
  sy_wait(lds, SY_HREADY, 1u);
  PROBE(3);
  STAMP(7);
#pragma unroll
  for (int b = 0; b < HD; ++b) load_h(H[b], b);
  const float gate = __builtin_bit_cast(float, __hip_atomic_load((GAS unsigned*)(a.ws + WS_GATE) + srow, __ATOMIC_RELAXED, __HIP_MEMORY_SCOPE_AGENT));
  FENCE();
  auto gstep = [&](const WF& cur, WF& nxt, HS4& h, HS4& hn, int b) {
    asm volatile("s_waitcnt vmcnt(%4) lgkmcnt(0)" : "+v"(h.f[0]), "+v"(h.f[1]), "+v"(h.f[2]), "+v"(h.f[3]) : "n"(g_younger(b)) : "memory");
    FENCE();
#pragma unroll
    for (int i = 0; i < 20; ++i) {
      acc[i & 3][i >> 2] = MFMA16(join(cur.f[i >> 2][0], cur.f[i >> 2][1]), __builtin_bit_cast(bf16x8, h.f[i & 3]), acc[i & 3][i >> 2]);
      FENCE();
      if (i < 8 && (i & 1) == 0 && b + HD < 12) { load_h1(hn, b + HD, i >> 1); FENCE(); }
      if (i == 7 && b == 9) {
#pragma unroll
        for (int c = 0; c < 5; ++c) qv[c] = *(const u32x2*)(a.x + (size_t)srow * D + 80 * di + 16 * c + 4 * g);
        asm volatile("" ::: "memory");
        FENCE();
      }
      if (i < 10 && b + 1 < 12) { read_w(nxt, i, b + 1); FENCE(); }
      if (i >= 10 && (i & 1) == 0 && b >= 1 && b + 4 < 12) { load_w1(b + 4, (i - 10) >> 1); FENCE(); }
      if (b == YPOLL_STEP && i == 17 && wave == 0) { sy_post(lds0 + L_SYNC + 4 * SY_GSTEP, 1u); FENCE(); }
      if (b == YPOLL_STEP && i == 19 && wave == 0) { wake(); FENCE(); }
    }
  };
#pragma unroll
  for (int b = 0; b < 12; b += 2) {
    gstep(w0, w1, H[b % (HD + 1)], H[(b + HD) % (HD + 1)], b);
    if (b == 0) STAMPI(14);
    gstep(w1, w0, H[(b + 1) % (HD + 1)], H[(b + 1 + HD) % (HD + 1)], b + 1);
    STAMPI(15 + (b >> 1));
  }
  PROBE(4);
  STAMP(8);

  f32x4 bsum[5];
  {
#pragma unroll
    for (int t = 0; t < 4; ++t) {
      if (t != wave) {
#pragma unroll
        for (int c = 0; c < 5; ++c) *(LAS f32x4*)(lds + wr0 + (((t - wave - 1) & 3) * 5 + c) * 1024 + lane * 16) = acc[t][c];
      } else {
#pragma unroll
        for (int c = 0; c < 5; ++c) bsum[c] = acc[t][c];
      }
    }
    sy_arrive(lds, SY_RED, lane);
    sy_wait(lds, SY_RED, 4u);
#pragma unroll
    for (int w = 0; w < 4; ++w) {
      if (w != wave) {
#pragma unroll
        for (int c = 0; c < 5; ++c) bsum[c] += *(LAS f32x4*)(lds + wring_off(w) + (((wave - w - 1) & 3) * 5 + c) * 1024 + lane * 16);
      }
    }
  }
  STAMP(9);
  sy_wait(lds, SY_YREADY, 1u);
  STAMP(10);
  {
    const int q = n >> 2, p = lane & 3;
    const __amdgpu_buffer_rsrc_t ro = make_rsrc(a.out, S * D * 2);
    const lptr ystr = lds + (wave == 0 ? ystrip_off(0) : wave == 1 ? ystrip_off(1) : wave == 2 ? ystrip_off(2) : ystrip_off(3));
#pragma unroll
    for (int c = 0; c < 5; ++c) {
      const s16x4 yv = lds_tr(ystr + (16 * c + 4 * g + q) * 32 + 8 * p);
      const u32x2 y = __builtin_bit_cast(u32x2, yv);
      const float o0 = gate * (bf_lo(qv[c][0]) * bf_lo(y[0])) + (1.f - gate) * bsum[c][0];
      const float o1 = gate * (bf_hi(qv[c][0]) * bf_hi(y[0])) + (1.f - gate) * bsum[c][1];
      const float o2 = gate * (bf_lo(qv[c][1]) * bf_lo(y[1])) + (1.f - gate) * bsum[c][2];
      const float o3 = gate * (bf_hi(qv[c][1]) * bf_hi(y[1])) + (1.f - gate) * bsum[c][3];
      __builtin_amdgcn_raw_buffer_store_b64((u32x2){pack_bf16(o0, o1), pack_bf16(o2, o3)}, ro,
                                            (unsigned)((srow * D + 80 * di + 16 * c + 4 * g) * 2), 0, OUT_AUX);
    }
  }
  PROBE(5);
  STAMP(11);
}

}

extern "C" void kernel_launch(void* const* d_in, const int* in_sizes, int n_in, void* d_out, int out_size, void* d_ws,
                              size_t ws_size, hipStream_t stream) {
  (void)in_sizes; (void)n_in; (void)out_size; (void)ws_size;
  Args a;
  a.x = (const u16*)d_in[0];
  a.k = (const u16*)d_in[1];
  a.v = (const u16*)d_in[2];
  a.shk = (const u16*)d_in[3];
  a.ssmk = (const u16*)d_in[4];
  a.ssmd = (const u16*)d_in[5];
  a.wg = (const u16*)d_in[7];
  a.wu = (const u16*)d_in[8];
  a.wd = (const u16*)d_in[9];
  a.gw = (const u16*)d_in[10];
  a.ws = (char*)d_ws;
  a.out = (u16*)d_out;
  (void)hipFuncSetAttribute((const void*)fused_kernel, hipFuncAttributeMaxDynamicSharedMemorySize, LDS_BYTES);
  fused_kernel<<<256, 512, LDS_BYTES, stream>>>(a);
}
```

```cpp
#include <hip/hip_runtime.h>
#include <hip/hip_bf16.h>

namespace {

typedef unsigned short u16;
typedef __bf16 bf16x8 __attribute__((ext_vector_type(8)));
typedef __bf16 bf16x2 __attribute__((ext_vector_type(2)));
typedef short s16x4 __attribute__((ext_vector_type(4)));
typedef float f32x2 __attribute__((ext_vector_type(2)));
typedef float f32x4 __attribute__((ext_vector_type(4)));
typedef unsigned u32x2 __attribute__((ext_vector_type(2)));
typedef unsigned u32x4 __attribute__((ext_vector_type(4)));
#define LAS __attribute__((address_space(3)))
#define GAS __attribute__((address_space(1)))
typedef LAS char* lptr;

constexpr int S = 2048, D = 640, F = 1536;
constexpr int SC1 = 16;

constexpr size_t WS_HT = 0;
constexpr size_t WS_KVR = WS_HT + (size_t)S * F * 2;
constexpr size_t WS_Y2T = WS_KVR + (size_t)S * D * 2;
constexpr size_t WS_GATE = WS_Y2T + (size_t)S * D * 2;
constexpr size_t WS_FLAGKV = WS_GATE + (size_t)S * 4;
constexpr size_t WS_FLAGH = WS_FLAGKV + 1024;
constexpr size_t WS_FLAGY = WS_FLAGH + 1024;
constexpr size_t WS_EPOCH = WS_FLAGY + 1024;
constexpr size_t WS_END = WS_EPOCH + 256;

constexpr int STAGE = 40960, XB = 0, GB = 16384, UB = 28672;
constexpr int L_AUX = 3 * STAGE;
constexpr int L_T = L_AUX, UR_BYTES = 4608;
constexpr int L_FREE = L_AUX + 3 * UR_BYTES;
constexpr int L_SYNC = L_FREE + 20480 + 2 * 2560;
constexpr int L_DUMMY = L_SYNC + 128;
constexpr int L_END = L_DUMMY + 256;
constexpr int KC_COPY = 4160, KC_BYTES = 4 * KC_COPY;
constexpr int WSLOT = 5120;
__device__ constexpr int wring_off(int w) { return w == 0 ? 0 : w == 1 ? 20480 : w == 2 ? 2 * STAGE + KC_BYTES : L_FREE; }
__device__ constexpr int ystrip_off(int w) { return w < 2 ? STAGE + 2 * KC_BYTES + w * 2560 : L_FREE + 20480 + (w - 2) * 2560; }
static_assert(2 * STAGE + KC_BYTES + 20480 <= 3 * STAGE && STAGE + 2 * KC_BYTES + 2 * 2560 <= 2 * STAGE, "second-phase LDS map");
enum { SY_LANDED = 0, SY_CONSUMED = 4, SY_KV = 8, SY_SIDE = 9, SY_HDONE = 10, SY_RED = 11, SY_CONV = 12, SY_AREADY = 13,
       SY_HREADY = 14, SY_YREADY = 15, SY_TAG = 16, SY_TAGOK = 17, SY_GSTEP = 18, SY_GATE = 19, SY_EPI = 20, SY_WPRE = 21 };

#define STAMP(i) do {} while (0)
#define STAMPC(i) do {} while (0)
#define STAMPI(i) do {} while (0)
#define STAMP_INIT() do {} while (0)
#define STAMP_DUMP() do {} while (0)
constexpr int LDS_BYTES = L_END;

#define PROBE_AT 0
#define PROBE_US 0
#define PROBE(k) do { if (PROBE_AT == (k)) { while (__builtin_amdgcn_s_memrealtime() - probe_t0 < 100ull * PROBE_US) __builtin_amdgcn_s_sleep(1); } } while (0)
#define PROBE_GLOBAL 0
#define MFMA16(a, b, c) __builtin_amdgcn_mfma_f32_16x16x32_bf16((a), (b), (c), 0, 0, 0)
#define WAIT_VM(n) asm volatile("s_waitcnt vmcnt(" #n ")" ::: "memory")
#define WAIT_LGKM0() asm volatile("s_waitcnt lgkmcnt(0)" ::: "memory")
#define BARRIER() do { asm volatile("" ::: "memory"); __builtin_amdgcn_s_barrier(); asm volatile("" ::: "memory"); } while (0)
#define FENCE() __builtin_amdgcn_sched_barrier(0)
constexpr unsigned SPIN_LDS = 1u << 22, SPIN_MEM = 1u << 16;

__device__ __forceinline__ unsigned pack_bf16(float lo, float hi) {
  f32x2 v = {lo, hi};
  return __builtin_bit_cast(unsigned, __builtin_convertvector(v, bf16x2));
}
__device__ __forceinline__ float bf_lo(unsigned w) { return __builtin_bit_cast(float, w << 16); }
__device__ __forceinline__ float bf_hi(unsigned w) { return __builtin_bit_cast(float, w & 0xffff0000u); }
__device__ __forceinline__ float sigmoidf_(float x) {
  return __builtin_amdgcn_rcpf(1.0f + __builtin_amdgcn_exp2f(x * -1.4426950408889634f));
}
__device__ __forceinline__ __amdgpu_buffer_rsrc_t make_rsrc(const void* p, unsigned bytes) {
  return __builtin_amdgcn_make_buffer_rsrc(const_cast<void*>(p), 0, bytes, 0x00020000);
}
__device__ __forceinline__ unsigned lds_addr(lptr p) { return (unsigned)(size_t)p; }
__device__ __forceinline__ void dma16(__amdgpu_buffer_rsrc_t r, unsigned dst, unsigned voff, unsigned soff) {
  asm volatile("s_mov_b32 m0, %0\n\ts_nop 0\n\tbuffer_load_dwordx4 %1, %2, %3 offen lds"
               :: "s"(dst), "v"(voff), "s"(r), "s"(soff) : "memory");
}
template <int N>
__device__ __forceinline__ void dma16xN(__amdgpu_buffer_rsrc_t r, unsigned dst, const unsigned* v, unsigned soff) {
  static_assert(N >= 1 && N <= 4, "12-bit instruction offset");
  if constexpr (N == 1)
    asm volatile("s_mov_b32 m0, %0\n\ts_nop 0\n\tbuffer_load_dwordx4 %1, %2, %3 offen lds"
                 :: "s"(dst), "v"(v[0]), "s"(r), "s"(soff) : "memory");
  else if constexpr (N == 2)
    asm volatile("s_mov_b32 m0, %0\n\ts_nop 0\n\tbuffer_load_dwordx4 %1, %3, %4 offen lds\n\t"
                 "buffer_load_dwordx4 %2, %3, %4 offen offset:1024 lds"
                 :: "s"(dst), "v"(v[0]), "v"(v[1]), "s"(r), "s"(soff) : "memory");
  else if constexpr (N == 3)
    asm volatile("s_mov_b32 m0, %0\n\ts_nop 0\n\tbuffer_load_dwordx4 %1, %4, %5 offen lds\n\t"
                 "buffer_load_dwordx4 %2, %4, %5 offen offset:1024 lds\n\tbuffer_load_dwordx4 %3, %4, %5 offen offset:2048 lds"
                 :: "s"(dst), "v"(v[0]), "v"(v[1]), "v"(v[2]), "s"(r), "s"(soff) : "memory");
  else
    asm volatile("s_mov_b32 m0, %0\n\ts_nop 0\n\tbuffer_load_dwordx4 %1, %5, %6 offen lds\n\t"
                 "buffer_load_dwordx4 %2, %5, %6 offen offset:1024 lds\n\tbuffer_load_dwordx4 %3, %5, %6 offen offset:2048 lds\n\t"
                 "buffer_load_dwordx4 %4, %5, %6 offen offset:3072 lds"
                 :: "s"(dst), "v"(v[0]), "v"(v[1]), "v"(v[2]), "v"(v[3]), "s"(r), "s"(soff) : "memory");
}
__device__ __forceinline__ void touch(const void* p, unsigned dummy_lds) {
  asm volatile("s_mov_b32 m0, %0\n\ts_nop 0\n\tglobal_load_lds_dword %1, off" :: "s"(dummy_lds), "v"(p) : "memory");
}
__device__ __forceinline__ s16x4 lds_tr(lptr p) { return __builtin_amdgcn_ds_read_tr16_b64_v4i16((LAS s16x4*)p); }
__device__ __forceinline__ bf16x8 join(s16x4 a, s16x4 b) {
  return __builtin_bit_cast(bf16x8, __builtin_shufflevector(a, b, 0, 1, 2, 3, 4, 5, 6, 7));
}
__device__ __forceinline__ bf16x8 lds_frag(lptr p) { return *(LAS bf16x8*)p; }
__device__ __forceinline__ u32x4 ld128(__amdgpu_buffer_rsrc_t r, unsigned voff, unsigned soff, int aux) {
  return aux ? __builtin_bit_cast(u32x4, __builtin_amdgcn_raw_buffer_load_b128(r, voff, soff, SC1))
             : __builtin_bit_cast(u32x4, __builtin_amdgcn_raw_buffer_load_b128(r, voff, soff, 0));
}

__device__ __forceinline__ LAS unsigned* sy(lptr lds, int i) { return (LAS unsigned*)(lds + L_SYNC) + i; }
__device__ __forceinline__ unsigned sy_get(lptr lds, int i) {
  return __builtin_amdgcn_readfirstlane(__hip_atomic_load(sy(lds, i), __ATOMIC_RELAXED, __HIP_MEMORY_SCOPE_WORKGROUP));
}
__device__ __forceinline__ void wake() { asm volatile("s_wakeup" ::: "memory"); }
__device__ __forceinline__ void sy_set(lptr lds, int i, unsigned v, int lane) {
  if (lane == 0) __hip_atomic_store(sy(lds, i), v, __ATOMIC_RELAXED, __HIP_MEMORY_SCOPE_WORKGROUP);
  asm volatile("s_waitcnt lgkmcnt(0)\n\ts_wakeup" ::: "memory");
}
#define TOUCH_H 0
#define LOADER_POST_FIRST 1
#define HPOLL_SLEEP 4
#define YPOLL_SLEEP 8
#define YPOLL_STEP 8
#ifndef M_PRIO
#define M_PRIO 2
#endif
#define G_PRIO 0
#define ROT1 1
#define DMA_SPACE 0
#define C_PRIO 2
#define G_HDEPTH 4
#define OUT_AUX 0
#define H_ACQUIRE 0
#define CODE_LINES 0
#define CODE_WAVE 7
#define LAND_RD 18
#define TOUCH_DELAY 8
#define TOUCH1 0
#define FILT_ITER 6
#define W7_DELAY 0
#define POLL_AFTER 8
#define LOADER_PRIO 0
#define SLEEP_LDS 4
__device__ __forceinline__ void sy_post(unsigned addr, unsigned v) {
  asm volatile("s_mov_b64 exec, 1\n\tds_write_b32 %0, %1\n\ts_mov_b64 exec, -1" :: "v"(addr), "v"(v) : "memory");
}
__device__ __forceinline__ void sy_wait(lptr lds, int i, unsigned v) {
#pragma unroll 1
  for (unsigned n = 0; n < SPIN_LDS; ++n) {
    if (sy_get(lds, i) >= v) break;
    __builtin_amdgcn_s_sleep(SLEEP_LDS);
  }
  asm volatile("" ::: "memory");
}
__device__ __forceinline__ unsigned sy_arrive(lptr lds, int i, int lane) {
  WAIT_LGKM0();
  unsigned old = 0;
  if (lane == 0) old = __hip_atomic_fetch_add(sy(lds, i), 1u, __ATOMIC_RELAXED, __HIP_MEMORY_SCOPE_WORKGROUP);
  old = __builtin_amdgcn_readfirstlane(old);
  wake();
  return old;
}
__device__ __forceinline__ unsigned min4(u32x4 v) { return min(min(v[0], v[1]), min(v[2], v[3])); }
__device__ __forceinline__ u32x4 sy_get4(lptr lds, int i) { return *(volatile LAS u32x4*)(lds + L_SYNC + 4 * i); }
__device__ __forceinline__ u32x2 sy_get2(lptr lds, int i) { return *(volatile LAS u32x2*)(lds + L_SYNC + 4 * i); }
__device__ __forceinline__ void sy_wait4(lptr lds, int i, unsigned v) {
#pragma unroll 1
  for (unsigned n = 0; n < SPIN_LDS; ++n) {
    if ((unsigned)__builtin_amdgcn_readfirstlane(min4(sy_get4(lds, i))) >= v) break;
    __builtin_amdgcn_s_sleep(SLEEP_LDS);
  }
  asm volatile("" ::: "memory");
}
#define LOADER_SLEEP 1
__device__ __forceinline__ void loader_wait(lptr lds, unsigned v) {
#pragma unroll 1
  for (unsigned n = 0; n < SPIN_LDS; ++n) {
    if ((unsigned)__builtin_amdgcn_readfirstlane(min4(sy_get4(lds, SY_CONSUMED))) >= v) break;
    if (LOADER_SLEEP) __builtin_amdgcn_s_sleep(LOADER_SLEEP);
  }
  asm volatile("" ::: "memory");
}
__device__ __forceinline__ void sy_wait4m(lptr lds, unsigned v) {
#pragma unroll 1
  for (unsigned n = 0; n < SPIN_LDS; ++n) {
    const u32x4 l = sy_get4(lds, SY_LANDED);
    if ((unsigned)__builtin_amdgcn_readfirstlane(min(min(l[0], l[1]), l[2])) >= v) break;
    __builtin_amdgcn_s_sleep(SLEEP_LDS);
  }
  asm volatile("" ::: "memory");
}
__device__ __forceinline__ unsigned get_tag(lptr lds) {
  sy_wait(lds, SY_TAGOK, 1u);
  return sy_get(lds, SY_TAG);
}
__device__ __forceinline__ void flag_store(char* ws, size_t off, unsigned tag) {
  __hip_atomic_store((GAS unsigned*)(ws + off), tag, __ATOMIC_RELAXED, __HIP_MEMORY_SCOPE_AGENT);
}
#define SIDE_PUB_ITER 3
#define POLL_SLEEP 32
template <int SLEEP = POLL_SLEEP>
__device__ __forceinline__ void flag_wait(char* ws, size_t off, bool on, unsigned tag) {
#pragma unroll 1
  for (unsigned n = 0; n < SPIN_MEM; ++n) {
    const unsigned v = __hip_atomic_load((GAS unsigned*)(ws + off), __ATOMIC_RELAXED, __HIP_MEMORY_SCOPE_AGENT);
    if (__all(!on || v == tag)) break;
    __builtin_amdgcn_s_sleep(SLEEP);
  }
  asm volatile("" ::: "memory");
}

struct Args {
  const u16* x; const u16* k; const u16* v; const u16* shk; const u16* ssmk; const u16* ssmd;
  const u16* wg; const u16* wu; const u16* wd; const u16* gw;
  char* ws; u16* out;
};

struct Frags { bf16x8 x[4]; s16x4 g[3][2]; s16x4 u[3][2]; };

__device__ constexpr int tri_start(int t) { return 8 * t - (t * (t - 1)) / 2; }
__device__ constexpr int tri_row(int p) {
  return p < 8 ? 0 : p < 15 ? 1 : p < 21 ? 2 : p < 26 ? 3 : p < 30 ? 4 : p < 33 ? 5 : p < 35 ? 6 : 7;
}

template <int N> __device__ __forceinline__ void wait_vm() { asm volatile("s_waitcnt vmcnt(%0)" ::"n"(N) : "memory"); }

struct Filter { unsigned d[5][6]; unsigned dbits; };
__device__ __forceinline__ void load_filter(const Args& a, Filter& f, int ch, int lane) {
  const __amdgpu_buffer_rsrc_t rk = make_rsrc(a.ssmk + (size_t)ch * S, S * 2);
#pragma unroll
  for (int it = 0; it < 5; ++it) {
    const unsigned off = (unsigned)(16 * (lane + 64 * it) - 32);
    const u32x4 v0 = __builtin_bit_cast(u32x4, __builtin_amdgcn_raw_buffer_load_b128(rk, off, 0, 0));
    const u32x2 v1 = __builtin_bit_cast(u32x2, __builtin_amdgcn_raw_buffer_load_b64(rk, off + 16, 0, 0));
    f.d[it][0] = v0[0]; f.d[it][1] = v0[1]; f.d[it][2] = v0[2]; f.d[it][3] = v0[3]; f.d[it][4] = v1[0]; f.d[it][5] = v1[1];
  }
  f.dbits = a.ssmd[ch];
}

template <int NX>
__device__ __forceinline__ void loader(const Args& a, lptr lds, int lw, int x0, int mi, int fi, int rot, Filter& flt, int ch, int lane) {
  const unsigned lds0 = lds_addr(lds);
  const __amdgpu_buffer_rsrc_t rx = make_rsrc(a.x, S * D * 2);
  const __amdgpu_buffer_rsrc_t rg = make_rsrc(a.wg, D * F * 2);
  const __amdgpu_buffer_rsrc_t ru = make_rsrc(a.wu, D * F * 2);
  unsigned vx[NX], vw[4];
#pragma unroll
  for (int i = 0; i < NX; ++i) {
    const int row = 8 * (x0 + i) + (lane >> 3), c = (lane & 7) ^ ((row >> 1) & 7);
    vx[i] = (unsigned)((128 * mi + row) * (D * 2) + c * 16 - 1024 * (i & 3));
  }
#pragma unroll
  for (int i = 0; i < 4; ++i) {
    const int slot = 64 * (4 * lw + i) + lane, k = slot / 12, chp = slot - 12 * k, c = chp ^ (2 * ((k >> 3) & 1));
    vw[i] = (unsigned)(k * (F * 2) + fi * 192 + c * 16 - 1024 * i);
  }
  const unsigned land_addr = lds0 + L_SYNC + 4 * (SY_LANDED + lw);
  auto slice = [&](int s) { const int k = s + rot; return k >= 10 ? k - 10 : k; };
  auto part_a = [&](int s) {
    const unsigned b = lds0 + (s % 3) * STAGE, so = (unsigned)slice(s) * (64 * F * 2);
    dma16xN<4>(rg, b + GB + 4 * lw * 1024, vw, so);
    if (DMA_SPACE) __builtin_amdgcn_s_sleep(DMA_SPACE);
    dma16xN<4>(ru, b + UB + 4 * lw * 1024, vw, so);
    if (DMA_SPACE) __builtin_amdgcn_s_sleep(DMA_SPACE);
  };
  auto part_b = [&](int s) {
    const unsigned b = lds0 + (s % 3) * STAGE, so = (unsigned)slice(s) * 128;
    dma16xN<4>(rx, b + XB + x0 * 1024, vx, so);
    if (DMA_SPACE) __builtin_amdgcn_s_sleep(DMA_SPACE);
    dma16xN<NX - 4>(rx, b + XB + (x0 + 4) * 1024, vx + 4, so);
  };
  constexpr int P = NX + 8;
  part_a(0); part_b(0);
  part_a(1); part_b(1);
  STAMP(1);
  wait_vm<P>();
  STAMP(2);
  BARRIER();
  part_a(2); part_b(2);
#pragma unroll
  for (int s = 0; s < 9; ++s) {
    if (s == 8) wait_vm<11>(); else wait_vm<P>();
    STAMPI(8 + 2 * s);
    BARRIER();
    STAMPI(9 + 2 * s);
    if (s < 7) { part_a(s + 3); part_b(s + 3); }
    if (s == 7) { FENCE(); load_filter(a, flt, ch, lane); FENCE(); asm volatile("" ::: "memory"); }
  }
}

struct KvTask { u32x4 kr[7], vr[4], wk[4]; };
__device__ __forceinline__ void kv_load(const Args& a, KvTask& t, int task, int tsi, int tdi) {
  const int dc = task % 10, sb = task / 10;
  const int s0 = 64 * tsi + 4 * sb, d0 = 80 * tdi + 8 * dc;
#pragma unroll
  for (int r = 0; r < 7; ++r) {
    const int s = s0 - 3 + r;
    t.kr[r] = *(const u32x4*)(a.k + (size_t)(s < 0 ? 0 : s) * D + d0);
  }
#pragma unroll
  for (int r = 0; r < 4; ++r) t.vr[r] = *(const u32x4*)(a.v + (size_t)(s0 + r) * D + d0);
#pragma unroll
  for (int i = 0; i < 4; ++i) t.wk[i] = *(const u32x4*)(a.shk + (size_t)d0 * 4 + i * 8);
}
__device__ __forceinline__ float dot2(unsigned x, unsigned y, float c) {
  return __builtin_amdgcn_fdot2_f32_bf16(__builtin_bit_cast(bf16x2, x), __builtin_bit_cast(bf16x2, y), c, false);
}
__device__ __forceinline__ float dot2z(unsigned x, unsigned y) {
  float r;
  asm("v_dot2_f32_bf16 %0, %1, %2, 0" : "=v"(r) : "v"(x), "v"(y));
  return r;
}
__device__ __forceinline__ void kv_compute(KvTask& t, lptr lds, int task, int tsi) {
  const int dc = task % 10, sb = task / 10;
  if (tsi == 0 && sb == 0) {
#pragma unroll
    for (int r = 0; r < 3; ++r) t.kr[r] = (u32x4){0u, 0u, 0u, 0u};
  }
  const int sg = 15 - sb;
  lptr dst = lds + L_T + 8 * dc * 128 + (((sg >> 1) ^ (dc & 7)) << 4) + ((sg & 1) << 3);
#pragma unroll
  for (int c = 0; c < 8; ++c) {
    const unsigned w01 = t.wk[c >> 1][2 * (c & 1)], w23 = t.wk[c >> 1][2 * (c & 1) + 1];
    const unsigned sel = (c & 1) ? 0x03020706u : 0x01000504u;
    unsigned pr[6];
#pragma unroll
    for (int i = 0; i < 6; ++i) pr[i] = __builtin_amdgcn_perm(t.kr[i + 1][c >> 1], t.kr[i][c >> 1], sel);
    float o[4];
#pragma unroll
    for (int r = 0; r < 4; ++r) {
      const float ks = dot2(pr[r + 2], w01, dot2z(pr[r], w23));
      o[r] = ks * ((c & 1) ? bf_hi(t.vr[r][c >> 1]) : bf_lo(t.vr[r][c >> 1]));
    }
    *(LAS u32x2*)(dst + c * 128) = (u32x2){pack_bf16(o[3], o[2]), pack_bf16(o[1], o[0])};
  }
}

__device__ __forceinline__ void conv_wave(const Args& a, lptr lds, lptr kc, lptr ur, const Filter& flt, int ch, int nconv, int lane) {
#pragma unroll
  for (int it = 0; it < 5; ++it) {
    const int c = lane + 64 * it;
    if (c < 260) {
      const unsigned a0 = flt.d[it][0], a1 = flt.d[it][1], a2 = flt.d[it][2], a3 = flt.d[it][3], a4 = flt.d[it][4], a5 = flt.d[it][5];
      const unsigned e0 = __builtin_amdgcn_alignbit(a1, a0, 16), e1 = __builtin_amdgcn_alignbit(a2, a1, 16),
                     e2 = __builtin_amdgcn_alignbit(a3, a2, 16), e3 = __builtin_amdgcn_alignbit(a4, a3, 16),
                     e4 = __builtin_amdgcn_alignbit(a5, a4, 16);
      lptr w = kc + 16 * c;
      *(LAS u32x4*)(w) = (u32x4){a0, a1, a2, a3};
      *(LAS u32x4*)(w + KC_COPY) = (u32x4){e0, e1, e2, e3};
      *(LAS u32x4*)(w + 2 * KC_COPY) = (u32x4){a1, a2, a3, a4};
      *(LAS u32x4*)(w + 3 * KC_COPY) = (u32x4){e1, e2, e3, e4};
    }
  }
  const float dcoef = bf_lo(flt.dbits);
  WAIT_LGKM0();
  sy_wait(lds, SY_AREADY, 1u);
  sy_wait(lds, SY_EPI, 4u);
  __builtin_amdgcn_s_setprio(C_PRIO);
  STAMP(4);
  STAMPC(28);

  const int m = lane & 15, g = lane >> 4;
  const int rho = (m + 1) & 3;
  lptr abase = kc + rho * KC_COPY + 2 * (8 * g + m - 31 - rho);
  lptr bbase = ur + 2 * (2000 - 16 * m + 8 * g) - 7 * 512;
  f32x4 acc[8];
#pragma unroll
  for (int t = 0; t < 8; ++t) acc[t] = (f32x4){0.f, 0.f, 0.f, 0.f};
  struct CF { s16x4 a[8][2]; bf16x8 b[8]; };
  auto cread = [&](CF& f, int p, int i) {
    if (p < 16) f.a[p >> 1][p & 1] = *(LAS s16x4*)(abase + 64 * i + 512 * (p >> 1) + 8 * (p & 1));
    else f.b[p - 16] = lds_frag(bbase + 64 * i + 512 * (7 - (p - 16)));
  };
  auto cmma = [&](const CF& f, int p) {
    const int t = tri_row(p), u = p - tri_start(t);
    acc[t + u] = MFMA16(join(f.a[t][0], f.a[t][1]), f.b[u], acc[t + u]);
  };
  auto residue = [&](const CF& cur, CF& nxt, int inext) {
#pragma unroll
    for (int p = 0; p < 36; ++p) {
      cmma(cur, p); FENCE();
      if (p < 24) { cread(nxt, p, inext); FENCE(); }
    }
  };
  CF c0, c1;
#pragma unroll
  for (int p = 0; p < 24; ++p) cread(c0, p, 1);
#pragma unroll 1
  for (int i = 1; i <= 8; i += 2) {
    FENCE();
    residue(c0, c1, i + 1);
    residue(c1, c0, (i + 2 <= 8) ? i + 2 : 8);
  }
  STAMP(5);
  STAMPC(29);
  const __amdgpu_buffer_rsrc_t ry = make_rsrc(a.ws + WS_Y2T + (size_t)ch * S * 2, S * 2);
#pragma unroll
  for (int t = 0; t < 8; t += 2) {
    unsigned pk[2][2];
#pragma unroll
    for (int e = 0; e < 2; ++e) {
      const int s = 256 * (t + e) + 16 * m + 4 * g;
      const u32x2 kv = *(LAS u32x2*)(ur + 2 * (2044 - s));
      pk[e][0] = pack_bf16(acc[t + e][0] + bf_hi(kv[1]) * dcoef, acc[t + e][1] + bf_lo(kv[1]) * dcoef);
      pk[e][1] = pack_bf16(acc[t + e][2] + bf_hi(kv[0]) * dcoef, acc[t + e][3] + bf_lo(kv[0]) * dcoef);
    }
    const auto r0 = __builtin_amdgcn_permlane16_swap(pk[0][0], pk[1][0], false, false);
    const auto r1 = __builtin_amdgcn_permlane16_swap(pk[0][1], pk[1][1], false, false);
    const u32x4 o = {r0[0], r1[0], r0[1], r1[1]};
    const unsigned off = 2 * (256 * (t + (g & 1)) + 16 * m + 8 * (g >> 1));
    __builtin_amdgcn_raw_buffer_store_b128(o, ry, off, 0, SC1);
  }
  STAMP(6);
  WAIT_VM(0);
  STAMP(7);
  if (sy_arrive(lds, SY_CONV, lane) == (unsigned)(nconv - 1)) {
    const unsigned tag = get_tag(lds);
    if (lane == 0) flag_store(a.ws, WS_FLAGY + 4 * blockIdx.x, tag);
  }
}

constexpr int HD = G_HDEPTH;
__device__ constexpr int g_younger(int b) {
  int count = 0, last = 0;
  for (int k = 0; k < HD; ++k) { count += 4; if (k == b) last = count; }
  count += 1;
  for (int s = 0; s < b; ++s) {
    if (s + HD <= 11) { count += 4; if (s + HD == b) last = count; }
    if (s == 9) count += 5;
    if (s >= 1 && s <= 7) { count += 5; if (s + 4 == b + 1) last = count; }
  }
  return count - last;
}
static_assert(G_HDEPTH != 3 || (g_younger(0) == 9 && g_younger(1) == 9 && g_younger(2) == 14 && g_younger(3) == 18 &&
              g_younger(8) == 18 && g_younger(9) == 13 && g_younger(10) == 9 && g_younger(11) == 5), "wait table");
__global__ __launch_bounds__(512, 2) void fused_kernel(Args a) {
  extern __shared__ __attribute__((aligned(16))) char smem[];
  lptr lds = (lptr)smem;
  const int tid = threadIdx.x, lane = tid & 63;
  const int wave = __builtin_amdgcn_readfirstlane(tid >> 6);
  const int bid = blockIdx.x;
  const int xcd = bid & 7, j = bid >> 3;
  const int mi = 8 * (xcd & 1) + (j & 7), fi = 4 * (xcd >> 1) + (j >> 3);
  const int si = 8 * (xcd & 3) + (j & 7), di = 4 * (xcd >> 2) + (j >> 3);
  const int c0 = (5 * bid) >> 1, nconv = 2 + (bid & 1);
  const unsigned lds0 = lds_addr(lds);
  STAMP_INIT();
  STAMP(0);

  if (wave == 7) {
    if (lane < 32) *sy(lds, lane) = 0u;
    WAIT_LGKM0();
  }
  BARRIER();

  if (wave == 7) {
    const unsigned epoch = __hip_atomic_load((GAS unsigned*)(a.ws + WS_EPOCH), __ATOMIC_RELAXED, __HIP_MEMORY_SCOPE_AGENT);
    {
      const int r0 = 128 * mi + 8 * fi, gr = lane >> 4, gq = lane & 15;
      u32x4 gx[2][5], gw[5];
#pragma unroll
      for (int i = 0; i < 5; ++i) {
        gw[i] = ((const u32x4*)a.gw)[gq + 16 * i];
        gx[0][i] = ((const u32x4*)(a.x + (size_t)(r0 + gr) * D))[gq + 16 * i];
        gx[1][i] = ((const u32x4*)(a.x + (size_t)(r0 + 4 + gr) * D))[gq + 16 * i];
      }
      FENCE();
      sy_set(lds, SY_TAG, epoch + 1u, lane);
      sy_set(lds, SY_TAGOK, 1u, lane);
      BARRIER();
#pragma unroll
      for (int pass = 0; pass < 2; ++pass) {
        float sum = 0.f;
#pragma unroll
        for (int i = 0; i < 5; ++i)
#pragma unroll
          for (int e = 0; e < 4; ++e) sum = dot2(gx[pass][i][e], gw[i][e], sum);
#pragma unroll
        for (int m = 8; m >= 1; m >>= 1) sum += __shfl_xor(sum, m);
        if (gq == 0)
          __hip_atomic_store((GAS unsigned*)(a.ws + WS_GATE) + r0 + 4 * pass + gr, __builtin_bit_cast(unsigned, sigmoidf_(sum)),
                             __ATOMIC_RELAXED, __HIP_MEMORY_SCOPE_AGENT);
      }
    }
    const unsigned tag = epoch + 1u;
    STAMP(1);
#pragma unroll 1
    for (int s = 0; s < 9; ++s) BARRIER();
    WAIT_VM(0);
    sy_set(lds, SY_GATE, 1u, lane);
    STAMP(2);
    {
      flag_wait(a.ws, WS_FLAGKV + 4 * (32 * (bid >> 5) + (lane & 31)), true, tag);
      STAMP(8);
      u32x4 uv[3][4];
#pragma unroll
      for (int cw = 0; cw < 3; ++cw) {
        const __amdgpu_buffer_rsrc_t ru = make_rsrc(a.ws + WS_KVR + (size_t)(c0 + (cw < nconv ? cw : 0)) * S * 2, S * 2);
#pragma unroll
        for (int i = 0; i < 4; ++i) uv[cw][i] = ld128(ru, (unsigned)(i * 1024 + lane * 16), 0, SC1);
      }
      FENCE();
#pragma unroll
      for (int cw = 0; cw < 3; ++cw) {
        lptr ur = lds + L_AUX + cw * UR_BYTES;
#pragma unroll
        for (int i = 0; i < 4; ++i) *(LAS u32x4*)(ur + i * 1024 + lane * 16) = uv[cw][i];
        if (lane < 32) *(LAS u32x4*)(ur + 4096 + lane * 16) = (u32x4){0u, 0u, 0u, 0u};
      }
      WAIT_LGKM0();
      sy_set(lds, SY_AREADY, 1u, lane);
    }
    STAMP(9);
    FENCE();
    {
      const char* base = (const char*)a.wd + 640 * (xcd >> 2);
#pragma unroll
      for (int t = 0; t < 4; ++t) {
        const int id = min(64 * t + lane, 239), r = id / 5, c = id - 5 * r;
        touch(base + (size_t)(48 * j + r) * (D * 2) + 128 * c, lds0 + L_DUMMY);
      }
    }
    if (bid == 0) {
#pragma unroll
      for (int i = 0; i < 4; ++i) flag_wait(a.ws, WS_FLAGKV + 4 * (lane + 64 * i), true, tag);
      if (lane == 0) flag_store(a.ws, WS_EPOCH, tag);
    }
    sy_wait(lds, SY_WPRE, 4u);
    flag_wait<HPOLL_SLEEP>(a.ws, WS_FLAGH + 4 * (16 * (si >> 1) + (lane & 15)), true, tag);
    sy_set(lds, SY_HREADY, 1u, lane);
    STAMP(10);
    sy_wait(lds, SY_CONV, nconv);
    sy_wait(lds, SY_GSTEP, 1u);
    flag_wait<YPOLL_SLEEP>(a.ws, WS_FLAGY + 4 * (32 * di + (lane & 31)), true, tag);
    STAMP(11);
    {
      const __amdgpu_buffer_rsrc_t ry = make_rsrc(a.ws + WS_Y2T, S * D * 2);
      u32x4 yv[10];
#pragma unroll
      for (int i = 0; i < 10; ++i) {
        const int id = lane + 64 * i;
        yv[i] = ld128(ry, (unsigned)((80 * di + (id >> 3)) * (S * 2) + 64 * si * 2 + (id & 7) * 16), 0, SC1);
      }
#pragma unroll
      for (int i = 0; i < 10; ++i) {
        const int id = lane + 64 * i, w = (id & 7) >> 1;
        const int so = w == 0 ? ystrip_off(0) : w == 1 ? ystrip_off(1) : w == 2 ? ystrip_off(2) : ystrip_off(3);
        *(LAS u32x4*)(lds + so + (id >> 3) * 32 + (id & 1) * 16) = yv[i];
      }
      WAIT_LGKM0();
      sy_set(lds, SY_YREADY, 1u, lane);
    }
    STAMP(12);
    WAIT_VM(0);
    STAMP_DUMP();
    return;
  }

  if (wave >= 4) {
    const int cw = wave - 4;
    const bool active = cw < nconv;
    const int ch = c0 + (active ? cw : 0);
    Filter flt;
    const int rot1 = ROT1 ? (3 * (j >> 3) + (j & 7)) % 10 : 0;
    if (cw == 0) loader<6>(a, lds, 0, 0, mi, fi, rot1, flt, ch, lane);
    else loader<5>(a, lds, cw, 1 + 5 * cw, mi, fi, rot1, flt, ch, lane);
    STAMP(3);
    if (active) {
      conv_wave(a, lds, lds + (cw == 2 ? 2 * STAGE : STAGE + cw * KC_BYTES), lds + L_AUX + cw * UR_BYTES, flt, ch, nconv, lane);
    }
    STAMP_DUMP();
    return;
  }

  unsigned long long probe_t0 = PROBE_AT ? __builtin_amdgcn_s_memrealtime() : 0ull;
  if (PROBE_GLOBAL && tid == 0)
    __hip_atomic_store((GAS unsigned long long*)(a.ws + (32u << 20)) + bid, probe_t0, __ATOMIC_RELAXED, __HIP_MEMORY_SCOPE_AGENT);
  (void)probe_t0;
  f32x4 ag[4][3], au[4][3];
  {
    const int tsi = bid >> 3, tdi = bid & 7;
    const int task = 40 * wave + (lane < 40 ? lane : 0);
    KvTask t0;
    kv_load(a, t0, task, tsi, tdi);
    FENCE();
#pragma unroll
    for (int t = 0; t < 4; ++t)
#pragma unroll
      for (int c = 0; c < 3; ++c) {
        ag[t][c] = (f32x4){0.f, 0.f, 0.f, 0.f}; au[t][c] = (f32x4){0.f, 0.f, 0.f, 0.f};
        asm volatile("" : "+v"(ag[t][c]), "+v"(au[t][c]));
      }
    FENCE();
    STAMP(1);
    if (lane < 40) kv_compute(t0, lds, task, tsi);
    sy_arrive(lds, SY_KV, lane);
    sy_wait(lds, SY_KV, 4u);
    STAMP(2);
    const __amdgpu_buffer_rsrc_t rkv = make_rsrc(a.ws + WS_KVR, S * D * 2);
#pragma unroll
    for (int i = 0; i < 3; ++i) {
      const int id = tid + 256 * i;
      if (id < 640) {
        const int dl = id >> 3, jj = id & 7;
        const u32x4 val = *(LAS u32x4*)(lds + L_T + dl * 128 + ((jj ^ ((dl >> 3) & 7)) << 4));
        __builtin_amdgcn_raw_buffer_store_b128(val, rkv, (unsigned)(((80 * tdi + dl) * S + (1984 - 64 * tsi) + 8 * jj) * 2), 0, SC1);
      }
    }
  }
  const int n = lane & 15, g = lane >> 4;
  {
    const int ws_ = wave & 1, wf = wave >> 1;
    const int q = n >> 2, p = lane & 3;
    const int xb0 = XB + (64 * ws_ + n) * 128 + ((g ^ (n >> 1)) << 4);
    int wb_[3];
#pragma unroll
    for (int c = 0; c < 3; ++c) wb_[c] = GB + (8 * g + q) * 192 + 8 * p + 32 * ((3 * wf + c) ^ (g & 1));
    auto read_piece = [&](Frags& f, int i, lptr b, int kk) {
      if (i < 4) {
        f.x[i] = lds_frag(b + (xb0 ^ (kk << 6)) + i * 2048);
      } else {
        const int c = (i - 4) >> 2, u = ((i - 4) >> 1) & 1, t = (i - 4) & 1;
        const s16x4 v = lds_tr(b + wb_[c] + kk * (32 * 192) + u * (UB - GB) + t * (4 * 192));
        if (u == 0) f.g[c][t] = v; else f.u[c][t] = v;
      }
    };
    auto mma_piece = [&](const Frags& f, int i) {
      const int c = i >> 3, t = (i >> 1) & 3;
      if ((i & 1) == 0) ag[t][c] = MFMA16(join(f.g[c][0], f.g[c][1]), f.x[t], ag[t][c]);
      else au[t][c] = MFMA16(join(f.u[c][0], f.u[c][1]), f.x[t], au[t][c]);
    };
    auto half = [&](const Frags& cur, Frags& nxt, lptr nb, int nkk, bool rd, auto&& hook) {
#pragma unroll
      for (int i = 0; i < 24; ++i) {
        mma_piece(cur, i); FENCE();
        if (rd && i < 16) { read_piece(nxt, i, nb, nkk); FENCE(); }
        hook(i);
      }
    };
    auto nohook = [](int) {};
    const unsigned cons_addr = lds0 + L_SYNC + 4 * (SY_CONSUMED + wave);
    BARRIER();
    PROBE(1);
    STAMP(3);
    STAMPC(28);
    __builtin_amdgcn_s_setprio(M_PRIO);
    Frags f0, f1;
#pragma unroll
    for (int i = 0; i < 16; ++i) read_piece(f0, i, lds, 0);
    FENCE();
    STAMPC(27);
    int buf = 0;
#pragma unroll 1
    for (int s = 0; s < 9; ++s) {
      const int nbuf = (buf == 2) ? 0 : buf + 1;
      FENCE();
      half(f0, f1, lds + buf * STAGE, 1, true, nohook);
      if (s == SIDE_PUB_ITER) {
        WAIT_VM(0);
        if (sy_arrive(lds, SY_SIDE, lane) == 3u) {
          const unsigned tag = get_tag(lds);
          if (lane == 0) flag_store(a.ws, WS_FLAGKV + 4 * (32 * (bid & 7) + (bid >> 3)), tag);
        }
      }
      WAIT_LGKM0();
      BARRIER();
      FENCE();
      half(f1, f0, lds + nbuf * STAGE, 0, true, nohook);
      buf = nbuf;
    }
    half(f0, f1, lds + buf * STAGE, 1, true, nohook);
    half(f1, f0, lds, 0, false, nohook);
    PROBE(2);
    STAMP(4);
    STAMPC(29);

    const __amdgpu_buffer_rsrc_t rhs = make_rsrc(a.ws + WS_HT, S * F * 2);
#pragma unroll
    for (int t = 0; t < 4; ++t) {
      u32x2 pk[3];
#pragma unroll
      for (int c = 0; c < 3; ++c) {
        float h[4];
#pragma unroll
        for (int r = 0; r < 4; ++r) h[r] = sigmoidf_(ag[t][c][r]) * au[t][c][r];
        pk[c] = (u32x2){pack_bf16(h[0], h[1]), pack_bf16(h[2], h[3])};
      }
      const unsigned base = (unsigned)(((8 * mi + 4 * ws_ + t) * 48 + 3 * fi) * 1024 + lane * 16);
      if (wf == 0) {
        __builtin_amdgcn_raw_buffer_store_b128((u32x4){pk[0][0], pk[0][1], pk[1][0], pk[1][1]}, rhs, base, 0, SC1);
        __builtin_amdgcn_raw_buffer_store_b64(pk[2], rhs, base + 1024, 0, SC1);
      } else {
        __builtin_amdgcn_raw_buffer_store_b64(pk[0], rhs, base + 1024 + 8, 0, SC1);
        __builtin_amdgcn_raw_buffer_store_b128((u32x4){pk[1][0], pk[1][1], pk[2][0], pk[2][1]}, rhs, base + 2048, 0, SC1);
      }
    }
  }
  STAMP(5);

  const __amdgpu_buffer_rsrc_t rh = make_rsrc(a.ws + WS_HT, S * F * 2);
  const __amdgpu_buffer_rsrc_t rw = make_rsrc(a.wd, F * D * 2);
  const unsigned wr0 = wave == 0 ? wring_off(0) : wave == 1 ? wring_off(1) : wave == 2 ? wring_off(2) : wring_off(3);
  const int rot = 3 * (j >> 3);
  auto kstep = [&](int b) { const int t = b + rot; return 12 * wave + (t >= 12 ? t - 12 : t); };
  unsigned vwd[5];
#pragma unroll
  for (int i = 0; i < 5; ++i) {
    const int slot = 64 * i + lane, row = slot / 10, ch = slot - 10 * row;
    vwd[i] = (unsigned)(row * (D * 2) + 160 * di + ch * 16 - 1024 * (i & 3));
  }
  auto load_w = [&](int b) {
    const unsigned dst = lds0 + wr0 + (b & 3) * WSLOT, so = (unsigned)kstep(b) * (32 * D * 2);
    dma16xN<4>(rw, dst, vwd, so);
    dma16xN<1>(rw, dst + 4096, vwd + 4, so);
  };
  auto load_w1 = [&](int b, int i) {
    dma16(rw, lds0 + wr0 + (b & 3) * WSLOT + i * 1024, vwd[i] + 1024 * (i & 3), (unsigned)kstep(b) * (32 * D * 2));
  };
  struct HS4 { u32x4 f[4]; };
  HS4 H[HD + 1];
  const unsigned voff = lane * 16;
  auto load_h = [&](HS4& h, int b) {
    const unsigned so = (unsigned)(4 * si * 48 + kstep(b)) * 1024;
#define HPOL " sc1"
    asm volatile("buffer_load_dwordx4 %0, %4, %5, %6 offen" HPOL "\n\tbuffer_load_dwordx4 %1, %4, %5, %7 offen" HPOL "\n\t"
                 "buffer_load_dwordx4 %2, %4, %5, %8 offen" HPOL "\n\tbuffer_load_dwordx4 %3, %4, %5, %9 offen" HPOL
                 : "=&v"(h.f[0]), "=&v"(h.f[1]), "=&v"(h.f[2]), "=&v"(h.f[3])
                 : "v"(voff), "s"(rh), "s"(so), "s"(so + 48 * 1024), "s"(so + 96 * 1024), "s"(so + 144 * 1024) : "memory");
  };
  auto load_h1 = [&](HS4& h, int b, int i) {
    const unsigned so = (unsigned)((4 * si + i) * 48 + kstep(b)) * 1024;
    asm volatile("buffer_load_dwordx4 %0, %1, %2, %3 offen" HPOL : "=&v"(h.f[i]) : "v"(voff), "s"(rh), "s"(so) : "memory");
  };
  const int srow = 64 * si + 16 * wave + n;
  u32x2 qv[5];
  FENCE();
  __builtin_amdgcn_s_setprio(3);
  sy_arrive(lds, SY_EPI, lane);
  STAMP(22);
  WAIT_VM(0);
  if (sy_arrive(lds, SY_HDONE, lane) == 3u) {
    sy_wait(lds, SY_GATE, 1u);
    const unsigned tag = get_tag(lds);
    if (lane == 0) flag_store(a.ws, WS_FLAGH + 4 * (16 * mi + fi), tag);
  }
  STAMP(23);
  FENCE();
  sy_wait(lds, SY_EPI, 4u);
  load_w(0); load_w(1); load_w(2); load_w(3);
  f32x4 acc[4][5];
#pragma unroll
  for (int t = 0; t < 4; ++t)
#pragma unroll
    for (int c = 0; c < 5; ++c) { acc[t][c] = (f32x4){0.f, 0.f, 0.f, 0.f}; asm volatile("" : "+v"(acc[t][c])); }
  struct WF { s16x4 f[5][2]; };
  WF w0, w1;
  const lptr wrd = lds + wr0 + (4 * g + (n >> 2)) * 160 + 8 * (lane & 3);
  auto read_w = [&](WF& w, int p, int b) {
    w.f[p >> 1][p & 1] = lds_tr(wrd + (b & 3) * WSLOT + (p & 1) * (16 * 160) + (p >> 1) * 32);
  };
  STAMP(24);
  WAIT_VM(15);
#pragma unroll
  for (int p = 0; p < 10; ++p) read_w(w0, p, 0);
  WAIT_LGKM0();
  load_w(4);
  sy_arrive(lds, SY_WPRE, lane);
  __builtin_amdgcn_s_setprio(G_PRIO);
  STAMP(6);
  sy_wait(lds, SY_HREADY, 1u);
  PROBE(3);
  STAMP(7);
#pragma unroll
  for (int b = 0; b < HD; ++b) load_h(H[b], b);
  const float gate = __builtin_bit_cast(float, __hip_atomic_load((GAS unsigned*)(a.ws + WS_GATE) + srow, __ATOMIC_RELAXED, __HIP_MEMORY_SCOPE_AGENT));
  FENCE();
  auto gstep = [&](const WF& cur, WF& nxt, HS4& h, HS4& hn, int b) {
    asm volatile("s_waitcnt vmcnt(%4) lgkmcnt(0)" : "+v"(h.f[0]), "+v"(h.f[1]), "+v"(h.f[2]), "+v"(h.f[3]) : "n"(g_younger(b)) : "memory");
    FENCE();
#pragma unroll
    for (int i = 0; i < 20; ++i) {
      acc[i & 3][i >> 2] = MFMA16(join(cur.f[i >> 2][0], cur.f[i >> 2][1]), __builtin_bit_cast(bf16x8, h.f[i & 3]), acc[i & 3][i >> 2]);
      FENCE();
      if (i < 8 && (i & 1) == 0 && b + HD < 12) { load_h1(hn, b + HD, i >> 1); FENCE(); }
      if (i == 7 && b == 9) {
#pragma unroll
        for (int c = 0; c < 5; ++c) qv[c] = *(const u32x2*)(a.x + (size_t)srow * D + 80 * di + 16 * c + 4 * g);
        asm volatile("" ::: "memory");
        FENCE();
      }
      if (i < 10 && b + 1 < 12) { read_w(nxt, i, b + 1); FENCE(); }
      if (i >= 10 && (i & 1) == 0 && b >= 1 && b + 4 < 12) { load_w1(b + 4, (i - 10) >> 1); FENCE(); }
      if (b == YPOLL_STEP && i == 17 && wave == 0) { sy_post(lds0 + L_SYNC + 4 * SY_GSTEP, 1u); FENCE(); }
      if (b == YPOLL_STEP && i == 19 && wave == 0) { wake(); FENCE(); }
    }
  };
#pragma unroll
  for (int b = 0; b < 12; b += 2) {
    gstep(w0, w1, H[b % (HD + 1)], H[(b + HD) % (HD + 1)], b);
    if (b == 0) STAMPI(14);
    gstep(w1, w0, H[(b + 1) % (HD + 1)], H[(b + 1 + HD) % (HD + 1)], b + 1);
    STAMPI(15 + (b >> 1));
  }
  PROBE(4);
  STAMP(8);

  f32x4 bsum[5];
  {
#pragma unroll
    for (int t = 0; t < 4; ++t) {
      if (t != wave) {
#pragma unroll
        for (int c = 0; c < 5; ++c) *(LAS f32x4*)(lds + wr0 + (((t - wave - 1) & 3) * 5 + c) * 1024 + lane * 16) = acc[t][c];
      } else {
#pragma unroll
        for (int c = 0; c < 5; ++c) bsum[c] = acc[t][c];
      }
    }
    sy_arrive(lds, SY_RED, lane);
    sy_wait(lds, SY_RED, 4u);
#pragma unroll
    for (int w = 0; w < 4; ++w) {
      if (w != wave) {
#pragma unroll
        for (int c = 0; c < 5; ++c) bsum[c] += *(LAS f32x4*)(lds + wring_off(w) + (((wave - w - 1) & 3) * 5 + c) * 1024 + lane * 16);
      }
    }
  }
  STAMP(9);
  sy_wait(lds, SY_YREADY, 1u);
  STAMP(10);
  {
    const int q = n >> 2, p = lane & 3;
    const __amdgpu_buffer_rsrc_t ro = make_rsrc(a.out, S * D * 2);
    const lptr ystr = lds + (wave == 0 ? ystrip_off(0) : wave == 1 ? ystrip_off(1) : wave == 2 ? ystrip_off(2) : ystrip_off(3));
#pragma unroll
    for (int c = 0; c < 5; ++c) {
      const s16x4 yv = lds_tr(ystr + (16 * c + 4 * g + q) * 32 + 8 * p);
      const u32x2 y = __builtin_bit_cast(u32x2, yv);
      const float o0 = gate * (bf_lo(qv[c][0]) * bf_lo(y[0])) + (1.f - gate) * bsum[c][0];
      const float o1 = gate * (bf_hi(qv[c][0]) * bf_hi(y[0])) + (1.f - gate) * bsum[c][1];
      const float o2 = gate * (bf_lo(qv[c][1]) * bf_lo(y[1])) + (1.f - gate) * bsum[c][2];
      const float o3 = gate * (bf_hi(qv[c][1]) * bf_hi(y[1])) + (1.f - gate) * bsum[c][3];
      __builtin_amdgcn_raw_buffer_store_b64((u32x2){pack_bf16(o0, o1), pack_bf16(o2, o3)}, ro,
                                            (unsigned)((srow * D + 80 * di + 16 * c + 4 * g) * 2), 0, OUT_AUX);
    }
  }
  if (PROBE_GLOBAL) {
    unsigned long long r = PROBE_GLOBAL == 1 ? ~0ull : 0ull;
    for (int i = 0; i < 4; ++i) {
      const unsigned long long v = __hip_atomic_load((GAS unsigned long long*)(a.ws + (32u << 20)) + lane + 64 * i, __ATOMIC_RELAXED, __HIP_MEMORY_SCOPE_AGENT);
      r = PROBE_GLOBAL == 1 ? min(r, v) : max(r, v);
    }
    for (int m = 32; m >= 1; m >>= 1) { const unsigned long long o = __shfl_xor(r, m); r = PROBE_GLOBAL == 1 ? min(r, o) : max(r, o); }
    probe_t0 = __builtin_amdgcn_readfirstlane((unsigned)r) | ((unsigned long long)__builtin_amdgcn_readfirstlane((unsigned)(r >> 32)) << 32);
  }
  PROBE(5);
  STAMP(11);
}

}

extern "C" void kernel_launch(void* const* d_in, const int* in_sizes, int n_in, void* d_out, int out_size, void* d_ws,
                              size_t ws_size, hipStream_t stream) {
  (void)in_sizes; (void)n_in; (void)out_size; (void)ws_size;
  Args a;
  a.x = (const u16*)d_in[0];
  a.k = (const u16*)d_in[1];
  a.v = (const u16*)d_in[2];
  a.shk = (const u16*)d_in[3];
  a.ssmk = (const u16*)d_in[4];
  a.ssmd = (const u16*)d_in[5];
  a.wg = (const u16*)d_in[7];
  a.wu = (const u16*)d_in[8];
  a.wd = (const u16*)d_in[9];
  a.gw = (const u16*)d_in[10];
  a.ws = (char*)d_ws;
  a.out = (u16*)d_out;
  (void)hipFuncSetAttribute((const void*)fused_kernel, hipFuncAttributeMaxDynamicSharedMemorySize, LDS_BYTES);
  fused_kernel<<<256, 512, LDS_BYTES, stream>>>(a);
}
```

```cpp
#include <hip/hip_runtime.h>
#include <hip/hip_bf16.h>

namespace {

typedef unsigned short u16;
typedef __bf16 bf16x8 __attribute__((ext_vector_type(8)));
typedef __bf16 bf16x2 __attribute__((ext_vector_type(2)));
typedef short s16x4 __attribute__((ext_vector_type(4)));
typedef float f32x2 __attribute__((ext_vector_type(2)));
typedef float f32x4 __attribute__((ext_vector_type(4)));
typedef unsigned u32x2 __attribute__((ext_vector_type(2)));
typedef unsigned u32x4 __attribute__((ext_vector_type(4)));
#define LAS __attribute__((address_space(3)))
#define GAS __attribute__((address_space(1)))
typedef LAS char* lptr;

constexpr int S = 2048, D = 640, F = 1536;
constexpr int SC1 = 16;

constexpr size_t WS_HT = 0;
constexpr size_t WS_KVR = WS_HT + (size_t)S * F * 2;
constexpr size_t WS_Y2T = WS_KVR + (size_t)S * D * 2;
constexpr size_t WS_GATE = WS_Y2T + (size_t)S * D * 2;
constexpr size_t WS_FLAGKV = WS_GATE + (size_t)S * 4;
constexpr size_t WS_FLAGH = WS_FLAGKV + 1024;
constexpr size_t WS_FLAGY = WS_FLAGH + 1024;
constexpr size_t WS_EPOCH = WS_FLAGY + 1024;
constexpr size_t WS_END = WS_EPOCH + 256;

constexpr int STAGE = 40960, XB = 0, GB = 16384, UB = 28672;
constexpr int L_AUX = 3 * STAGE;
constexpr int L_T = L_AUX, UR_BYTES = 4608;
constexpr int L_FREE = L_AUX + 3 * UR_BYTES;
constexpr int L_SYNC = L_FREE + 20480 + 2 * 2560;
constexpr int L_DUMMY = L_SYNC + 128;
constexpr int L_END = L_DUMMY + 256;
constexpr int KC_COPY = 4160, KC_BYTES = 4 * KC_COPY;
constexpr int WSLOT = 5120;
__device__ constexpr int wring_off(int w) { return w == 0 ? 0 : w == 1 ? 20480 : w == 2 ? 2 * STAGE + KC_BYTES : L_FREE; }
__device__ constexpr int ystrip_off(int w) { return w < 2 ? STAGE + 2 * KC_BYTES + w * 2560 : L_FREE + 20480 + (w - 2) * 2560; }
static_assert(2 * STAGE + KC_BYTES + 20480 <= 3 * STAGE && STAGE + 2 * KC_BYTES + 2 * 2560 <= 2 * STAGE, "second-phase LDS map");
enum { SY_LANDED = 0, SY_CONSUMED = 4, SY_KV = 8, SY_SIDE = 9, SY_HDONE = 10, SY_RED = 11, SY_CONV = 12, SY_AREADY = 13,
       SY_HREADY = 14, SY_YREADY = 15, SY_TAG = 16, SY_TAGOK = 17, SY_GSTEP = 18, SY_GATE = 19, SY_EPI = 20, SY_WPRE = 21 };

#define STAMP(i) do {} while (0)
#define STAMPC(i) do {} while (0)
#define STAMPI(i) do {} while (0)
#define STAMP_INIT() do {} while (0)
#define STAMP_DUMP() do {} while (0)
constexpr int LDS_BYTES = L_END;

#define PROBE_AT 0
#define PROBE_US 0
#define PROBE(k) do { if (PROBE_AT == (k)) { while (__builtin_amdgcn_s_memrealtime() - probe_t0 < 100ull * PROBE_US) __builtin_amdgcn_s_sleep(1); } } while (0)
#define PROBE_GLOBAL 0
#define MFMA16(a, b, c) __builtin_amdgcn_mfma_f32_16x16x32_bf16((a), (b), (c), 0, 0, 0)
#define WAIT_VM(n) asm volatile("s_waitcnt vmcnt(" #n ")" ::: "memory")
#define WAIT_LGKM0() asm volatile("s_waitcnt lgkmcnt(0)" ::: "memory")
#define BARRIER() do { asm volatile("" ::: "memory"); __builtin_amdgcn_s_barrier(); asm volatile("" ::: "memory"); } while (0)
#define FENCE() __builtin_amdgcn_sched_barrier(0)
constexpr unsigned SPIN_LDS = 1u << 22, SPIN_MEM = 1u << 16;

__device__ __forceinline__ unsigned pack_bf16(float lo, float hi) {
  f32x2 v = {lo, hi};
  return __builtin_bit_cast(unsigned, __builtin_convertvector(v, bf16x2));
}
__device__ __forceinline__ float bf_lo(unsigned w) { return __builtin_bit_cast(float, w << 16); }
__device__ __forceinline__ float bf_hi(unsigned w) { return __builtin_bit_cast(float, w & 0xffff0000u); }
__device__ __forceinline__ float sigmoidf_(float x) {
  return __builtin_amdgcn_rcpf(1.0f + __builtin_amdgcn_exp2f(x * -1.4426950408889634f));
}
__device__ __forceinline__ __amdgpu_buffer_rsrc_t make_rsrc(const void* p, unsigned bytes) {
  return __builtin_amdgcn_make_buffer_rsrc(const_cast<void*>(p), 0, bytes, 0x00020000);
}
__device__ __forceinline__ unsigned lds_addr(lptr p) { return (unsigned)(size_t)p; }
__device__ __forceinline__ void dma16(__amdgpu_buffer_rsrc_t r, unsigned dst, unsigned voff, unsigned soff) {
  asm volatile("s_mov_b32 m0, %0\n\ts_nop 0\n\tbuffer_load_dwordx4 %1, %2, %3 offen lds"
               :: "s"(dst), "v"(voff), "s"(r), "s"(soff) : "memory");
}
template <int N>
__device__ __forceinline__ void dma16xN(__amdgpu_buffer_rsrc_t r, unsigned dst, const unsigned* v, unsigned soff) {
  static_assert(N >= 1 && N <= 4, "12-bit instruction offset");
  if constexpr (N == 1)
    asm volatile("s_mov_b32 m0, %0\n\ts_nop 0\n\tbuffer_load_dwordx4 %1, %2, %3 offen lds"
                 :: "s"(dst), "v"(v[0]), "s"(r), "s"(soff) : "memory");
  else if constexpr (N == 2)
    asm volatile("s_mov_b32 m0, %0\n\ts_nop 0\n\tbuffer_load_dwordx4 %1, %3, %4 offen lds\n\t"
                 "buffer_load_dwordx4 %2, %3, %4 offen offset:1024 lds"
                 :: "s"(dst), "v"(v[0]), "v"(v[1]), "s"(r), "s"(soff) : "memory");
  else if constexpr (N == 3)
    asm volatile("s_mov_b32 m0, %0\n\ts_nop 0\n\tbuffer_load_dwordx4 %1, %4, %5 offen lds\n\t"
                 "buffer_load_dwordx4 %2, %4, %5 offen offset:1024 lds\n\tbuffer_load_dwordx4 %3, %4, %5 offen offset:2048 lds"
                 :: "s"(dst), "v"(v[0]), "v"(v[1]), "v"(v[2]), "s"(r), "s"(soff) : "memory");
  else
    asm volatile("s_mov_b32 m0, %0\n\ts_nop 0\n\tbuffer_load_dwordx4 %1, %5, %6 offen lds\n\t"
                 "buffer_load_dwordx4 %2, %5, %6 offen offset:1024 lds\n\tbuffer_load_dwordx4 %3, %5, %6 offen offset:2048 lds\n\t"
                 "buffer_load_dwordx4 %4, %5, %6 offen offset:3072 lds"
                 :: "s"(dst), "v"(v[0]), "v"(v[1]), "v"(v[2]), "v"(v[3]), "s"(r), "s"(soff) : "memory");
}
__device__ __forceinline__ void touch(const void* p, unsigned dummy_lds) {
  asm volatile("s_mov_b32 m0, %0\n\ts_nop 0\n\tglobal_load_lds_dword %1, off" :: "s"(dummy_lds), "v"(p) : "memory");
}
__device__ __forceinline__ s16x4 lds_tr(lptr p) { return __builtin_amdgcn_ds_read_tr16_b64_v4i16((LAS s16x4*)p); }
__device__ __forceinline__ bf16x8 join(s16x4 a, s16x4 b) {
  return __builtin_bit_cast(bf16x8, __builtin_shufflevector(a, b, 0, 1, 2, 3, 4, 5, 6, 7));
}
__device__ __forceinline__ bf16x8 lds_frag(lptr p) { return *(LAS bf16x8*)p; }
__device__ __forceinline__ u32x4 ld128(__amdgpu_buffer_rsrc_t r, unsigned voff, unsigned soff, int aux) {
  return aux ? __builtin_bit_cast(u32x4, __builtin_amdgcn_raw_buffer_load_b128(r, voff, soff, SC1))
             : __builtin_bit_cast(u32x4, __builtin_amdgcn_raw_buffer_load_b128(r, voff, soff, 0));
}

__device__ __forceinline__ LAS unsigned* sy(lptr lds, int i) { return (LAS unsigned*)(lds + L_SYNC) + i; }
__device__ __forceinline__ unsigned sy_get(lptr lds, int i) {
  return __builtin_amdgcn_readfirstlane(__hip_atomic_load(sy(lds, i), __ATOMIC_RELAXED, __HIP_MEMORY_SCOPE_WORKGROUP));
}
__device__ __forceinline__ void wake() { asm volatile("s_wakeup" ::: "memory"); }
__device__ __forceinline__ void sy_set(lptr lds, int i, unsigned v, int lane) {
  if (lane == 0) __hip_atomic_store(sy(lds, i), v, __ATOMIC_RELAXED, __HIP_MEMORY_SCOPE_WORKGROUP);
  asm volatile("s_waitcnt lgkmcnt(0)\n\ts_wakeup" ::: "memory");
}
#define TOUCH_H 0
#define LOADER_POST_FIRST 1
#define HPOLL_SLEEP 4
#define YPOLL_SLEEP 8
#define YPOLL_STEP 8
#ifndef M_PRIO
#define M_PRIO 2
#endif
#define G_PRIO 0
#define ROT1 1
#define DMA_SPACE 0
#define C_PRIO 2
#define G_HDEPTH 4
#define OUT_AUX 0
#define H_ACQUIRE 0
#define CODE_LINES 0
#define CODE_WAVE 7
#define LAND_RD 18
#define TOUCH_DELAY 8
#define TOUCH1 0
#define FILT_ITER 6
#define W7_DELAY 0
#define POLL_AFTER 8
#define LOADER_PRIO 0
#define SLEEP_LDS 4
__device__ __forceinline__ void sy_post(unsigned addr, unsigned v) {
  asm volatile("s_mov_b64 exec, 1\n\tds_write_b32 %0, %1\n\ts_mov_b64 exec, -1" :: "v"(addr), "v"(v) : "memory");
}
__device__ __forceinline__ void sy_wait(lptr lds, int i, unsigned v) {
#pragma unroll 1
  for (unsigned n = 0; n < SPIN_LDS; ++n) {
    if (sy_get(lds, i) >= v) break;
    __builtin_amdgcn_s_sleep(SLEEP_LDS);
  }
  asm volatile("" ::: "memory");
}
__device__ __forceinline__ unsigned sy_arrive(lptr lds, int i, int lane) {
  WAIT_LGKM0();
  unsigned old = 0;
  if (lane == 0) old = __hip_atomic_fetch_add(sy(lds, i), 1u, __ATOMIC_RELAXED, __HIP_MEMORY_SCOPE_WORKGROUP);
  old = __builtin_amdgcn_readfirstlane(old);
  wake();
  return old;
}
__device__ __forceinline__ unsigned min4(u32x4 v) { return min(min(v[0], v[1]), min(v[2], v[3])); }
__device__ __forceinline__ u32x4 sy_get4(lptr lds, int i) { return *(volatile LAS u32x4*)(lds + L_SYNC + 4 * i); }
__device__ __forceinline__ u32x2 sy_get2(lptr lds, int i) { return *(volatile LAS u32x2*)(lds + L_SYNC + 4 * i); }
__device__ __forceinline__ void sy_wait4(lptr lds, int i, unsigned v) {
#pragma unroll 1
  for (unsigned n = 0; n < SPIN_LDS; ++n) {
    if ((unsigned)__builtin_amdgcn_readfirstlane(min4(sy_get4(lds, i))) >= v) break;
    __builtin_amdgcn_s_sleep(SLEEP_LDS);
  }
  asm volatile("" ::: "memory");
}
#define LOADER_SLEEP 1
__device__ __forceinline__ void loader_wait(lptr lds, unsigned v) {
#pragma unroll 1
  for (unsigned n = 0; n < SPIN_LDS; ++n) {
    if ((unsigned)__builtin_amdgcn_readfirstlane(min4(sy_get4(lds, SY_CONSUMED))) >= v) break;
    if (LOADER_SLEEP) __builtin_amdgcn_s_sleep(LOADER_SLEEP);
  }
  asm volatile("" ::: "memory");
}
__device__ __forceinline__ void sy_wait4m(lptr lds, unsigned v) {
#pragma unroll 1
  for (unsigned n = 0; n < SPIN_LDS; ++n) {
    const u32x4 l = sy_get4(lds, SY_LANDED);
    if ((unsigned)__builtin_amdgcn_readfirstlane(min(min(l[0], l[1]), l[2])) >= v) break;
    __builtin_amdgcn_s_sleep(SLEEP_LDS);
  }
  asm volatile("" ::: "memory");
}
__device__ __forceinline__ unsigned get_tag(lptr lds) {
  sy_wait(lds, SY_TAGOK, 1u);
  return sy_get(lds, SY_TAG);
}
__device__ __forceinline__ void flag_store(char* ws, size_t off, unsigned tag) {
  __hip_atomic_store((GAS unsigned*)(ws + off), tag, __ATOMIC_RELAXED, __HIP_MEMORY_SCOPE_AGENT);
}
#define SIDE_PUB_ITER 3
#define POLL_SLEEP 32
template <int SLEEP = POLL_SLEEP>
__device__ __forceinline__ void flag_wait(char* ws, size_t off, bool on, unsigned tag) {
#pragma unroll 1
  for (unsigned n = 0; n < SPIN_MEM; ++n) {
    const unsigned v = __hip_atomic_load((GAS unsigned*)(ws + off), __ATOMIC_RELAXED, __HIP_MEMORY_SCOPE_AGENT);
    if (__all(!on || v == tag)) break;
    __builtin_amdgcn_s_sleep(SLEEP);
  }
  asm volatile("" ::: "memory");
}

struct Args {
  const u16* x; const u16* k; const u16* v; const u16* shk; const u16* ssmk; const u16* ssmd;
  const u16* wg; const u16* wu; const u16* wd; const u16* gw;
  char* ws; u16* out;
};

struct Frags { bf16x8 x[4]; s16x4 g[3][2]; s16x4 u[3][2]; };

__device__ constexpr int tri_start(int t) { return 8 * t - (t * (t - 1)) / 2; }
__device__ constexpr int tri_row(int p) {
  return p < 8 ? 0 : p < 15 ? 1 : p < 21 ? 2 : p < 26 ? 3 : p < 30 ? 4 : p < 33 ? 5 : p < 35 ? 6 : 7;
}

template <int N> __device__ __forceinline__ void wait_vm() { asm volatile("s_waitcnt vmcnt(%0)" ::"n"(N) : "memory"); }

struct Filter { unsigned d[5][6]; unsigned dbits; };
__device__ __forceinline__ void load_filter(const Args& a, Filter& f, int ch, int lane) {
  const __amdgpu_buffer_rsrc_t rk = make_rsrc(a.ssmk + (size_t)ch * S, S * 2);
#pragma unroll
  for (int it = 0; it < 5; ++it) {
    const unsigned off = (unsigned)(16 * (lane + 64 * it) - 32);
    const u32x4 v0 = __builtin_bit_cast(u32x4, __builtin_amdgcn_raw_buffer_load_b128(rk, off, 0, 0));
    const u32x2 v1 = __builtin_bit_cast(u32x2, __builtin_amdgcn_raw_buffer_load_b64(rk, off + 16, 0, 0));
    f.d[it][0] = v0[0]; f.d[it][1] = v0[1]; f.d[it][2] = v0[2]; f.d[it][3] = v0[3]; f.d[it][4] = v1[0]; f.d[it][5] = v1[1];
  }
  f.dbits = a.ssmd[ch];
}

template <int NX>
__device__ __forceinline__ void loader(const Args& a, lptr lds, int lw, int x0, int mi, int fi, int rot, Filter& flt, int ch, int lane) {
  const unsigned lds0 = lds_addr(lds);
  const __amdgpu_buffer_rsrc_t rx = make_rsrc(a.x, S * D * 2);
  const __amdgpu_buffer_rsrc_t rg = make_rsrc(a.wg, D * F * 2);
  const __amdgpu_buffer_rsrc_t ru = make_rsrc(a.wu, D * F * 2);
  unsigned vx[NX], vw[4];
#pragma unroll
  for (int i = 0; i < NX; ++i) {
    const int row = 8 * (x0 + i) + (lane >> 3), c = (lane & 7) ^ ((row >> 1) & 7);
    vx[i] = (unsigned)((128 * mi + row) * (D * 2) + c * 16 - 1024 * (i & 3));
  }
#pragma unroll
  for (int i = 0; i < 4; ++i) {
    const int slot = 64 * (4 * lw + i) + lane, k = slot / 12, chp = slot - 12 * k, c = chp ^ (2 * ((k >> 3) & 1));
    vw[i] = (unsigned)(k * (F * 2) + fi * 192 + c * 16 - 1024 * i);
  }
  const unsigned land_addr = lds0 + L_SYNC + 4 * (SY_LANDED + lw);
  auto slice = [&](int s) { const int k = s + rot; return k >= 10 ? k - 10 : k; };
  auto part_a = [&](int s) {
    const unsigned b = lds0 + (s % 3) * STAGE, so = (unsigned)slice(s) * (64 * F * 2);
    dma16xN<4>(rg, b + GB + 4 * lw * 1024, vw, so);
    if (DMA_SPACE) __builtin_amdgcn_s_sleep(DMA_SPACE);
    dma16xN<4>(ru, b + UB + 4 * lw * 1024, vw, so);
    if (DMA_SPACE) __builtin_amdgcn_s_sleep(DMA_SPACE);
  };
  auto part_b = [&](int s) {
    const unsigned b = lds0 + (s % 3) * STAGE, so = (unsigned)slice(s) * 128;
    dma16xN<4>(rx, b + XB + x0 * 1024, vx, so);
    if (DMA_SPACE) __builtin_amdgcn_s_sleep(DMA_SPACE);
    dma16xN<NX - 4>(rx, b + XB + (x0 + 4) * 1024, vx + 4, so);
  };
  constexpr int P = NX + 8;
  part_a(0); part_b(0);
  part_a(1); part_b(1);
  STAMP(1);
  wait_vm<P>();
  STAMP(2);
  BARRIER();
  part_a(2); part_b(2);
#pragma unroll
  for (int s = 0; s < 9; ++s) {
    if (s == 8) wait_vm<11>(); else wait_vm<P>();
    STAMPI(8 + 2 * s);
    BARRIER();
    STAMPI(9 + 2 * s);
    if (s < 7) { part_a(s + 3); part_b(s + 3); }
    if (s == 7) { FENCE(); load_filter(a, flt, ch, lane); FENCE(); asm volatile("" ::: "memory"); }
  }
}

struct KvTask { u32x4 kr[7], vr[4], wk[4]; };
__device__ __forceinline__ void kv_load(const Args& a, KvTask& t, int task, int tsi, int tdi) {
  const int dc = task % 10, sb = task / 10;
  const int s0 = 64 * tsi + 4 * sb, d0 = 80 * tdi + 8 * dc;
#pragma unroll
  for (int r = 0; r < 7; ++r) {
    const int s = s0 - 3 + r;
    t.kr[r] = *(const u32x4*)(a.k + (size_t)(s < 0 ? 0 : s) * D + d0);
  }
#pragma unroll
  for (int r = 0; r < 4; ++r) t.vr[r] = *(const u32x4*)(a.v + (size_t)(s0 + r) * D + d0);
#pragma unroll
  for (int i = 0; i < 4; ++i) t.wk[i] = *(const u32x4*)(a.shk + (size_t)d0 * 4 + i * 8);
}
__device__ __forceinline__ float dot2(unsigned x, unsigned y, float c) {
  return __builtin_amdgcn_fdot2_f32_bf16(__builtin_bit_cast(bf16x2, x), __builtin_bit_cast(bf16x2, y), c, false);
}
__device__ __forceinline__ float dot2z(unsigned x, unsigned y) {
  float r;
  asm("v_dot2_f32_bf16 %0, %1, %2, 0" : "=v"(r) : "v"(x), "v"(y));
  return r;
}
__device__ __forceinline__ void kv_compute(KvTask& t, lptr lds, int task, int tsi) {
  const int dc = task % 10, sb = task / 10;
  if (tsi == 0 && sb == 0) {
#pragma unroll
    for (int r = 0; r < 3; ++r) t.kr[r] = (u32x4){0u, 0u, 0u, 0u};
  }
  const int sg = 15 - sb;
  lptr dst = lds + L_T + 8 * dc * 128 + (((sg >> 1) ^ (dc & 7)) << 4) + ((sg & 1) << 3);
#pragma unroll
  for (int c = 0; c < 8; ++c) {
    const unsigned w01 = t.wk[c >> 1][2 * (c & 1)], w23 = t.wk[c >> 1][2 * (c & 1) + 1];
    const unsigned sel = (c & 1) ? 0x03020706u : 0x01000504u;
    unsigned pr[6];
#pragma unroll
    for (int i = 0; i < 6; ++i) pr[i] = __builtin_amdgcn_perm(t.kr[i + 1][c >> 1], t.kr[i][c >> 1], sel);
    float o[4];
#pragma unroll
    for (int r = 0; r < 4; ++r) {
      const float ks = dot2(pr[r + 2], w01, dot2z(pr[r], w23));
      o[r] = ks * ((c & 1) ? bf_hi(t.vr[r][c >> 1]) : bf_lo(t.vr[r][c >> 1]));
    }
    *(LAS u32x2*)(dst + c * 128) = (u32x2){pack_bf16(o[3], o[2]), pack_bf16(o[1], o[0])};
  }
}

__device__ __forceinline__ void conv_wave(const Args& a, lptr lds, lptr kc, lptr ur, const Filter& flt, int ch, int nconv, int lane) {
#pragma unroll
  for (int it = 0; it < 5; ++it) {
    const int c = lane + 64 * it;
    if (c < 260) {
      const unsigned a0 = flt.d[it][0], a1 = flt.d[it][1], a2 = flt.d[it][2], a3 = flt.d[it][3], a4 = flt.d[it][4], a5 = flt.d[it][5];
      const unsigned e0 = __builtin_amdgcn_alignbit(a1, a0, 16), e1 = __builtin_amdgcn_alignbit(a2, a1, 16),
                     e2 = __builtin_amdgcn_alignbit(a3, a2, 16), e3 = __builtin_amdgcn_alignbit(a4, a3, 16),
                     e4 = __builtin_amdgcn_alignbit(a5, a4, 16);
      lptr w = kc + 16 * c;
      *(LAS u32x4*)(w) = (u32x4){a0, a1, a2, a3};
      *(LAS u32x4*)(w + KC_COPY) = (u32x4){e0, e1, e2, e3};
      *(LAS u32x4*)(w + 2 * KC_COPY) = (u32x4){a1, a2, a3, a4};
      *(LAS u32x4*)(w + 3 * KC_COPY) = (u32x4){e1, e2, e3, e4};
    }
  }
  const float dcoef = bf_lo(flt.dbits);
  WAIT_LGKM0();
  sy_wait(lds, SY_AREADY, 1u);
  sy_wait(lds, SY_EPI, 4u);
  __builtin_amdgcn_s_setprio(C_PRIO);
  STAMP(4);
  STAMPC(28);

  const int m = lane & 15, g = lane >> 4;
  const int rho = (m + 1) & 3;
  lptr abase = kc + rho * KC_COPY + 2 * (8 * g + m - 31 - rho);
  lptr bbase = ur + 2 * (2000 - 16 * m + 8 * g) - 7 * 512;
  f32x4 acc[8];
#pragma unroll
  for (int t = 0; t < 8; ++t) acc[t] = (f32x4){0.f, 0.f, 0.f, 0.f};
  struct CF { s16x4 a[8][2]; bf16x8 b[8]; };
  auto cread = [&](CF& f, int p, int i) {
    if (p < 16) f.a[p >> 1][p & 1] = *(LAS s16x4*)(abase + 64 * i + 512 * (p >> 1) + 8 * (p & 1));
    else f.b[p - 16] = lds_frag(bbase + 64 * i + 512 * (7 - (p - 16)));
  };
  auto cmma = [&](const CF& f, int p) {
    const int t = tri_row(p), u = p - tri_start(t);
    acc[t + u] = MFMA16(join(f.a[t][0], f.a[t][1]), f.b[u], acc[t + u]);
  };
  auto residue = [&](const CF& cur, CF& nxt, int inext) {
#pragma unroll
    for (int p = 0; p < 36; ++p) {
      cmma(cur, p); FENCE();
      if (p < 24) { cread(nxt, p, inext); FENCE(); }
    }
  };
  CF c0, c1;
#pragma unroll
  for (int p = 0; p < 24; ++p) cread(c0, p, 1);
#pragma unroll 1
  for (int i = 1; i <= 8; i += 2) {
    FENCE();
    residue(c0, c1, i + 1);
    residue(c1, c0, (i + 2 <= 8) ? i + 2 : 8);
  }
  STAMP(5);
  STAMPC(29);
  const __amdgpu_buffer_rsrc_t ry = make_rsrc(a.ws + WS_Y2T + (size_t)ch * S * 2, S * 2);
#pragma unroll
  for (int t = 0; t < 8; t += 2) {
    unsigned pk[2][2];
#pragma unroll
    for (int e = 0; e < 2; ++e) {
      const int s = 256 * (t + e) + 16 * m + 4 * g;
      const u32x2 kv = *(LAS u32x2*)(ur + 2 * (2044 - s));
      pk[e][0] = pack_bf16(acc[t + e][0] + bf_hi(kv[1]) * dcoef, acc[t + e][1] + bf_lo(kv[1]) * dcoef);
      pk[e][1] = pack_bf16(acc[t + e][2] + bf_hi(kv[0]) * dcoef, acc[t + e][3] + bf_lo(kv[0]) * dcoef);
    }
    const auto r0 = __builtin_amdgcn_permlane16_swap(pk[0][0], pk[1][0], false, false);
    const auto r1 = __builtin_amdgcn_permlane16_swap(pk[0][1], pk[1][1], false, false);
    const u32x4 o = {r0[0], r1[0], r0[1], r1[1]};
    const unsigned off = 2 * (256 * (t + (g & 1)) + 16 * m + 8 * (g >> 1));
    __builtin_amdgcn_raw_buffer_store_b128(o, ry, off, 0, SC1);
  }
  STAMP(6);
  WAIT_VM(0);
  STAMP(7);
  if (sy_arrive(lds, SY_CONV, lane) == (unsigned)(nconv - 1)) {
    const unsigned tag = get_tag(lds);
    if (lane == 0) flag_store(a.ws, WS_FLAGY + 4 * blockIdx.x, tag);
  }
}

constexpr int HD = G_HDEPTH;
__device__ constexpr int g_younger(int b) {
  int count = 0, last = 0;
  for (int k = 0; k < HD; ++k) { count += 4; if (k == b) last = count; }
  count += 1;
  for (int s = 0; s < b; ++s) {
    if (s + HD <= 11) { count += 4; if (s + HD == b) last = count; }
    if (s == 9) count += 5;
    if (s >= 2 && s <= 7) { count += 5; if (s + 4 == b + 1) last = count; }
  }
  return count - last;
}
static_assert(G_HDEPTH != 3 || (g_younger(0) == 9 && g_younger(1) == 9 && g_younger(2) == 14 && g_younger(3) == 18 &&
              g_younger(8) == 18 && g_younger(9) == 13 && g_younger(10) == 9 && g_younger(11) == 5), "wait table");
__global__ __launch_bounds__(512, 2) void fused_kernel(Args a) {
  extern __shared__ __attribute__((aligned(16))) char smem[];
  lptr lds = (lptr)smem;
  const int tid = threadIdx.x, lane = tid & 63;
  const int wave = __builtin_amdgcn_readfirstlane(tid >> 6);
  const int bid = blockIdx.x;
  const int xcd = bid & 7, j = bid >> 3;
  const int mi = 8 * (xcd & 1) + (j & 7), fi = 4 * (xcd >> 1) + (j >> 3);
  const int si = 8 * (xcd & 3) + (j & 7), di = 4 * (xcd >> 2) + (j >> 3);
  const int c0 = (5 * bid) >> 1, nconv = 2 + (bid & 1);
  const unsigned lds0 = lds_addr(lds);
  STAMP_INIT();
  STAMP(0);

  if (wave == 7) {
    if (lane < 32) *sy(lds, lane) = 0u;
    WAIT_LGKM0();
  }
  BARRIER();

  if (wave == 7) {
    const unsigned epoch = __hip_atomic_load((GAS unsigned*)(a.ws + WS_EPOCH), __ATOMIC_RELAXED, __HIP_MEMORY_SCOPE_AGENT);
    {
      const int r0 = 128 * mi + 8 * fi, gr = lane >> 4, gq = lane & 15;
      u32x4 gx[2][5], gw[5];
#pragma unroll
      for (int i = 0; i < 5; ++i) {
        gw[i] = ((const u32x4*)a.gw)[gq + 16 * i];
        gx[0][i] = ((const u32x4*)(a.x + (size_t)(r0 + gr) * D))[gq + 16 * i];
        gx[1][i] = ((const u32x4*)(a.x + (size_t)(r0 + 4 + gr) * D))[gq + 16 * i];
      }
      FENCE();
      sy_set(lds, SY_TAG, epoch + 1u, lane);
      sy_set(lds, SY_TAGOK, 1u, lane);
      BARRIER();
#pragma unroll
      for (int pass = 0; pass < 2; ++pass) {
        float sum = 0.f;
#pragma unroll
        for (int i = 0; i < 5; ++i)
#pragma unroll
          for (int e = 0; e < 4; ++e) sum = dot2(gx[pass][i][e], gw[i][e], sum);
#pragma unroll
        for (int m = 8; m >= 1; m >>= 1) sum += __shfl_xor(sum, m);
        if (gq == 0)
          __hip_atomic_store((GAS unsigned*)(a.ws + WS_GATE) + r0 + 4 * pass + gr, __builtin_bit_cast(unsigned, sigmoidf_(sum)),
                             __ATOMIC_RELAXED, __HIP_MEMORY_SCOPE_AGENT);
      }
    }
    const unsigned tag = epoch + 1u;
    STAMP(1);
#pragma unroll 1
    for (int s = 0; s < 9; ++s) BARRIER();
    WAIT_VM(0);
    sy_set(lds, SY_GATE, 1u, lane);
    STAMP(2);
    {
      flag_wait(a.ws, WS_FLAGKV + 4 * (32 * (bid >> 5) + (lane & 31)), true, tag);
      STAMP(8);
      u32x4 uv[3][4];
#pragma unroll
      for (int cw = 0; cw < 3; ++cw) {
        const __amdgpu_buffer_rsrc_t ru = make_rsrc(a.ws + WS_KVR + (size_t)(c0 + (cw < nconv ? cw : 0)) * S * 2, S * 2);
#pragma unroll
        for (int i = 0; i < 4; ++i) uv[cw][i] = ld128(ru, (unsigned)(i * 1024 + lane * 16), 0, SC1);
      }
      FENCE();
#pragma unroll
      for (int cw = 0; cw < 3; ++cw) {
        lptr ur = lds + L_AUX + cw * UR_BYTES;
#pragma unroll
        for (int i = 0; i < 4; ++i) *(LAS u32x4*)(ur + i * 1024 + lane * 16) = uv[cw][i];
        if (lane < 32) *(LAS u32x4*)(ur + 4096 + lane * 16) = (u32x4){0u, 0u, 0u, 0u};
      }
      WAIT_LGKM0();
      sy_set(lds, SY_AREADY, 1u, lane);
    }
    STAMP(9);
    FENCE();
    {
      const char* base = (const char*)a.wd + 640 * (xcd >> 2);
#pragma unroll
      for (int t = 0; t < 4; ++t) {
        const int id = min(64 * t + lane, 239), r = id / 5, c = id - 5 * r;
        touch(base + (size_t)(48 * j + r) * (D * 2) + 128 * c, lds0 + L_DUMMY);
      }
    }
    if (bid == 0) {
#pragma unroll
      for (int i = 0; i < 4; ++i) flag_wait(a.ws, WS_FLAGKV + 4 * (lane + 64 * i), true, tag);
      if (lane == 0) flag_store(a.ws, WS_EPOCH, tag);
    }
    sy_wait(lds, SY_WPRE, 4u);
    flag_wait<HPOLL_SLEEP>(a.ws, WS_FLAGH + 4 * (16 * (si >> 1) + (lane & 15)), true, tag);
    sy_set(lds, SY_HREADY, 1u, lane);
    STAMP(10);
    sy_wait(lds, SY_CONV, nconv);
    sy_wait(lds, SY_GSTEP, 1u);
    flag_wait<YPOLL_SLEEP>(a.ws, WS_FLAGY + 4 * (32 * di + (lane & 31)), true, tag);
    STAMP(11);
    {
      const __amdgpu_buffer_rsrc_t ry = make_rsrc(a.ws + WS_Y2T, S * D * 2);
      u32x4 yv[10];
#pragma unroll
      for (int i = 0; i < 10; ++i) {
        const int id = lane + 64 * i;
        yv[i] = ld128(ry, (unsigned)((80 * di + (id >> 3)) * (S * 2) + 64 * si * 2 + (id & 7) * 16), 0, SC1);
      }
#pragma unroll
      for (int i = 0; i < 10; ++i) {
        const int id = lane + 64 * i, w = (id & 7) >> 1;
        const int so = w == 0 ? ystrip_off(0) : w == 1 ? ystrip_off(1) : w == 2 ? ystrip_off(2) : ystrip_off(3);
        *(LAS u32x4*)(lds + so + (id >> 3) * 32 + (id & 1) * 16) = yv[i];
      }
      WAIT_LGKM0();
      sy_set(lds, SY_YREADY, 1u, lane);
    }
    STAMP(12);
    WAIT_VM(0);
    STAMP_DUMP();
    return;
  }

  if (wave >= 4) {
    const int cw = wave - 4;
    const bool active = cw < nconv;
    const int ch = c0 + (active ? cw : 0);
    Filter flt;
    const int rot1 = ROT1 ? (3 * (j >> 3) + (j & 7)) % 10 : 0;
    if (cw == 0) loader<6>(a, lds, 0, 0, mi, fi, rot1, flt, ch, lane);
    else loader<5>(a, lds, cw, 1 + 5 * cw, mi, fi, rot1, flt, ch, lane);
    STAMP(3);
    if (active) {
      conv_wave(a, lds, lds + (cw == 2 ? 2 * STAGE : STAGE + cw * KC_BYTES), lds + L_AUX + cw * UR_BYTES, flt, ch, nconv, lane);
    }
    STAMP_DUMP();
    return;
  }

  unsigned long long probe_t0 = PROBE_AT ? __builtin_amdgcn_s_memrealtime() : 0ull;
  if (PROBE_GLOBAL && tid == 0)
    __hip_atomic_store((GAS unsigned long long*)(a.ws + (32u << 20)) + bid, probe_t0, __ATOMIC_RELAXED, __HIP_MEMORY_SCOPE_AGENT);
  (void)probe_t0;
  f32x4 ag[4][3], au[4][3];
  {
    const int tsi = bid >> 3, tdi = bid & 7;
    const int task = 40 * wave + (lane < 40 ? lane : 0);
    KvTask t0;
    kv_load(a, t0, task, tsi, tdi);
    FENCE();
#pragma unroll
    for (int t = 0; t < 4; ++t)
#pragma unroll
      for (int c = 0; c < 3; ++c) {
        ag[t][c] = (f32x4){0.f, 0.f, 0.f, 0.f}; au[t][c] = (f32x4){0.f, 0.f, 0.f, 0.f};
        asm volatile("" : "+v"(ag[t][c]), "+v"(au[t][c]));
      }
    FENCE();
    STAMP(1);
    if (lane < 40) kv_compute(t0, lds, task, tsi);
    sy_arrive(lds, SY_KV, lane);
    sy_wait(lds, SY_KV, 4u);
    STAMP(2);
    const __amdgpu_buffer_rsrc_t rkv = make_rsrc(a.ws + WS_KVR, S * D * 2);
#pragma unroll
    for (int i = 0; i < 3; ++i) {
      const int id = tid + 256 * i;
      if (id < 640) {
        const int dl = id >> 3, jj = id & 7;
        const u32x4 val = *(LAS u32x4*)(lds + L_T + dl * 128 + ((jj ^ ((dl >> 3) & 7)) << 4));
        __builtin_amdgcn_raw_buffer_store_b128(val, rkv, (unsigned)(((80 * tdi + dl) * S + (1984 - 64 * tsi) + 8 * jj) * 2), 0, SC1);
      }
    }
  }
  const int n = lane & 15, g = lane >> 4;
  {
    const int ws_ = wave & 1, wf = wave >> 1;
    const int q = n >> 2, p = lane & 3;
    const int xb0 = XB + (64 * ws_ + n) * 128 + ((g ^ (n >> 1)) << 4);
    int wb_[3];
#pragma unroll
    for (int c = 0; c < 3; ++c) wb_[c] = GB + (8 * g + q) * 192 + 8 * p + 32 * ((3 * wf + c) ^ (g & 1));
    auto read_piece = [&](Frags& f, int i, lptr b, int kk) {
      if (i < 4) {
        f.x[i] = lds_frag(b + (xb0 ^ (kk << 6)) + i * 2048);
      } else {
        const int c = (i - 4) >> 2, u = ((i - 4) >> 1) & 1, t = (i - 4) & 1;
        const s16x4 v = lds_tr(b + wb_[c] + kk * (32 * 192) + u * (UB - GB) + t * (4 * 192));
        if (u == 0) f.g[c][t] = v; else f.u[c][t] = v;
      }
    };
    auto mma_piece = [&](const Frags& f, int i) {
      const int c = i >> 3, t = (i >> 1) & 3;
      if ((i & 1) == 0) ag[t][c] = MFMA16(join(f.g[c][0], f.g[c][1]), f.x[t], ag[t][c]);
      else au[t][c] = MFMA16(join(f.u[c][0], f.u[c][1]), f.x[t], au[t][c]);
    };
    auto half = [&](const Frags& cur, Frags& nxt, lptr nb, int nkk, bool rd, auto&& hook) {
#pragma unroll
      for (int i = 0; i < 24; ++i) {
        mma_piece(cur, i); FENCE();
        if (rd && i < 16) { read_piece(nxt, i, nb, nkk); FENCE(); }
        hook(i);
      }
    };
    auto nohook = [](int) {};
    const unsigned cons_addr = lds0 + L_SYNC + 4 * (SY_CONSUMED + wave);
    BARRIER();
    PROBE(1);
    STAMP(3);
    STAMPC(28);
    __builtin_amdgcn_s_setprio(M_PRIO);
    Frags f0, f1;
#pragma unroll
    for (int i = 0; i < 16; ++i) read_piece(f0, i, lds, 0);
    FENCE();
    STAMPC(27);
    int buf = 0;
#pragma unroll 1
    for (int s = 0; s < 9; ++s) {
      const int nbuf = (buf == 2) ? 0 : buf + 1;
      FENCE();
      half(f0, f1, lds + buf * STAGE, 1, true, nohook);
      if (s == SIDE_PUB_ITER) {
        WAIT_VM(0);
        if (sy_arrive(lds, SY_SIDE, lane) == 3u) {
          const unsigned tag = get_tag(lds);
          if (lane == 0) flag_store(a.ws, WS_FLAGKV + 4 * (32 * (bid & 7) + (bid >> 3)), tag);
        }
      }
      WAIT_LGKM0();
      BARRIER();
      FENCE();
      half(f1, f0, lds + nbuf * STAGE, 0, true, nohook);
      buf = nbuf;
    }
    half(f0, f1, lds + buf * STAGE, 1, true, nohook);
    const __amdgpu_buffer_rsrc_t rhs = make_rsrc(a.ws + WS_HT, S * F * 2);
    auto epilogue = [&](int t) {
      u32x2 pk[3];
#pragma unroll
      for (int c = 0; c < 3; ++c) {
        const f32x2 s01 = (f32x2){ag[t][c][0], ag[t][c][1]} * -1.4426950408889634f, s23 = (f32x2){ag[t][c][2], ag[t][c][3]} * -1.4426950408889634f;
        const f32x2 d01 = (f32x2){__builtin_amdgcn_exp2f(s01[0]), __builtin_amdgcn_exp2f(s01[1])} + 1.0f;
        const f32x2 d23 = (f32x2){__builtin_amdgcn_exp2f(s23[0]), __builtin_amdgcn_exp2f(s23[1])} + 1.0f;
        const f32x2 h01 = (f32x2){__builtin_amdgcn_rcpf(d01[0]), __builtin_amdgcn_rcpf(d01[1])} * (f32x2){au[t][c][0], au[t][c][1]};
        const f32x2 h23 = (f32x2){__builtin_amdgcn_rcpf(d23[0]), __builtin_amdgcn_rcpf(d23[1])} * (f32x2){au[t][c][2], au[t][c][3]};
        pk[c] = (u32x2){pack_bf16(h01[0], h01[1]), pack_bf16(h23[0], h23[1])};
      }
      const unsigned base = (unsigned)(((8 * mi + 4 * ws_ + t) * 48 + 3 * fi) * 1024 + lane * 16);
      if (wf == 0) {
        __builtin_amdgcn_raw_buffer_store_b128((u32x4){pk[0][0], pk[0][1], pk[1][0], pk[1][1]}, rhs, base, 0, SC1);
        __builtin_amdgcn_raw_buffer_store_b64(pk[2], rhs, base + 1024, 0, SC1);
      } else {
        __builtin_amdgcn_raw_buffer_store_b64(pk[0], rhs, base + 1024 + 8, 0, SC1);
        __builtin_amdgcn_raw_buffer_store_b128((u32x4){pk[1][0], pk[1][1], pk[2][0], pk[2][1]}, rhs, base + 2048, 0, SC1);
      }
    };
#pragma unroll
    for (int t = 0; t < 4; ++t) {
      FENCE();
#pragma unroll
      for (int c = 0; c < 3; ++c) {
        ag[t][c] = MFMA16(join(f1.g[c][0], f1.g[c][1]), f1.x[t], ag[t][c]);
        au[t][c] = MFMA16(join(f1.u[c][0], f1.u[c][1]), f1.x[t], au[t][c]);
      }
      FENCE();
      if (t == 0) { PROBE(2); STAMP(4); STAMPC(29); }
      if (t > 0) epilogue(t - 1);
    }
    FENCE();
    epilogue(3);
  }
  STAMP(5);

  const __amdgpu_buffer_rsrc_t rh = make_rsrc(a.ws + WS_HT, S * F * 2);
  const __amdgpu_buffer_rsrc_t rw = make_rsrc(a.wd, F * D * 2);
  const unsigned wr0 = wave == 0 ? wring_off(0) : wave == 1 ? wring_off(1) : wave == 2 ? wring_off(2) : wring_off(3);
  const int rot = 3 * (j >> 3);
  auto kstep = [&](int b) { const int t = b + rot; return 12 * wave + (t >= 12 ? t - 12 : t); };
  unsigned vwd[5];
#pragma unroll
  for (int i = 0; i < 5; ++i) {
    const int slot = 64 * i + lane, row = slot / 10, ch = slot - 10 * row;
    vwd[i] = (unsigned)(row * (D * 2) + 160 * di + ch * 16 - 1024 * (i & 3));
  }
  auto load_w = [&](int b) {
    const unsigned dst = lds0 + wr0 + (b & 3) * WSLOT, so = (unsigned)kstep(b) * (32 * D * 2);
    dma16xN<4>(rw, dst, vwd, so);
    dma16xN<1>(rw, dst + 4096, vwd + 4, so);
  };
  auto load_w1 = [&](int b, int i) {
    dma16(rw, lds0 + wr0 + (b & 3) * WSLOT + i * 1024, vwd[i] + 1024 * (i & 3), (unsigned)kstep(b) * (32 * D * 2));
  };
  struct HS4 { u32x4 f[4]; };
  HS4 H[HD + 1];
  const unsigned voff = lane * 16;
  auto load_h = [&](HS4& h, int b) {
    const unsigned so = (unsigned)(4 * si * 48 + kstep(b)) * 1024;
#define HPOL " sc1"
    asm volatile("buffer_load_dwordx4 %0, %4, %5, %6 offen" HPOL "\n\tbuffer_load_dwordx4 %1, %4, %5, %7 offen" HPOL "\n\t"
                 "buffer_load_dwordx4 %2, %4, %5, %8 offen" HPOL "\n\tbuffer_load_dwordx4 %3, %4, %5, %9 offen" HPOL
                 : "=&v"(h.f[0]), "=&v"(h.f[1]), "=&v"(h.f[2]), "=&v"(h.f[3])
                 : "v"(voff), "s"(rh), "s"(so), "s"(so + 48 * 1024), "s"(so + 96 * 1024), "s"(so + 144 * 1024) : "memory");
  };
  auto load_h1 = [&](HS4& h, int b, int i) {
    const unsigned so = (unsigned)((4 * si + i) * 48 + kstep(b)) * 1024;
    asm volatile("buffer_load_dwordx4 %0, %1, %2, %3 offen" HPOL : "=&v"(h.f[i]) : "v"(voff), "s"(rh), "s"(so) : "memory");
  };
  const int srow = 64 * si + 16 * wave + n;
  u32x2 qv[5];
  FENCE();
  __builtin_amdgcn_s_setprio(3);
  sy_arrive(lds, SY_EPI, lane);
  STAMP(22);
  WAIT_VM(0);
  if (sy_arrive(lds, SY_HDONE, lane) == 3u) {
    sy_wait(lds, SY_GATE, 1u);
    const unsigned tag = get_tag(lds);
    if (lane == 0) flag_store(a.ws, WS_FLAGH + 4 * (16 * mi + fi), tag);
  }
  STAMP(23);
  FENCE();
  sy_wait(lds, SY_EPI, 4u);
  load_w(0); load_w(1); load_w(2); load_w(3);
  f32x4 acc[4][5];
#pragma unroll
  for (int t = 0; t < 4; ++t)
#pragma unroll
    for (int c = 0; c < 5; ++c) { acc[t][c] = (f32x4){0.f, 0.f, 0.f, 0.f}; asm volatile("" : "+v"(acc[t][c])); }
  struct WF { s16x4 f[5][2]; };
  WF w0, w1;
  const lptr wrd = lds + wr0 + (4 * g + (n >> 2)) * 160 + 8 * (lane & 3);
  auto read_w = [&](WF& w, int p, int b) {
    w.f[p >> 1][p & 1] = lds_tr(wrd + (b & 3) * WSLOT + (p & 1) * (16 * 160) + (p >> 1) * 32);
  };
  STAMP(24);
  WAIT_VM(10);
#pragma unroll
  for (int p = 0; p < 10; ++p) read_w(w0, p, 0);
#pragma unroll
  for (int p = 0; p < 10; ++p) read_w(w1, p, 1);
  WAIT_LGKM0();
  load_w(4);
  load_w(5);
  sy_arrive(lds, SY_WPRE, lane);
  __builtin_amdgcn_s_setprio(G_PRIO);
  STAMP(6);
  sy_wait(lds, SY_HREADY, 1u);
  PROBE(3);
  STAMP(7);
#pragma unroll
  for (int b = 0; b < HD; ++b) load_h(H[b], b);
  const float gate = __builtin_bit_cast(float, __hip_atomic_load((GAS unsigned*)(a.ws + WS_GATE) + srow, __ATOMIC_RELAXED, __HIP_MEMORY_SCOPE_AGENT));
  FENCE();
  auto gstep = [&](const WF& cur, WF& nxt, HS4& h, HS4& hn, int b) {
    asm volatile("s_waitcnt vmcnt(%4) lgkmcnt(0)" : "+v"(h.f[0]), "+v"(h.f[1]), "+v"(h.f[2]), "+v"(h.f[3]) : "n"(g_younger(b)) : "memory");
    FENCE();
#pragma unroll
    for (int i = 0; i < 20; ++i) {
      acc[i & 3][i >> 2] = MFMA16(join(cur.f[i >> 2][0], cur.f[i >> 2][1]), __builtin_bit_cast(bf16x8, h.f[i & 3]), acc[i & 3][i >> 2]);
      FENCE();
      if (i < 8 && (i & 1) == 0 && b + HD < 12) { load_h1(hn, b + HD, i >> 1); FENCE(); }
      if (i == 7 && b == 9) {
#pragma unroll
        for (int c = 0; c < 5; ++c) qv[c] = *(const u32x2*)(a.x + (size_t)srow * D + 80 * di + 16 * c + 4 * g);
        asm volatile("" ::: "memory");
        FENCE();
      }
      if (i < 10 && b >= 1 && b + 1 < 12) { read_w(nxt, i, b + 1); FENCE(); }
      if (i >= 10 && (i & 1) == 0 && b >= 2 && b + 4 < 12) { load_w1(b + 4, (i - 10) >> 1); FENCE(); }
      if (b == YPOLL_STEP && i == 17 && wave == 0) { sy_post(lds0 + L_SYNC + 4 * SY_GSTEP, 1u); FENCE(); }
      if (b == YPOLL_STEP && i == 19 && wave == 0) { wake(); FENCE(); }
    }
  };
#pragma unroll
  for (int b = 0; b < 12; b += 2) {
    gstep(w0, w1, H[b % (HD + 1)], H[(b + HD) % (HD + 1)], b);
    if (b == 0) STAMPI(14);
    gstep(w1, w0, H[(b + 1) % (HD + 1)], H[(b + 1 + HD) % (HD + 1)], b + 1);
    STAMPI(15 + (b >> 1));
  }
  PROBE(4);
  STAMP(8);

  f32x4 bsum[5];
  {
#pragma unroll
    for (int t = 0; t < 4; ++t) {
      if (t != wave) {
#pragma unroll
        for (int c = 0; c < 5; ++c) *(LAS f32x4*)(lds + wr0 + (((t - wave - 1) & 3) * 5 + c) * 1024 + lane * 16) = acc[t][c];
      } else {
#pragma unroll
        for (int c = 0; c < 5; ++c) bsum[c] = acc[t][c];
      }
    }
    sy_arrive(lds, SY_RED, lane);
    sy_wait(lds, SY_RED, 4u);
#pragma unroll
    for (int w = 0; w < 4; ++w) {
      if (w != wave) {
#pragma unroll
        for (int c = 0; c < 5; ++c) bsum[c] += *(LAS f32x4*)(lds + wring_off(w) + (((wave - w - 1) & 3) * 5 + c) * 1024 + lane * 16);
      }
    }
  }
  STAMP(9);
  sy_wait(lds, SY_YREADY, 1u);
  STAMP(10);
  {
    const int q = n >> 2, p = lane & 3;
    const __amdgpu_buffer_rsrc_t ro = make_rsrc(a.out, S * D * 2);
    const lptr ystr = lds + (wave == 0 ? ystrip_off(0) : wave == 1 ? ystrip_off(1) : wave == 2 ? ystrip_off(2) : ystrip_off(3));
#pragma unroll
    for (int c = 0; c < 5; ++c) {
      const s16x4 yv = lds_tr(ystr + (16 * c + 4 * g + q) * 32 + 8 * p);
      const u32x2 y = __builtin_bit_cast(u32x2, yv);
      const float o0 = gate * (bf_lo(qv[c][0]) * bf_lo(y[0])) + (1.f - gate) * bsum[c][0];
      const float o1 = gate * (bf_hi(qv[c][0]) * bf_hi(y[0])) + (1.f - gate) * bsum[c][1];
      const float o2 = gate * (bf_lo(qv[c][1]) * bf_lo(y[1])) + (1.f - gate) * bsum[c][2];
      const float o3 = gate * (bf_hi(qv[c][1]) * bf_hi(y[1])) + (1.f - gate) * bsum[c][3];
      __builtin_amdgcn_raw_buffer_store_b64((u32x2){pack_bf16(o0, o1), pack_bf16(o2, o3)}, ro,
                                            (unsigned)((srow * D + 80 * di + 16 * c + 4 * g) * 2), 0, OUT_AUX);
    }
  }
  if (PROBE_GLOBAL) {
    unsigned long long r = PROBE_GLOBAL == 1 ? ~0ull : 0ull;
    for (int i = 0; i < 4; ++i) {
      const unsigned long long v = __hip_atomic_load((GAS unsigned long long*)(a.ws + (32u << 20)) + lane + 64 * i, __ATOMIC_RELAXED, __HIP_MEMORY_SCOPE_AGENT);
      r = PROBE_GLOBAL == 1 ? min(r, v) : max(r, v);
    }
    for (int m = 32; m >= 1; m >>= 1) { const unsigned long long o = __shfl_xor(r, m); r = PROBE_GLOBAL == 1 ? min(r, o) : max(r, o); }
    probe_t0 = __builtin_amdgcn_readfirstlane((unsigned)r) | ((unsigned long long)__builtin_amdgcn_readfirstlane((unsigned)(r >> 32)) << 32);
  }
  PROBE(5);
  STAMP(11);
}

}

extern "C" void kernel_launch(void* const* d_in, const int* in_sizes, int n_in, void* d_out, int out_size, void* d_ws,
                              size_t ws_size, hipStream_t stream) {
  (void)in_sizes; (void)n_in; (void)out_size; (void)ws_size;
  Args a;
  a.x = (const u16*)d_in[0];
  a.k = (const u16*)d_in[1];
  a.v = (const u16*)d_in[2];
  a.shk = (const u16*)d_in[3];
  a.ssmk = (const u16*)d_in[4];
  a.ssmd = (const u16*)d_in[5];
  a.wg = (const u16*)d_in[7];
  a.wu = (const u16*)d_in[8];
  a.wd = (const u16*)d_in[9];
  a.gw = (const u16*)d_in[10];
  a.ws = (char*)d_ws;
  a.out = (u16*)d_out;
  (void)hipFuncSetAttribute((const void*)fused_kernel, hipFuncAttributeMaxDynamicSharedMemorySize, LDS_BYTES);
  fused_kernel<<<256, 512, LDS_BYTES, stream>>>(a);
}
```

```cpp
#include <hip/hip_runtime.h>
#include <hip/hip_bf16.h>

namespace {

typedef unsigned short u16;
typedef __bf16 bf16x8 __attribute__((ext_vector_type(8)));
typedef __bf16 bf16x2 __attribute__((ext_vector_type(2)));
typedef short s16x4 __attribute__((ext_vector_type(4)));
typedef float f32x2 __attribute__((ext_vector_type(2)));
typedef float f32x4 __attribute__((ext_vector_type(4)));
typedef unsigned u32x2 __attribute__((ext_vector_type(2)));
typedef unsigned u32x4 __attribute__((ext_vector_type(4)));
#define LAS __attribute__((address_space(3)))
#define GAS __attribute__((address_space(1)))
typedef LAS char* lptr;

constexpr int S = 2048, D = 640, F = 1536;
constexpr int SC1 = 16;

constexpr size_t WS_HT = 0;
constexpr size_t WS_KVR = WS_HT + (size_t)S * F * 2;
constexpr size_t WS_Y2T = WS_KVR + (size_t)S * D * 2;
constexpr size_t WS_GATE = WS_Y2T + (size_t)S * D * 2;
constexpr size_t WS_FLAGKV = WS_GATE + (size_t)S * 4;
constexpr size_t WS_FLAGH = WS_FLAGKV + 1024;
constexpr size_t WS_FLAGY = WS_FLAGH + 1024;
constexpr size_t WS_EPOCH = WS_FLAGY + 1024;
constexpr size_t WS_END = WS_EPOCH + 256;

constexpr int STAGE = 40960, XB = 0, GB = 16384, UB = 28672;
constexpr int L_AUX = 3 * STAGE;
constexpr int L_T = L_AUX, UR_BYTES = 4608;
constexpr int L_FREE = L_AUX + 3 * UR_BYTES;
constexpr int L_SYNC = L_FREE + 20480 + 2 * 2560;
constexpr int L_DUMMY = L_SYNC + 128;
constexpr int L_END = L_DUMMY + 256;
constexpr int KC_COPY = 4160, KC_BYTES = 4 * KC_COPY;
constexpr int WSLOT = 5120;
__device__ constexpr int wring_off(int w) { return w == 0 ? 0 : w == 1 ? 20480 : w == 2 ? 2 * STAGE + KC_BYTES : L_FREE; }
__device__ constexpr int ystrip_off(int w) { return w < 2 ? STAGE + 2 * KC_BYTES + w * 2560 : L_FREE + 20480 + (w - 2) * 2560; }
static_assert(2 * STAGE + KC_BYTES + 20480 <= 3 * STAGE && STAGE + 2 * KC_BYTES + 2 * 2560 <= 2 * STAGE, "second-phase LDS map");
enum { SY_LANDED = 0, SY_CONSUMED = 4, SY_KV = 8, SY_SIDE = 9, SY_HDONE = 10, SY_RED = 11, SY_CONV = 12, SY_AREADY = 13,
       SY_HREADY = 14, SY_YREADY = 15, SY_TAG = 16, SY_TAGOK = 17, SY_GSTEP = 18, SY_GATE = 19, SY_EPI = 20, SY_WPRE = 21, SY_EX = 22 };

#define STAMP(i) do {} while (0)
#define STAMPC(i) do {} while (0)
#define STAMPI(i) do {} while (0)
#define STAMP_INIT() do {} while (0)
#define STAMP_DUMP() do {} while (0)
constexpr int LDS_BYTES = L_END;

#define PROBE_AT 0
#define PROBE_US 0
#define PROBE(k) do { if (PROBE_AT == (k)) { while (__builtin_amdgcn_s_memrealtime() - probe_t0 < 100ull * PROBE_US) __builtin_amdgcn_s_sleep(1); } } while (0)
#define PROBE_GLOBAL 0
#define MFMA16(a, b, c) __builtin_amdgcn_mfma_f32_16x16x32_bf16((a), (b), (c), 0, 0, 0)
#define WAIT_VM(n) asm volatile("s_waitcnt vmcnt(" #n ")" ::: "memory")
#define WAIT_LGKM0() asm volatile("s_waitcnt lgkmcnt(0)" ::: "memory")
#define BARRIER() do { asm volatile("" ::: "memory"); __builtin_amdgcn_s_barrier(); asm volatile("" ::: "memory"); } while (0)
#define FENCE() __builtin_amdgcn_sched_barrier(0)
constexpr unsigned SPIN_LDS = 1u << 22, SPIN_MEM = 1u << 16;

__device__ __forceinline__ unsigned pack_bf16(float lo, float hi) {
  f32x2 v = {lo, hi};
  return __builtin_bit_cast(unsigned, __builtin_convertvector(v, bf16x2));
}
__device__ __forceinline__ float bf_lo(unsigned w) { return __builtin_bit_cast(float, w << 16); }
__device__ __forceinline__ float bf_hi(unsigned w) { return __builtin_bit_cast(float, w & 0xffff0000u); }
__device__ __forceinline__ float sigmoidf_(float x) {
  return __builtin_amdgcn_rcpf(1.0f + __builtin_amdgcn_exp2f(x * -1.4426950408889634f));
}
__device__ __forceinline__ __amdgpu_buffer_rsrc_t make_rsrc(const void* p, unsigned bytes) {
  return __builtin_amdgcn_make_buffer_rsrc(const_cast<void*>(p), 0, bytes, 0x00020000);
}
__device__ __forceinline__ unsigned lds_addr(lptr p) { return (unsigned)(size_t)p; }
__device__ __forceinline__ void dma16(__amdgpu_buffer_rsrc_t r, unsigned dst, unsigned voff, unsigned soff) {
  asm volatile("s_mov_b32 m0, %0\n\ts_nop 0\n\tbuffer_load_dwordx4 %1, %2, %3 offen lds"
               :: "s"(dst), "v"(voff), "s"(r), "s"(soff) : "memory");
}
template <int N>
__device__ __forceinline__ void dma16xN(__amdgpu_buffer_rsrc_t r, unsigned dst, const unsigned* v, unsigned soff) {
  static_assert(N >= 1 && N <= 4, "12-bit instruction offset");
  if constexpr (N == 1)
    asm volatile("s_mov_b32 m0, %0\n\ts_nop 0\n\tbuffer_load_dwordx4 %1, %2, %3 offen lds"
                 :: "s"(dst), "v"(v[0]), "s"(r), "s"(soff) : "memory");
  else if constexpr (N == 2)
    asm volatile("s_mov_b32 m0, %0\n\ts_nop 0\n\tbuffer_load_dwordx4 %1, %3, %4 offen lds\n\t"
                 "buffer_load_dwordx4 %2, %3, %4 offen offset:1024 lds"
                 :: "s"(dst), "v"(v[0]), "v"(v[1]), "s"(r), "s"(soff) : "memory");
  else if constexpr (N == 3)
    asm volatile("s_mov_b32 m0, %0\n\ts_nop 0\n\tbuffer_load_dwordx4 %1, %4, %5 offen lds\n\t"
                 "buffer_load_dwordx4 %2, %4, %5 offen offset:1024 lds\n\tbuffer_load_dwordx4 %3, %4, %5 offen offset:2048 lds"
                 :: "s"(dst), "v"(v[0]), "v"(v[1]), "v"(v[2]), "s"(r), "s"(soff) : "memory");
  else
    asm volatile("s_mov_b32 m0, %0\n\ts_nop 0\n\tbuffer_load_dwordx4 %1, %5, %6 offen lds\n\t"
                 "buffer_load_dwordx4 %2, %5, %6 offen offset:1024 lds\n\tbuffer_load_dwordx4 %3, %5, %6 offen offset:2048 lds\n\t"
                 "buffer_load_dwordx4 %4, %5, %6 offen offset:3072 lds"
                 :: "s"(dst), "v"(v[0]), "v"(v[1]), "v"(v[2]), "v"(v[3]), "s"(r), "s"(soff) : "memory");
}
__device__ __forceinline__ void touch(const void* p, unsigned dummy_lds) {
  asm volatile("s_mov_b32 m0, %0\n\ts_nop 0\n\tglobal_load_lds_dword %1, off" :: "s"(dummy_lds), "v"(p) : "memory");
}
__device__ __forceinline__ s16x4 lds_tr(lptr p) { return __builtin_amdgcn_ds_read_tr16_b64_v4i16((LAS s16x4*)p); }
__device__ __forceinline__ bf16x8 join(s16x4 a, s16x4 b) {
  return __builtin_bit_cast(bf16x8, __builtin_shufflevector(a, b, 0, 1, 2, 3, 4, 5, 6, 7));
}
__device__ __forceinline__ bf16x8 lds_frag(lptr p) { return *(LAS bf16x8*)p; }
__device__ __forceinline__ u32x4 ld128(__amdgpu_buffer_rsrc_t r, unsigned voff, unsigned soff, int aux) {
  return aux ? __builtin_bit_cast(u32x4, __builtin_amdgcn_raw_buffer_load_b128(r, voff, soff, SC1))
             : __builtin_bit_cast(u32x4, __builtin_amdgcn_raw_buffer_load_b128(r, voff, soff, 0));
}

__device__ __forceinline__ LAS unsigned* sy(lptr lds, int i) { return (LAS unsigned*)(lds + L_SYNC) + i; }
__device__ __forceinline__ unsigned sy_get(lptr lds, int i) {
  return __builtin_amdgcn_readfirstlane(__hip_atomic_load(sy(lds, i), __ATOMIC_RELAXED, __HIP_MEMORY_SCOPE_WORKGROUP));
}
__device__ __forceinline__ void wake() { asm volatile("s_wakeup" ::: "memory"); }
__device__ __forceinline__ void sy_set(lptr lds, int i, unsigned v, int lane) {
  if (lane == 0) __hip_atomic_store(sy(lds, i), v, __ATOMIC_RELAXED, __HIP_MEMORY_SCOPE_WORKGROUP);
  asm volatile("s_waitcnt lgkmcnt(0)\n\ts_wakeup" ::: "memory");
}
#define TOUCH_H 0
#define LOADER_POST_FIRST 1
#define HPOLL_SLEEP 4
#define YPOLL_SLEEP 8
#define YPOLL_STEP 8
#ifndef M_PRIO
#define M_PRIO 2
#endif
#define G_PRIO 0
#define ROT1 1
#define DMA_SPACE 0
#define C_PRIO 2
#define G_HDEPTH 4
#define OUT_AUX 0
#define H_ACQUIRE 0
#define CODE_LINES 0
#define CODE_WAVE 7
#define PUB_FIRST 1
#define LAND_RD 18
#define TOUCH_DELAY 8
#define TOUCH1 0
#define FILT_ITER 6
#define W7_DELAY 0
#define POLL_AFTER 8
#define LOADER_PRIO 0
#define SLEEP_LDS 4
__device__ __forceinline__ void sy_post(unsigned addr, unsigned v) {
  asm volatile("s_mov_b64 exec, 1\n\tds_write_b32 %0, %1\n\ts_mov_b64 exec, -1" :: "v"(addr), "v"(v) : "memory");
}
__device__ __forceinline__ void sy_wait(lptr lds, int i, unsigned v) {
#pragma unroll 1
  for (unsigned n = 0; n < SPIN_LDS; ++n) {
    if (sy_get(lds, i) >= v) break;
    __builtin_amdgcn_s_sleep(SLEEP_LDS);
  }
  asm volatile("" ::: "memory");
}
__device__ __forceinline__ unsigned sy_arrive(lptr lds, int i, int lane) {
  WAIT_LGKM0();
  unsigned old = 0;
  if (lane == 0) old = __hip_atomic_fetch_add(sy(lds, i), 1u, __ATOMIC_RELAXED, __HIP_MEMORY_SCOPE_WORKGROUP);
  old = __builtin_amdgcn_readfirstlane(old);
  wake();
  return old;
}
__device__ __forceinline__ unsigned min4(u32x4 v) { return min(min(v[0], v[1]), min(v[2], v[3])); }
__device__ __forceinline__ u32x4 sy_get4(lptr lds, int i) { return *(volatile LAS u32x4*)(lds + L_SYNC + 4 * i); }
__device__ __forceinline__ u32x2 sy_get2(lptr lds, int i) { return *(volatile LAS u32x2*)(lds + L_SYNC + 4 * i); }
__device__ __forceinline__ void sy_wait4(lptr lds, int i, unsigned v) {
#pragma unroll 1
  for (unsigned n = 0; n < SPIN_LDS; ++n) {
    if ((unsigned)__builtin_amdgcn_readfirstlane(min4(sy_get4(lds, i))) >= v) break;
    __builtin_amdgcn_s_sleep(SLEEP_LDS);
  }
  asm volatile("" ::: "memory");
}
#define LOADER_SLEEP 1
__device__ __forceinline__ void loader_wait(lptr lds, unsigned v) {
#pragma unroll 1
  for (unsigned n = 0; n < SPIN_LDS; ++n) {
    if ((unsigned)__builtin_amdgcn_readfirstlane(min4(sy_get4(lds, SY_CONSUMED))) >= v) break;
    if (LOADER_SLEEP) __builtin_amdgcn_s_sleep(LOADER_SLEEP);
  }
  asm volatile("" ::: "memory");
}
__device__ __forceinline__ void sy_wait4m(lptr lds, unsigned v) {
#pragma unroll 1
  for (unsigned n = 0; n < SPIN_LDS; ++n) {
    const u32x4 l = sy_get4(lds, SY_LANDED);
    if ((unsigned)__builtin_amdgcn_readfirstlane(min(min(l[0], l[1]), l[2])) >= v) break;
    __builtin_amdgcn_s_sleep(SLEEP_LDS);
  }
  asm volatile("" ::: "memory");
}
__device__ __forceinline__ unsigned get_tag(lptr lds) {
  sy_wait(lds, SY_TAGOK, 1u);
  return sy_get(lds, SY_TAG);
}
__device__ __forceinline__ void flag_store(char* ws, size_t off, unsigned tag) {
  __hip_atomic_store((GAS unsigned*)(ws + off), tag, __ATOMIC_RELAXED, __HIP_MEMORY_SCOPE_AGENT);
}
#define SIDE_PUB_ITER 3
#define POLL_SLEEP 32
template <int SLEEP = POLL_SLEEP>
__device__ __forceinline__ void flag_wait(char* ws, size_t off, bool on, unsigned tag) {
#pragma unroll 1
  for (unsigned n = 0; n < SPIN_MEM; ++n) {
    const unsigned v = __hip_atomic_load((GAS unsigned*)(ws + off), __ATOMIC_RELAXED, __HIP_MEMORY_SCOPE_AGENT);
    if (__all(!on || v == tag)) break;
    __builtin_amdgcn_s_sleep(SLEEP);
  }
  asm volatile("" ::: "memory");
}

struct Args {
  const u16* x; const u16* k; const u16* v; const u16* shk; const u16* ssmk; const u16* ssmd;
  const u16* wg; const u16* wu; const u16* wd; const u16* gw;
  char* ws; u16* out;
};

struct Frags { bf16x8 x[4]; s16x4 g[3][2]; s16x4 u[3][2]; };

__device__ constexpr int tri_start(int t) { return 8 * t - (t * (t - 1)) / 2; }
__device__ constexpr int tri_row(int p) {
  return p < 8 ? 0 : p < 15 ? 1 : p < 21 ? 2 : p < 26 ? 3 : p < 30 ? 4 : p < 33 ? 5 : p < 35 ? 6 : 7;
}

template <int N> __device__ __forceinline__ void wait_vm() { asm volatile("s_waitcnt vmcnt(%0)" ::"n"(N) : "memory"); }

struct Filter { unsigned d[5][6]; unsigned dbits; };
__device__ __forceinline__ void load_filter(const Args& a, Filter& f, int ch, int lane) {
  const __amdgpu_buffer_rsrc_t rk = make_rsrc(a.ssmk + (size_t)ch * S, S * 2);
#pragma unroll
  for (int it = 0; it < 5; ++it) {
    const unsigned off = (unsigned)(16 * (lane + 64 * it) - 32);
    const u32x4 v0 = __builtin_bit_cast(u32x4, __builtin_amdgcn_raw_buffer_load_b128(rk, off, 0, 0));
    const u32x2 v1 = __builtin_bit_cast(u32x2, __builtin_amdgcn_raw_buffer_load_b64(rk, off + 16, 0, 0));
    f.d[it][0] = v0[0]; f.d[it][1] = v0[1]; f.d[it][2] = v0[2]; f.d[it][3] = v0[3]; f.d[it][4] = v1[0]; f.d[it][5] = v1[1];
  }
  f.dbits = a.ssmd[ch];
}

template <int NX>
__device__ __forceinline__ void loader(const Args& a, lptr lds, int lw, int x0, int mi, int fi, int rot, Filter& flt, int ch, int lane) {
  const unsigned lds0 = lds_addr(lds);
  const __amdgpu_buffer_rsrc_t rx = make_rsrc(a.x, S * D * 2);
  const __amdgpu_buffer_rsrc_t rg = make_rsrc(a.wg, D * F * 2);
  const __amdgpu_buffer_rsrc_t ru = make_rsrc(a.wu, D * F * 2);
  unsigned vx[NX], vw[4];
#pragma unroll
  for (int i = 0; i < NX; ++i) {
    const int row = 8 * (x0 + i) + (lane >> 3), c = (lane & 7) ^ ((row >> 1) & 7);
    vx[i] = (unsigned)((128 * mi + row) * (D * 2) + c * 16 - 1024 * (i & 3));
  }
#pragma unroll
  for (int i = 0; i < 4; ++i) {
    const int slot = 64 * (4 * lw + i) + lane, k = slot / 12, chp = slot - 12 * k, c = chp ^ (2 * ((k >> 3) & 1));
    vw[i] = (unsigned)(k * (F * 2) + fi * 192 + c * 16 - 1024 * i);
  }
  const unsigned land_addr = lds0 + L_SYNC + 4 * (SY_LANDED + lw);
  auto slice = [&](int s) { const int k = s + rot; return k >= 10 ? k - 10 : k; };
  auto part_a = [&](int s) {
    const unsigned b = lds0 + (s % 3) * STAGE, so = (unsigned)slice(s) * (64 * F * 2);
    dma16xN<4>(rg, b + GB + 4 * lw * 1024, vw, so);
    if (DMA_SPACE) __builtin_amdgcn_s_sleep(DMA_SPACE);
    dma16xN<4>(ru, b + UB + 4 * lw * 1024, vw, so);
    if (DMA_SPACE) __builtin_amdgcn_s_sleep(DMA_SPACE);
  };
  auto part_b = [&](int s) {
    const unsigned b = lds0 + (s % 3) * STAGE, so = (unsigned)slice(s) * 128;
    dma16xN<4>(rx, b + XB + x0 * 1024, vx, so);
    if (DMA_SPACE) __builtin_amdgcn_s_sleep(DMA_SPACE);
    dma16xN<NX - 4>(rx, b + XB + (x0 + 4) * 1024, vx + 4, so);
  };
  constexpr int P = NX + 8;
  part_a(0); part_b(0);
  part_a(1); part_b(1);
  STAMP(1);
  wait_vm<P>();
  STAMP(2);
  BARRIER();
  part_a(2); part_b(2);
#pragma unroll
  for (int s = 0; s < 9; ++s) {
    if (s == 8) wait_vm<11>(); else wait_vm<P>();
    STAMPI(8 + 2 * s);
    BARRIER();
    STAMPI(9 + 2 * s);
    if (s < 7) { part_a(s + 3); part_b(s + 3); }
    if (s == 7) { FENCE(); load_filter(a, flt, ch, lane); FENCE(); asm volatile("" ::: "memory"); }
  }
}

struct KvTask { u32x4 kr[7], vr[4], wk[4]; };
__device__ __forceinline__ void kv_load(const Args& a, KvTask& t, int task, int tsi, int tdi) {
  const int dc = task % 10, sb = task / 10;
  const int s0 = 64 * tsi + 4 * sb, d0 = 80 * tdi + 8 * dc;
#pragma unroll
  for (int r = 0; r < 7; ++r) {
    const int s = s0 - 3 + r;
    t.kr[r] = *(const u32x4*)(a.k + (size_t)(s < 0 ? 0 : s) * D + d0);
  }
#pragma unroll
  for (int r = 0; r < 4; ++r) t.vr[r] = *(const u32x4*)(a.v + (size_t)(s0 + r) * D + d0);
#pragma unroll
  for (int i = 0; i < 4; ++i) t.wk[i] = *(const u32x4*)(a.shk + (size_t)d0 * 4 + i * 8);
}
__device__ __forceinline__ float dot2(unsigned x, unsigned y, float c) {
  return __builtin_amdgcn_fdot2_f32_bf16(__builtin_bit_cast(bf16x2, x), __builtin_bit_cast(bf16x2, y), c, false);
}
__device__ __forceinline__ float dot2z(unsigned x, unsigned y) {
  float r;
  asm("v_dot2_f32_bf16 %0, %1, %2, 0" : "=v"(r) : "v"(x), "v"(y));
  return r;
}
__device__ __forceinline__ void kv_compute(KvTask& t, lptr lds, int task, int tsi) {
  const int dc = task % 10, sb = task / 10;
  if (tsi == 0 && sb == 0) {
#pragma unroll
    for (int r = 0; r < 3; ++r) t.kr[r] = (u32x4){0u, 0u, 0u, 0u};
  }
  const int sg = 15 - sb;
  lptr dst = lds + L_T + 8 * dc * 128 + (((sg >> 1) ^ (dc & 7)) << 4) + ((sg & 1) << 3);
#pragma unroll
  for (int c = 0; c < 8; ++c) {
    const unsigned w01 = t.wk[c >> 1][2 * (c & 1)], w23 = t.wk[c >> 1][2 * (c & 1) + 1];
    const unsigned sel = (c & 1) ? 0x03020706u : 0x01000504u;
    unsigned pr[6];
#pragma unroll
    for (int i = 0; i < 6; ++i) pr[i] = __builtin_amdgcn_perm(t.kr[i + 1][c >> 1], t.kr[i][c >> 1], sel);
    float o[4];
#pragma unroll
    for (int r = 0; r < 4; ++r) {
      const float ks = dot2(pr[r + 2], w01, dot2z(pr[r], w23));
      o[r] = ks * ((c & 1) ? bf_hi(t.vr[r][c >> 1]) : bf_lo(t.vr[r][c >> 1]));
    }
    *(LAS u32x2*)(dst + c * 128) = (u32x2){pack_bf16(o[3], o[2]), pack_bf16(o[1], o[0])};
  }
}

__device__ __forceinline__ void conv_wave(const Args& a, lptr lds, lptr kc, lptr ur, const Filter& flt, int ch, int nconv, int lane) {
#pragma unroll
  for (int it = 0; it < 5; ++it) {
    const int c = lane + 64 * it;
    if (c < 260) {
      const unsigned a0 = flt.d[it][0], a1 = flt.d[it][1], a2 = flt.d[it][2], a3 = flt.d[it][3], a4 = flt.d[it][4], a5 = flt.d[it][5];
      const unsigned e0 = __builtin_amdgcn_alignbit(a1, a0, 16), e1 = __builtin_amdgcn_alignbit(a2, a1, 16),
                     e2 = __builtin_amdgcn_alignbit(a3, a2, 16), e3 = __builtin_amdgcn_alignbit(a4, a3, 16),
                     e4 = __builtin_amdgcn_alignbit(a5, a4, 16);
      lptr w = kc + 16 * c;
      *(LAS u32x4*)(w) = (u32x4){a0, a1, a2, a3};
      *(LAS u32x4*)(w + KC_COPY) = (u32x4){e0, e1, e2, e3};
      *(LAS u32x4*)(w + 2 * KC_COPY) = (u32x4){a1, a2, a3, a4};
      *(LAS u32x4*)(w + 3 * KC_COPY) = (u32x4){e1, e2, e3, e4};
    }
  }
  const float dcoef = bf_lo(flt.dbits);
  WAIT_LGKM0();
  sy_wait(lds, SY_AREADY, 1u);
  sy_wait(lds, SY_EPI, 4u);
  __builtin_amdgcn_s_setprio(C_PRIO);
  STAMP(4);
  STAMPC(28);

  const int m = lane & 15, g = lane >> 4;
  const int rho = (m + 1) & 3;
  lptr abase = kc + rho * KC_COPY + 2 * (8 * g + m - 31 - rho);
  lptr bbase = ur + 2 * (2000 - 16 * m + 8 * g) - 7 * 512;
  f32x4 acc[8];
#pragma unroll
  for (int t = 0; t < 8; ++t) acc[t] = (f32x4){0.f, 0.f, 0.f, 0.f};
  struct CF { s16x4 a[8][2]; bf16x8 b[8]; };
  auto cread = [&](CF& f, int p, int i) {
    if (p < 16) f.a[p >> 1][p & 1] = *(LAS s16x4*)(abase + 64 * i + 512 * (p >> 1) + 8 * (p & 1));
    else f.b[p - 16] = lds_frag(bbase + 64 * i + 512 * (7 - (p - 16)));
  };
  auto cmma = [&](const CF& f, int p) {
    const int t = tri_row(p), u = p - tri_start(t);
    acc[t + u] = MFMA16(join(f.a[t][0], f.a[t][1]), f.b[u], acc[t + u]);
  };
  auto residue = [&](const CF& cur, CF& nxt, int inext) {
#pragma unroll
    for (int p = 0; p < 36; ++p) {
      cmma(cur, p); FENCE();
      if (p < 24) { cread(nxt, p, inext); FENCE(); }
    }
  };
  CF c0, c1;
#pragma unroll
  for (int p = 0; p < 24; ++p) cread(c0, p, 1);
#pragma unroll 1
  for (int i = 1; i <= 8; i += 2) {
    FENCE();
    residue(c0, c1, i + 1);
    residue(c1, c0, (i + 2 <= 8) ? i + 2 : 8);
  }
  STAMP(5);
  STAMPC(29);
  const __amdgpu_buffer_rsrc_t ry = make_rsrc(a.ws + WS_Y2T + (size_t)ch * S * 2, S * 2);
#pragma unroll
  for (int t = 0; t < 8; t += 2) {
    unsigned pk[2][2];
#pragma unroll
    for (int e = 0; e < 2; ++e) {
      const int s = 256 * (t + e) + 16 * m + 4 * g;
      const u32x2 kv = *(LAS u32x2*)(ur + 2 * (2044 - s));
      pk[e][0] = pack_bf16(acc[t + e][0] + bf_hi(kv[1]) * dcoef, acc[t + e][1] + bf_lo(kv[1]) * dcoef);
      pk[e][1] = pack_bf16(acc[t + e][2] + bf_hi(kv[0]) * dcoef, acc[t + e][3] + bf_lo(kv[0]) * dcoef);
    }
    const auto r0 = __builtin_amdgcn_permlane16_swap(pk[0][0], pk[1][0], false, false);
    const auto r1 = __builtin_amdgcn_permlane16_swap(pk[0][1], pk[1][1], false, false);
    const u32x4 o = {r0[0], r1[0], r0[1], r1[1]};
    const unsigned off = 2 * (256 * (t + (g & 1)) + 16 * m + 8 * (g >> 1));
    __builtin_amdgcn_raw_buffer_store_b128(o, ry, off, 0, SC1);
  }
  STAMP(6);
  WAIT_VM(0);
  STAMP(7);
  if (sy_arrive(lds, SY_CONV, lane) == (unsigned)(nconv - 1)) {
    const unsigned tag = get_tag(lds);
    if (lane == 0) flag_store(a.ws, WS_FLAGY + 4 * blockIdx.x, tag);
  }
}

constexpr int HD = G_HDEPTH;
__device__ constexpr int g_younger(int b) {
  int count = 0, last = 0;
  for (int k = 0; k < HD; ++k) { count += 4; if (k == b) last = count; }
  count += 1;
  for (int s = 0; s < b; ++s) {
    if (s + HD <= 11) { count += 4; if (s + HD == b) last = count; }
    if (s == 9) count += 5;
    if (s >= 2 && s <= 7) { count += 5; if (s + 4 == b + 1) last = count; }
  }
  return count - last;
}
__global__ __launch_bounds__(512, 2) void fused_kernel(Args a) {
  extern __shared__ __attribute__((aligned(16))) char smem[];
  lptr lds = (lptr)smem;
  const int tid = threadIdx.x, lane = tid & 63;
  const int wave = __builtin_amdgcn_readfirstlane(tid >> 6);
  const int bid = blockIdx.x;
  const int xcd = bid & 7, j = bid >> 3;
  const int mi = 8 * (xcd & 1) + (j & 7), fi = 4 * (xcd >> 1) + (j >> 3);
  const int si = 8 * (xcd & 3) + (j & 7), di = 4 * (xcd >> 2) + (j >> 3);
  const int c0 = (5 * bid) >> 1, nconv = 2 + (bid & 1);
  const unsigned lds0 = lds_addr(lds);
  STAMP_INIT();
  STAMP(0);

  if (wave == 7) {
    if (lane < 32) *sy(lds, lane) = 0u;
    WAIT_LGKM0();
  }
  BARRIER();

  if (wave == 7) {
    const unsigned epoch = __hip_atomic_load((GAS unsigned*)(a.ws + WS_EPOCH), __ATOMIC_RELAXED, __HIP_MEMORY_SCOPE_AGENT);
    {
      const int r0 = 128 * mi + 8 * fi, gr = lane >> 4, gq = lane & 15;
      u32x4 gx[2][5], gw[5];
#pragma unroll
      for (int i = 0; i < 5; ++i) {
        gw[i] = ((const u32x4*)a.gw)[gq + 16 * i];
        gx[0][i] = ((const u32x4*)(a.x + (size_t)(r0 + gr) * D))[gq + 16 * i];
        gx[1][i] = ((const u32x4*)(a.x + (size_t)(r0 + 4 + gr) * D))[gq + 16 * i];
      }
      FENCE();
      sy_set(lds, SY_TAG, epoch + 1u, lane);
      sy_set(lds, SY_TAGOK, 1u, lane);
      BARRIER();
#pragma unroll
      for (int pass = 0; pass < 2; ++pass) {
        float sum = 0.f;
#pragma unroll
        for (int i = 0; i < 5; ++i)
#pragma unroll
          for (int e = 0; e < 4; ++e) sum = dot2(gx[pass][i][e], gw[i][e], sum);
#pragma unroll
        for (int m = 8; m >= 1; m >>= 1) sum += __shfl_xor(sum, m);
        if (gq == 0)
          __hip_atomic_store((GAS unsigned*)(a.ws + WS_GATE) + r0 + 4 * pass + gr, __builtin_bit_cast(unsigned, sigmoidf_(sum)),
                             __ATOMIC_RELAXED, __HIP_MEMORY_SCOPE_AGENT);
      }
    }
    const unsigned tag = epoch + 1u;
    STAMP(1);
#pragma unroll 1
    for (int s = 0; s < 9; ++s) {
      BARRIER();
      if (s == 3 && lane == 0) flag_store(a.ws, WS_FLAGKV + 4 * (32 * (bid & 7) + (bid >> 3)), tag);
    }
    WAIT_VM(0);
    sy_set(lds, SY_GATE, 1u, lane);
    STAMP(2);
    {
      flag_wait(a.ws, WS_FLAGKV + 4 * (32 * (bid >> 5) + (lane & 31)), true, tag);
      STAMP(8);
      u32x4 uv[3][4];
#pragma unroll
      for (int cw = 0; cw < 3; ++cw) {
        const __amdgpu_buffer_rsrc_t ru = make_rsrc(a.ws + WS_KVR + (size_t)(c0 + (cw < nconv ? cw : 0)) * S * 2, S * 2);
#pragma unroll
        for (int i = 0; i < 4; ++i) uv[cw][i] = ld128(ru, (unsigned)(i * 1024 + lane * 16), 0, SC1);
      }
      FENCE();
#pragma unroll
      for (int cw = 0; cw < 3; ++cw) {
        lptr ur = lds + L_AUX + cw * UR_BYTES;
#pragma unroll
        for (int i = 0; i < 4; ++i) *(LAS u32x4*)(ur + i * 1024 + lane * 16) = uv[cw][i];
        if (lane < 32) *(LAS u32x4*)(ur + 4096 + lane * 16) = (u32x4){0u, 0u, 0u, 0u};
      }
      WAIT_LGKM0();
      sy_set(lds, SY_AREADY, 1u, lane);
    }
    STAMP(9);
    FENCE();
    {
      const char* base = (const char*)a.wd + 640 * (xcd >> 2);
#pragma unroll
      for (int t = 0; t < 4; ++t) {
        const int id = min(64 * t + lane, 239), r = id / 5, c = id - 5 * r;
        touch(base + (size_t)(48 * j + r) * (D * 2) + 128 * c, lds0 + L_DUMMY);
      }
    }
    if (bid == 0) {
#pragma unroll
      for (int i = 0; i < 4; ++i) flag_wait(a.ws, WS_FLAGKV + 4 * (lane + 64 * i), true, tag);
      if (lane == 0) flag_store(a.ws, WS_EPOCH, tag);
    }
    sy_wait(lds, SY_WPRE, 4u);
    flag_wait<HPOLL_SLEEP>(a.ws, WS_FLAGH + 4 * (16 * (si >> 1) + (lane & 15)), true, tag);
    sy_set(lds, SY_HREADY, 1u, lane);
    STAMP(10);
    sy_wait(lds, SY_CONV, nconv);
    sy_wait(lds, SY_GSTEP, 1u);
    flag_wait<YPOLL_SLEEP>(a.ws, WS_FLAGY + 4 * (32 * di + (lane & 31)), true, tag);
    STAMP(11);
    {
      const __amdgpu_buffer_rsrc_t ry = make_rsrc(a.ws + WS_Y2T, S * D * 2);
      u32x4 yv[10];
#pragma unroll
      for (int i = 0; i < 10; ++i) {
        const int id = lane + 64 * i;
        yv[i] = ld128(ry, (unsigned)((80 * di + (id >> 3)) * (S * 2) + 64 * si * 2 + (id & 7) * 16), 0, SC1);
      }
#pragma unroll
      for (int i = 0; i < 10; ++i) {
        const int id = lane + 64 * i, w = (id & 7) >> 1;
        const int so = w == 0 ? ystrip_off(0) : w == 1 ? ystrip_off(1) : w == 2 ? ystrip_off(2) : ystrip_off(3);
        *(LAS u32x4*)(lds + so + (id >> 3) * 32 + (id & 1) * 16) = yv[i];
      }
      WAIT_LGKM0();
      sy_set(lds, SY_YREADY, 1u, lane);
    }
    STAMP(12);
    WAIT_VM(0);
    STAMP_DUMP();
    return;
  }

  if (wave >= 4) {
    const int cw = wave - 4;
    const bool active = cw < nconv;
    const int ch = c0 + (active ? cw : 0);
    Filter flt;
    const int rot1 = ROT1 ? (3 * (j >> 3) + (j & 7)) % 10 : 0;
    if (cw == 0) loader<6>(a, lds, 0, 0, mi, fi, rot1, flt, ch, lane);
    else loader<5>(a, lds, cw, 1 + 5 * cw, mi, fi, rot1, flt, ch, lane);
    STAMP(3);
    if (active) {
      conv_wave(a, lds, lds + (cw == 2 ? 2 * STAGE : STAGE + cw * KC_BYTES), lds + L_AUX + cw * UR_BYTES, flt, ch, nconv, lane);
    }
    STAMP_DUMP();
    return;
  }

  unsigned long long probe_t0 = PROBE_AT ? __builtin_amdgcn_s_memrealtime() : 0ull;
  if (PROBE_GLOBAL && tid == 0)
    __hip_atomic_store((GAS unsigned long long*)(a.ws + (32u << 20)) + bid, probe_t0, __ATOMIC_RELAXED, __HIP_MEMORY_SCOPE_AGENT);
  (void)probe_t0;
  f32x4 ag[4][3], au[4][3];
  {
    const int tsi = bid >> 3, tdi = bid & 7;
    const int task = 40 * wave + (lane < 40 ? lane : 0);
    KvTask t0;
    kv_load(a, t0, task, tsi, tdi);
    FENCE();
#pragma unroll
    for (int t = 0; t < 4; ++t)
#pragma unroll
      for (int c = 0; c < 3; ++c) {
        ag[t][c] = (f32x4){0.f, 0.f, 0.f, 0.f}; au[t][c] = (f32x4){0.f, 0.f, 0.f, 0.f};
        asm volatile("" : "+v"(ag[t][c]), "+v"(au[t][c]));
      }
    FENCE();
    STAMP(1);
    if (lane < 40) kv_compute(t0, lds, task, tsi);
    sy_arrive(lds, SY_KV, lane);
    sy_wait(lds, SY_KV, 4u);
    STAMP(2);
    const __amdgpu_buffer_rsrc_t rkv = make_rsrc(a.ws + WS_KVR, S * D * 2);
#pragma unroll
    for (int i = 0; i < 3; ++i) {
      const int id = tid + 256 * i;
      if (id < 640) {
        const int dl = id >> 3, jj = id & 7;
        const u32x4 val = *(LAS u32x4*)(lds + L_T + dl * 128 + ((jj ^ ((dl >> 3) & 7)) << 4));
        __builtin_amdgcn_raw_buffer_store_b128(val, rkv, (unsigned)(((80 * tdi + dl) * S + (1984 - 64 * tsi) + 8 * jj) * 2), 0, SC1);
      }
    }
  }
  const int n = lane & 15, g = lane >> 4;
  {
    const int ws_ = wave & 1, wf = wave >> 1;
    const int q = n >> 2, p = lane & 3;
    const int xb0 = XB + (64 * ws_ + n) * 128 + ((g ^ (n >> 1)) << 4);
    int wb_[3];
#pragma unroll
    for (int c = 0; c < 3; ++c) wb_[c] = GB + (8 * g + q) * 192 + 8 * p + 32 * ((3 * wf + c) ^ (g & 1));
    lptr xa[3][2], wa[3][3];
#pragma unroll
    for (int b = 0; b < 3; ++b) {
#pragma unroll
      for (int kk = 0; kk < 2; ++kk) { xa[b][kk] = lds + b * STAGE + (xb0 ^ (kk << 6)); asm volatile("" : "+v"(xa[b][kk])); }
#pragma unroll
      for (int c = 0; c < 3; ++c) { wa[b][c] = lds + b * STAGE + wb_[c]; asm volatile("" : "+v"(wa[b][c])); }
    }
    auto read_piece = [&](Frags& f, int i, int b, int kk) {
      if (i < 4) {
        f.x[i] = lds_frag(xa[b][kk] + i * 2048);
      } else {
        const int c = (i - 4) >> 2, u = ((i - 4) >> 1) & 1, t = (i - 4) & 1;
        const s16x4 v = lds_tr(wa[b][c] + kk * (32 * 192) + u * (UB - GB) + t * (4 * 192));
        if (u == 0) f.g[c][t] = v; else f.u[c][t] = v;
      }
    };
    auto mma_piece = [&](const Frags& f, int i) {
      const int c = i >> 3, t = (i >> 1) & 3;
      if ((i & 1) == 0) ag[t][c] = MFMA16(join(f.g[c][0], f.g[c][1]), f.x[t], ag[t][c]);
      else au[t][c] = MFMA16(join(f.u[c][0], f.u[c][1]), f.x[t], au[t][c]);
    };
    auto half = [&](const Frags& cur, Frags& nxt, int nb, int nkk) {
#pragma unroll
      for (int i = 0; i < 24; ++i) {
        mma_piece(cur, i); FENCE();
        if (i < 16) { read_piece(nxt, i, nb, nkk); FENCE(); }
      }
    };
    BARRIER();
    PROBE(1);
    STAMP(3);
    STAMPC(28);
    __builtin_amdgcn_s_setprio(M_PRIO);
    Frags f0, f1;
#pragma unroll
    for (int i = 0; i < 16; ++i) read_piece(f0, i, 0, 0);
    FENCE();
    STAMPC(27);
    auto iteration = [&](int b, bool drain) {
      FENCE();
      half(f0, f1, b, 1);
      if (drain) WAIT_VM(0);
      WAIT_LGKM0();
      BARRIER();
      FENCE();
      half(f1, f0, (b + 1) % 3, 0);
    };
#pragma unroll 1
    for (int s3 = 0; s3 < 3; ++s3) {
      iteration(0, s3 == 1);
      iteration(1, false);
      iteration(2, false);
    }
    half(f0, f1, 0, 1);
    const __amdgpu_buffer_rsrc_t rhs = make_rsrc(a.ws + WS_HT, S * F * 2);
    const lptr exch = lds + L_FREE + ws_ * 2048 + lane * 8;
    u32x2 keep[4];
    auto epilogue = [&](int t) {
      u32x2 pk[3];
#pragma unroll
      for (int c = 0; c < 3; ++c) {
        const f32x2 s01 = (f32x2){ag[t][c][0], ag[t][c][1]} * -1.4426950408889634f, s23 = (f32x2){ag[t][c][2], ag[t][c][3]} * -1.4426950408889634f;
        const f32x2 d01 = (f32x2){__builtin_amdgcn_exp2f(s01[0]), __builtin_amdgcn_exp2f(s01[1])} + 1.0f;
        const f32x2 d23 = (f32x2){__builtin_amdgcn_exp2f(s23[0]), __builtin_amdgcn_exp2f(s23[1])} + 1.0f;
        const f32x2 h01 = (f32x2){__builtin_amdgcn_rcpf(d01[0]), __builtin_amdgcn_rcpf(d01[1])} * (f32x2){au[t][c][0], au[t][c][1]};
        const f32x2 h23 = (f32x2){__builtin_amdgcn_rcpf(d23[0]), __builtin_amdgcn_rcpf(d23[1])} * (f32x2){au[t][c][2], au[t][c][3]};
        pk[c] = (u32x2){pack_bf16(h01[0], h01[1]), pack_bf16(h23[0], h23[1])};
      }
      const unsigned base = (unsigned)(((8 * mi + 4 * ws_ + t) * 48 + 3 * fi) * 1024 + lane * 16);
      if (wf == 0) {
        __builtin_amdgcn_raw_buffer_store_b128((u32x4){pk[0][0], pk[0][1], pk[1][0], pk[1][1]}, rhs, base, 0, SC1);
        keep[t] = pk[2];
      } else {
        *(LAS u32x2*)(exch + t * 512) = pk[0];
        __builtin_amdgcn_raw_buffer_store_b128((u32x4){pk[1][0], pk[1][1], pk[2][0], pk[2][1]}, rhs, base + 2048, 0, SC1);
      }
    };
#pragma unroll
    for (int t = 0; t < 4; ++t) {
      FENCE();
#pragma unroll
      for (int c = 0; c < 3; ++c) {
        ag[t][c] = MFMA16(join(f1.g[c][0], f1.g[c][1]), f1.x[t], ag[t][c]);
        au[t][c] = MFMA16(join(f1.u[c][0], f1.u[c][1]), f1.x[t], au[t][c]);
      }
      FENCE();
      if (t == 0) { PROBE(2); STAMP(4); STAMPC(29); }
      if (t > 0) epilogue(t - 1);
    }
    FENCE();
    epilogue(3);
    if (wf == 0) {
      sy_wait(lds, SY_EX + ws_, 1u);
#pragma unroll
      for (int t = 0; t < 4; ++t) {
        const u32x2 o = *(LAS u32x2*)(exch + t * 512);
        __builtin_amdgcn_raw_buffer_store_b128((u32x4){keep[t][0], keep[t][1], o[0], o[1]}, rhs,
                                               (unsigned)(((8 * mi + 4 * ws_ + t) * 48 + 3 * fi + 1) * 1024 + lane * 16), 0, SC1);
      }
    } else {
      WAIT_LGKM0();
      sy_set(lds, SY_EX + ws_, 1u, lane);
    }
  }
  STAMP(5);

  const __amdgpu_buffer_rsrc_t rh = make_rsrc(a.ws + WS_HT, S * F * 2);
  const __amdgpu_buffer_rsrc_t rw = make_rsrc(a.wd, F * D * 2);
  const unsigned wr0 = wave == 0 ? wring_off(0) : wave == 1 ? wring_off(1) : wave == 2 ? wring_off(2) : wring_off(3);
  const int rot = 3 * (j >> 3);
  auto kstep = [&](int b) { const int t = b + rot; return 12 * wave + (t >= 12 ? t - 12 : t); };
  unsigned vwd[5];
#pragma unroll
  for (int i = 0; i < 5; ++i) {
    const int slot = 64 * i + lane, row = slot / 10, ch = slot - 10 * row;
    vwd[i] = (unsigned)(row * (D * 2) + 160 * di + ch * 16 - 1024 * (i & 3));
  }
  auto load_w = [&](int b) {
    const unsigned dst = lds0 + wr0 + (b & 3) * WSLOT, so = (unsigned)kstep(b) * (32 * D * 2);
    dma16xN<4>(rw, dst, vwd, so);
    dma16xN<1>(rw, dst + 4096, vwd + 4, so);
  };
  auto load_w1 = [&](int b, int i) {
    dma16(rw, lds0 + wr0 + (b & 3) * WSLOT + i * 1024, vwd[i] + 1024 * (i & 3), (unsigned)kstep(b) * (32 * D * 2));
  };
  struct HS4 { u32x4 f[4]; };
  HS4 H[HD + 1];
  const unsigned voff = lane * 16;
  auto load_h = [&](HS4& h, int b) {
    const unsigned so = (unsigned)(4 * si * 48 + kstep(b)) * 1024;
#define HPOL " sc1"
    asm volatile("buffer_load_dwordx4 %0, %4, %5, %6 offen" HPOL "\n\tbuffer_load_dwordx4 %1, %4, %5, %7 offen" HPOL "\n\t"
                 "buffer_load_dwordx4 %2, %4, %5, %8 offen" HPOL "\n\tbuffer_load_dwordx4 %3, %4, %5, %9 offen" HPOL
                 : "=&v"(h.f[0]), "=&v"(h.f[1]), "=&v"(h.f[2]), "=&v"(h.f[3])
                 : "v"(voff), "s"(rh), "s"(so), "s"(so + 48 * 1024), "s"(so + 96 * 1024), "s"(so + 144 * 1024) : "memory");
  };
  auto load_h1 = [&](HS4& h, int b, int i) {
    const unsigned so = (unsigned)((4 * si + i) * 48 + kstep(b)) * 1024;
    asm volatile("buffer_load_dwordx4 %0, %1, %2, %3 offen" HPOL : "=&v"(h.f[i]) : "v"(voff), "s"(rh), "s"(so) : "memory");
  };
  const int srow = 64 * si + 16 * wave + n;
  u32x2 qv[5];
  FENCE();
  __builtin_amdgcn_s_setprio(3);
  sy_arrive(lds, SY_EPI, lane);
  STAMP(22);
  FENCE();
  WAIT_VM(0);
  if (sy_arrive(lds, SY_HDONE, lane) == 3u) {
    sy_wait(lds, SY_GATE, 1u);
    const unsigned tag = get_tag(lds);
    if (lane == 0) flag_store(a.ws, WS_FLAGH + 4 * (16 * mi + fi), tag);
    WAIT_VM(0);
  }
  sy_wait(lds, SY_EPI, 4u);
  load_w(0); load_w(1); load_w(2); load_w(3);
  STAMP(23);
  f32x4 acc[4][5];
#pragma unroll
  for (int t = 0; t < 4; ++t)
#pragma unroll
    for (int c = 0; c < 5; ++c) { acc[t][c] = (f32x4){0.f, 0.f, 0.f, 0.f}; asm volatile("" : "+v"(acc[t][c])); }
  struct WF { s16x4 f[5][2]; };
  WF w0, w1;
  const lptr wrd = lds + wr0 + (4 * g + (n >> 2)) * 160 + 8 * (lane & 3);
  auto read_w = [&](WF& w, int p, int b) {
    w.f[p >> 1][p & 1] = lds_tr(wrd + (b & 3) * WSLOT + (p & 1) * (16 * 160) + (p >> 1) * 32);
  };
  STAMP(24);
  WAIT_VM(10);
#pragma unroll
  for (int p = 0; p < 10; ++p) read_w(w0, p, 0);
#pragma unroll
  for (int p = 0; p < 10; ++p) read_w(w1, p, 1);
  WAIT_LGKM0();
  load_w(4);
  load_w(5);
  sy_arrive(lds, SY_WPRE, lane);
  __builtin_amdgcn_s_setprio(G_PRIO);
  STAMP(6);
  sy_wait(lds, SY_HREADY, 1u);
  PROBE(3);
  STAMP(7);
#pragma unroll
  for (int b = 0; b < HD; ++b) load_h(H[b], b);
  const float gate = __builtin_bit_cast(float, __hip_atomic_load((GAS unsigned*)(a.ws + WS_GATE) + srow, __ATOMIC_RELAXED, __HIP_MEMORY_SCOPE_AGENT));
  FENCE();
  auto gstep = [&](const WF& cur, WF& nxt, HS4& h, HS4& hn, int b) {
    asm volatile("s_waitcnt vmcnt(%4) lgkmcnt(0)" : "+v"(h.f[0]), "+v"(h.f[1]), "+v"(h.f[2]), "+v"(h.f[3]) : "n"(g_younger(b)) : "memory");
    FENCE();
#pragma unroll
    for (int i = 0; i < 20; ++i) {
      acc[i & 3][i >> 2] = MFMA16(join(cur.f[i >> 2][0], cur.f[i >> 2][1]), __builtin_bit_cast(bf16x8, h.f[i & 3]), acc[i & 3][i >> 2]);
      FENCE();
      if (i < 8 && (i & 1) == 0 && b + HD < 12) { load_h1(hn, b + HD, i >> 1); FENCE(); }
      if (i == 7 && b == 9) {
#pragma unroll
        for (int c = 0; c < 5; ++c) qv[c] = *(const u32x2*)(a.x + (size_t)srow * D + 80 * di + 16 * c + 4 * g);
        asm volatile("" ::: "memory");
        FENCE();
      }
      if (i < 10 && b >= 1 && b + 1 < 12) { read_w(nxt, i, b + 1); FENCE(); }
      if (i >= 10 && (i & 1) == 0 && b >= 2 && b + 4 < 12) { load_w1(b + 4, (i - 10) >> 1); FENCE(); }
      if (b == YPOLL_STEP && i == 17 && wave == 0) { sy_post(lds0 + L_SYNC + 4 * SY_GSTEP, 1u); FENCE(); }
      if (b == YPOLL_STEP && i == 19 && wave == 0) { wake(); FENCE(); }
    }
  };
#pragma unroll
  for (int b = 0; b < 12; b += 2) {
    gstep(w0, w1, H[b % (HD + 1)], H[(b + HD) % (HD + 1)], b);
    if (b == 0) STAMPI(14);
    gstep(w1, w0, H[(b + 1) % (HD + 1)], H[(b + 1 + HD) % (HD + 1)], b + 1);
    STAMPI(15 + (b >> 1));
  }
  PROBE(4);
  STAMP(8);

  f32x4 bsum[5];
  {
#pragma unroll
    for (int t = 0; t < 4; ++t) {
      if (t != wave) {
#pragma unroll
        for (int c = 0; c < 5; ++c) *(LAS f32x4*)(lds + wr0 + (((t - wave - 1) & 3) * 5 + c) * 1024 + lane * 16) = acc[t][c];
      } else {
#pragma unroll
        for (int c = 0; c < 5; ++c) bsum[c] = acc[t][c];
      }
    }
    sy_arrive(lds, SY_RED, lane);
    sy_wait(lds, SY_RED, 4u);
#pragma unroll
    for (int w = 0; w < 4; ++w) {
      if (w != wave) {
#pragma unroll
        for (int c = 0; c < 5; ++c) bsum[c] += *(LAS f32x4*)(lds + wring_off(w) + (((wave - w - 1) & 3) * 5 + c) * 1024 + lane * 16);
      }
    }
  }
  STAMP(9);
  sy_wait(lds, SY_YREADY, 1u);
  STAMP(10);
  {
    const int q = n >> 2, p = lane & 3;
    const __amdgpu_buffer_rsrc_t ro = make_rsrc(a.out, S * D * 2);
    const lptr ystr = lds + (wave == 0 ? ystrip_off(0) : wave == 1 ? ystrip_off(1) : wave == 2 ? ystrip_off(2) : ystrip_off(3));
#pragma unroll
    for (int c = 0; c < 5; ++c) {
      const s16x4 yv = lds_tr(ystr + (16 * c + 4 * g + q) * 32 + 8 * p);
      const u32x2 y = __builtin_bit_cast(u32x2, yv);
      const float o0 = gate * (bf_lo(qv[c][0]) * bf_lo(y[0])) + (1.f - gate) * bsum[c][0];
      const float o1 = gate * (bf_hi(qv[c][0]) * bf_hi(y[0])) + (1.f - gate) * bsum[c][1];
      const float o2 = gate * (bf_lo(qv[c][1]) * bf_lo(y[1])) + (1.f - gate) * bsum[c][2];
      const float o3 = gate * (bf_hi(qv[c][1]) * bf_hi(y[1])) + (1.f - gate) * bsum[c][3];
      __builtin_amdgcn_raw_buffer_store_b64((u32x2){pack_bf16(o0, o1), pack_bf16(o2, o3)}, ro,
                                            (unsigned)((srow * D + 80 * di + 16 * c + 4 * g) * 2), 0, OUT_AUX);
    }
  }
  if (PROBE_GLOBAL) {
    unsigned long long r = PROBE_GLOBAL == 1 ? ~0ull : 0ull;
    for (int i = 0; i < 4; ++i) {
      const unsigned long long v = __hip_atomic_load((GAS unsigned long long*)(a.ws + (32u << 20)) + lane + 64 * i, __ATOMIC_RELAXED, __HIP_MEMORY_SCOPE_AGENT);
      r = PROBE_GLOBAL == 1 ? min(r, v) : max(r, v);
    }
    for (int m = 32; m >= 1; m >>= 1) { const unsigned long long o = __shfl_xor(r, m); r = PROBE_GLOBAL == 1 ? min(r, o) : max(r, o); }
    probe_t0 = __builtin_amdgcn_readfirstlane((unsigned)r) | ((unsigned long long)__builtin_amdgcn_readfirstlane((unsigned)(r >> 32)) << 32);
  }
  PROBE(5);
  STAMP(11);
}

}

extern "C" void kernel_launch(void* const* d_in, const int* in_sizes, int n_in, void* d_out, int out_size, void* d_ws,
                              size_t ws_size, hipStream_t stream) {
  (void)in_sizes; (void)n_in; (void)out_size; (void)ws_size;
  Args a;
  a.x = (const u16*)d_in[0];
  a.k = (const u16*)d_in[1];
  a.v = (const u16*)d_in[2];
  a.shk = (const u16*)d_in[3];
  a.ssmk = (const u16*)d_in[4];
  a.ssmd = (const u16*)d_in[5];
  a.wg = (const u16*)d_in[7];
  a.wu = (const u16*)d_in[8];
  a.wd = (const u16*)d_in[9];
  a.gw = (const u16*)d_in[10];
  a.ws = (char*)d_ws;
  a.out = (u16*)d_out;
  (void)hipFuncSetAttribute((const void*)fused_kernel, hipFuncAttributeMaxDynamicSharedMemorySize, LDS_BYTES);
  fused_kernel<<<256, 512, LDS_BYTES, stream>>>(a);
}
```

```cpp
#include <hip/hip_runtime.h>
#include <hip/hip_bf16.h>

namespace {

typedef unsigned short u16;
typedef __bf16 bf16x8 __attribute__((ext_vector_type(8)));
typedef __bf16 bf16x2 __attribute__((ext_vector_type(2)));
typedef short s16x4 __attribute__((ext_vector_type(4)));
typedef float f32x2 __attribute__((ext_vector_type(2)));
typedef float f32x4 __attribute__((ext_vector_type(4)));
typedef unsigned u32x2 __attribute__((ext_vector_type(2)));
typedef unsigned u32x4 __attribute__((ext_vector_type(4)));
#define LAS __attribute__((address_space(3)))
#define GAS __attribute__((address_space(1)))
typedef LAS char* lptr;

constexpr int S = 2048, D = 640, F = 1536;
constexpr int SC1 = 16;

constexpr size_t WS_HT = 0;
constexpr size_t WS_KVR = WS_HT + (size_t)S * F * 2;
constexpr size_t WS_Y2T = WS_KVR + (size_t)S * D * 2;
constexpr size_t WS_GATE = WS_Y2T + (size_t)S * D * 2;
constexpr size_t WS_FLAGKV = WS_GATE + (size_t)S * 4;
constexpr size_t WS_FLAGH = WS_FLAGKV + 1024;
constexpr size_t WS_FLAGY = WS_FLAGH + 1024;
constexpr size_t WS_EPOCH = WS_FLAGY + 1024;
constexpr size_t WS_END = WS_EPOCH + 256;

constexpr int STAGE = 40960, XB = 0, GB = 16384, UB = 28672;
constexpr int L_AUX = 3 * STAGE;
constexpr int L_T = L_AUX, UR_BYTES = 4608;
constexpr int L_FREE = L_AUX + 3 * UR_BYTES;
constexpr int L_SYNC = L_FREE + 20480 + 2 * 2560;
constexpr int L_DUMMY = L_SYNC + 128;
constexpr int L_END = L_DUMMY + 256;
constexpr int KC_COPY = 4160, KC_BYTES = 4 * KC_COPY;
constexpr int WSLOT = 5120;
__device__ constexpr int wring_off(int w) { return w == 0 ? 0 : w == 1 ? 20480 : w == 2 ? 2 * STAGE + KC_BYTES : L_FREE; }
__device__ constexpr int ystrip_off(int w) { return w < 2 ? STAGE + 2 * KC_BYTES + w * 2560 : L_FREE + 20480 + (w - 2) * 2560; }
static_assert(2 * STAGE + KC_BYTES + 20480 <= 3 * STAGE && STAGE + 2 * KC_BYTES + 2 * 2560 <= 2 * STAGE, "second-phase LDS map");
enum { SY_LANDED = 0, SY_CONSUMED = 4, SY_KV = 8, SY_SIDE = 9, SY_HDONE = 10, SY_RED = 11, SY_CONV = 12, SY_AREADY = 13,
       SY_HREADY = 14, SY_YREADY = 15, SY_TAG = 16, SY_TAGOK = 17, SY_GSTEP = 18, SY_GATE = 19, SY_EPI = 20, SY_WPRE = 21, SY_EX = 22 };

#define STAMP(i) do {} while (0)
#define STAMPC(i) do {} while (0)
#define STAMPI(i) do {} while (0)
#define STAMP_INIT() do {} while (0)
#define STAMP_DUMP() do {} while (0)
constexpr int LDS_BYTES = L_END;

#define PROBE_AT 0
#define PROBE_US 0
#define PROBE(k) do { if (PROBE_AT == (k)) { while (__builtin_amdgcn_s_memrealtime() - probe_t0 < 100ull * PROBE_US) __builtin_amdgcn_s_sleep(1); } } while (0)
#define PROBE_ENTRY 0
#define PROBE_GLOBAL 0
#define MFMA16(a, b, c) __builtin_amdgcn_mfma_f32_16x16x32_bf16((a), (b), (c), 0, 0, 0)
#define WAIT_VM(n) asm volatile("s_waitcnt vmcnt(" #n ")" ::: "memory")
#define WAIT_LGKM0() asm volatile("s_waitcnt lgkmcnt(0)" ::: "memory")
#define BARRIER() do { asm volatile("" ::: "memory"); __builtin_amdgcn_s_barrier(); asm volatile("" ::: "memory"); } while (0)
#define FENCE() __builtin_amdgcn_sched_barrier(0)
constexpr unsigned SPIN_LDS = 1u << 22, SPIN_MEM = 1u << 16;

__device__ __forceinline__ unsigned pack_bf16(float lo, float hi) {
  f32x2 v = {lo, hi};
  return __builtin_bit_cast(unsigned, __builtin_convertvector(v, bf16x2));
}
__device__ __forceinline__ float bf_lo(unsigned w) { return __builtin_bit_cast(float, w << 16); }
__device__ __forceinline__ float bf_hi(unsigned w) { return __builtin_bit_cast(float, w & 0xffff0000u); }
__device__ __forceinline__ float sigmoidf_(float x) {
  return __builtin_amdgcn_rcpf(1.0f + __builtin_amdgcn_exp2f(x * -1.4426950408889634f));
}
__device__ __forceinline__ __amdgpu_buffer_rsrc_t make_rsrc(const void* p, unsigned bytes) {
  return __builtin_amdgcn_make_buffer_rsrc(const_cast<void*>(p), 0, bytes, 0x00020000);
}
__device__ __forceinline__ unsigned lds_addr(lptr p) { return (unsigned)(size_t)p; }
__device__ __forceinline__ void dma16(__amdgpu_buffer_rsrc_t r, unsigned dst, unsigned voff, unsigned soff) {
  asm volatile("s_mov_b32 m0, %0\n\ts_nop 0\n\tbuffer_load_dwordx4 %1, %2, %3 offen lds"
               :: "s"(dst), "v"(voff), "s"(r), "s"(soff) : "memory");
}
template <int N>
__device__ __forceinline__ void dma16xN(__amdgpu_buffer_rsrc_t r, unsigned dst, const unsigned* v, unsigned soff) {
  static_assert(N >= 1 && N <= 4, "12-bit instruction offset");
  if constexpr (N == 1)
    asm volatile("s_mov_b32 m0, %0\n\ts_nop 0\n\tbuffer_load_dwordx4 %1, %2, %3 offen lds"
                 :: "s"(dst), "v"(v[0]), "s"(r), "s"(soff) : "memory");
  else if constexpr (N == 2)
    asm volatile("s_mov_b32 m0, %0\n\ts_nop 0\n\tbuffer_load_dwordx4 %1, %3, %4 offen lds\n\t"
                 "buffer_load_dwordx4 %2, %3, %4 offen offset:1024 lds"
                 :: "s"(dst), "v"(v[0]), "v"(v[1]), "s"(r), "s"(soff) : "memory");
  else if constexpr (N == 3)
    asm volatile("s_mov_b32 m0, %0\n\ts_nop 0\n\tbuffer_load_dwordx4 %1, %4, %5 offen lds\n\t"
                 "buffer_load_dwordx4 %2, %4, %5 offen offset:1024 lds\n\tbuffer_load_dwordx4 %3, %4, %5 offen offset:2048 lds"
                 :: "s"(dst), "v"(v[0]), "v"(v[1]), "v"(v[2]), "s"(r), "s"(soff) : "memory");
  else
    asm volatile("s_mov_b32 m0, %0\n\ts_nop 0\n\tbuffer_load_dwordx4 %1, %5, %6 offen lds\n\t"
                 "buffer_load_dwordx4 %2, %5, %6 offen offset:1024 lds\n\tbuffer_load_dwordx4 %3, %5, %6 offen offset:2048 lds\n\t"
                 "buffer_load_dwordx4 %4, %5, %6 offen offset:3072 lds"
                 :: "s"(dst), "v"(v[0]), "v"(v[1]), "v"(v[2]), "v"(v[3]), "s"(r), "s"(soff) : "memory");
}
__device__ __forceinline__ void touch(const void* p, unsigned dummy_lds) {
  asm volatile("s_mov_b32 m0, %0\n\ts_nop 0\n\tglobal_load_lds_dword %1, off" :: "s"(dummy_lds), "v"(p) : "memory");
}
__device__ __forceinline__ s16x4 lds_tr(lptr p) { return __builtin_amdgcn_ds_read_tr16_b64_v4i16((LAS s16x4*)p); }
__device__ __forceinline__ bf16x8 join(s16x4 a, s16x4 b) {
  return __builtin_bit_cast(bf16x8, __builtin_shufflevector(a, b, 0, 1, 2, 3, 4, 5, 6, 7));
}
__device__ __forceinline__ bf16x8 lds_frag(lptr p) { return *(LAS bf16x8*)p; }
__device__ __forceinline__ u32x4 ld128(__amdgpu_buffer_rsrc_t r, unsigned voff, unsigned soff, int aux) {
  return aux ? __builtin_bit_cast(u32x4, __builtin_amdgcn_raw_buffer_load_b128(r, voff, soff, SC1))
             : __builtin_bit_cast(u32x4, __builtin_amdgcn_raw_buffer_load_b128(r, voff, soff, 0));
}

__device__ __forceinline__ LAS unsigned* sy(lptr lds, int i) { return (LAS unsigned*)(lds + L_SYNC) + i; }
__device__ __forceinline__ unsigned sy_get(lptr lds, int i) {
  return __builtin_amdgcn_readfirstlane(__hip_atomic_load(sy(lds, i), __ATOMIC_RELAXED, __HIP_MEMORY_SCOPE_WORKGROUP));
}
__device__ __forceinline__ void wake() { asm volatile("s_wakeup" ::: "memory"); }
__device__ __forceinline__ void sy_set(lptr lds, int i, unsigned v, int lane) {
  if (lane == 0) __hip_atomic_store(sy(lds, i), v, __ATOMIC_RELAXED, __HIP_MEMORY_SCOPE_WORKGROUP);
  asm volatile("s_waitcnt lgkmcnt(0)\n\ts_wakeup" ::: "memory");
}
#define TOUCH_H 0
#define LOADER_POST_FIRST 1
#define HPOLL_SLEEP 4
#define YPOLL_SLEEP 8
#define YPOLL_STEP 8
#ifndef M_PRIO
#define M_PRIO 2
#endif
#define G_PRIO 0
#define ROT1 1
#define DMA_SPACE 0
#define C_PRIO 2
#define G_HDEPTH 4
#define OUT_AUX 0
#define H_ACQUIRE 0
#define CODE_LINES 0
#define CODE_WAVE 7
#define PUB_FIRST 1
#define LAND_RD 18
#define TOUCH_DELAY 8
#define TOUCH1 0
#define FILT_ITER 6
#define W7_DELAY 0
#define POLL_AFTER 8
#define LOADER_PRIO 0
#define SLEEP_LDS 4
__device__ __forceinline__ void sy_post(unsigned addr, unsigned v) {
  asm volatile("s_mov_b64 exec, 1\n\tds_write_b32 %0, %1\n\ts_mov_b64 exec, -1" :: "v"(addr), "v"(v) : "memory");
}
__device__ __forceinline__ void sy_wait(lptr lds, int i, unsigned v) {
#pragma unroll 1
  for (unsigned n = 0; n < SPIN_LDS; ++n) {
    if (sy_get(lds, i) >= v) break;
    __builtin_amdgcn_s_sleep(SLEEP_LDS);
  }
  asm volatile("" ::: "memory");
}
__device__ __forceinline__ unsigned sy_arrive(lptr lds, int i, int lane) {
  WAIT_LGKM0();
  unsigned old = 0;
  if (lane == 0) old = __hip_atomic_fetch_add(sy(lds, i), 1u, __ATOMIC_RELAXED, __HIP_MEMORY_SCOPE_WORKGROUP);
  old = __builtin_amdgcn_readfirstlane(old);
  wake();
  return old;
}
__device__ __forceinline__ unsigned min4(u32x4 v) { return min(min(v[0], v[1]), min(v[2], v[3])); }
__device__ __forceinline__ u32x4 sy_get4(lptr lds, int i) { return *(volatile LAS u32x4*)(lds + L_SYNC + 4 * i); }
__device__ __forceinline__ u32x2 sy_get2(lptr lds, int i) { return *(volatile LAS u32x2*)(lds + L_SYNC + 4 * i); }
__device__ __forceinline__ void sy_wait4(lptr lds, int i, unsigned v) {
#pragma unroll 1
  for (unsigned n = 0; n < SPIN_LDS; ++n) {
    if ((unsigned)__builtin_amdgcn_readfirstlane(min4(sy_get4(lds, i))) >= v) break;
    __builtin_amdgcn_s_sleep(SLEEP_LDS);
  }
  asm volatile("" ::: "memory");
}
#define LOADER_SLEEP 1
__device__ __forceinline__ void loader_wait(lptr lds, unsigned v) {
#pragma unroll 1
  for (unsigned n = 0; n < SPIN_LDS; ++n) {
    if ((unsigned)__builtin_amdgcn_readfirstlane(min4(sy_get4(lds, SY_CONSUMED))) >= v) break;
    if (LOADER_SLEEP) __builtin_amdgcn_s_sleep(LOADER_SLEEP);
  }
  asm volatile("" ::: "memory");
}
__device__ __forceinline__ void sy_wait4m(lptr lds, unsigned v) {
#pragma unroll 1
  for (unsigned n = 0; n < SPIN_LDS; ++n) {
    const u32x4 l = sy_get4(lds, SY_LANDED);
    if ((unsigned)__builtin_amdgcn_readfirstlane(min(min(l[0], l[1]), l[2])) >= v) break;
    __builtin_amdgcn_s_sleep(SLEEP_LDS);
  }
  asm volatile("" ::: "memory");
}
__device__ __forceinline__ unsigned get_tag(lptr lds) {
  sy_wait(lds, SY_TAGOK, 1u);
  return sy_get(lds, SY_TAG);
}
__device__ __forceinline__ void flag_store(char* ws, size_t off, unsigned tag) {
  __hip_atomic_store((GAS unsigned*)(ws + off), tag, __ATOMIC_RELAXED, __HIP_MEMORY_SCOPE_AGENT);
}
#define SIDE_PUB_ITER 3
#define POLL_SLEEP 32
template <int SLEEP = POLL_SLEEP>
__device__ __forceinline__ void flag_wait(char* ws, size_t off, bool on, unsigned tag) {
#pragma unroll 1
  for (unsigned n = 0; n < SPIN_MEM; ++n) {
    const unsigned v = __hip_atomic_load((GAS unsigned*)(ws + off), __ATOMIC_RELAXED, __HIP_MEMORY_SCOPE_AGENT);
    if (__all(!on || v == tag)) break;
    __builtin_amdgcn_s_sleep(SLEEP);
  }
  asm volatile("" ::: "memory");
}

struct Args {
  const u16* x; const u16* k; const u16* v; const u16* shk; const u16* ssmk; const u16* ssmd;
  const u16* wg; const u16* wu; const u16* wd; const u16* gw;
  char* ws; u16* out;
};

struct Frags { bf16x8 x[4]; s16x4 g[3][2]; s16x4 u[3][2]; };

__device__ constexpr int tri_start(int t) { return 8 * t - (t * (t - 1)) / 2; }
__device__ constexpr int tri_row(int p) {
  return p < 8 ? 0 : p < 15 ? 1 : p < 21 ? 2 : p < 26 ? 3 : p < 30 ? 4 : p < 33 ? 5 : p < 35 ? 6 : 7;
}

template <int N> __device__ __forceinline__ void wait_vm() { asm volatile("s_waitcnt vmcnt(%0)" ::"n"(N) : "memory"); }

struct Filter { unsigned d[5][6]; unsigned dbits; };
__device__ __forceinline__ void load_filter(const Args& a, Filter& f, int ch, int lane) {
  const __amdgpu_buffer_rsrc_t rk = make_rsrc(a.ssmk + (size_t)ch * S, S * 2);
#pragma unroll
  for (int it = 0; it < 5; ++it) {
    const unsigned off = (unsigned)(16 * (lane + 64 * it) - 32);
    const u32x4 v0 = __builtin_bit_cast(u32x4, __builtin_amdgcn_raw_buffer_load_b128(rk, off, 0, 0));
    const u32x2 v1 = __builtin_bit_cast(u32x2, __builtin_amdgcn_raw_buffer_load_b64(rk, off + 16, 0, 0));
    f.d[it][0] = v0[0]; f.d[it][1] = v0[1]; f.d[it][2] = v0[2]; f.d[it][3] = v0[3]; f.d[it][4] = v1[0]; f.d[it][5] = v1[1];
  }
  f.dbits = a.ssmd[ch];
}

template <int NX>
__device__ __forceinline__ void loader(const Args& a, lptr lds, int lw, int x0, int mi, int fi, int rot, Filter& flt, int ch, int lane) {
  const unsigned lds0 = lds_addr(lds);
  const __amdgpu_buffer_rsrc_t rx = make_rsrc(a.x, S * D * 2);
  const __amdgpu_buffer_rsrc_t rg = make_rsrc(a.wg, D * F * 2);
  const __amdgpu_buffer_rsrc_t ru = make_rsrc(a.wu, D * F * 2);
  unsigned vx[NX], vw[4];
#pragma unroll
  for (int i = 0; i < NX; ++i) {
    const int row = 8 * (x0 + i) + (lane >> 3), c = (lane & 7) ^ ((row >> 1) & 7);
    vx[i] = (unsigned)((128 * mi + row) * (D * 2) + c * 16 - 1024 * (i & 3));
  }
#pragma unroll
  for (int i = 0; i < 4; ++i) {
    const int slot = 64 * (4 * lw + i) + lane, k = slot / 12, chp = slot - 12 * k, c = chp ^ (2 * ((k >> 3) & 1));
    vw[i] = (unsigned)(k * (F * 2) + fi * 192 + c * 16 - 1024 * i);
  }
  const unsigned land_addr = lds0 + L_SYNC + 4 * (SY_LANDED + lw);
  auto slice = [&](int s) { const int k = s + rot; return k >= 10 ? k - 10 : k; };
  auto part_a = [&](int s) {
    const unsigned b = lds0 + (s % 3) * STAGE, so = (unsigned)slice(s) * (64 * F * 2);
    dma16xN<4>(rg, b + GB + 4 * lw * 1024, vw, so);
    if (DMA_SPACE) __builtin_amdgcn_s_sleep(DMA_SPACE);
    dma16xN<4>(ru, b + UB + 4 * lw * 1024, vw, so);
    if (DMA_SPACE) __builtin_amdgcn_s_sleep(DMA_SPACE);
  };
  auto part_b = [&](int s) {
    const unsigned b = lds0 + (s % 3) * STAGE, so = (unsigned)slice(s) * 128;
    dma16xN<4>(rx, b + XB + x0 * 1024, vx, so);
    if (DMA_SPACE) __builtin_amdgcn_s_sleep(DMA_SPACE);
    dma16xN<NX - 4>(rx, b + XB + (x0 + 4) * 1024, vx + 4, so);
  };
  constexpr int P = NX + 8;
  part_a(0); part_b(0);
  part_a(1); part_b(1);
  STAMP(1);
  wait_vm<P>();
  STAMP(2);
  BARRIER();
  part_a(2); part_b(2);
#pragma unroll
  for (int s = 0; s < 9; ++s) {
    if (s == 8) wait_vm<11>(); else wait_vm<P>();
    STAMPI(8 + 2 * s);
    BARRIER();
    STAMPI(9 + 2 * s);
    if (s < 7) { part_a(s + 3); part_b(s + 3); }
    if (s == 7) { FENCE(); load_filter(a, flt, ch, lane); FENCE(); asm volatile("" ::: "memory"); }
  }
}

struct KvTask { u32x4 kr[7], vr[4], wk[4]; };
__device__ __forceinline__ void kv_load(const Args& a, KvTask& t, int task, int tsi, int tdi) {
  const int dc = task % 10, sb = task / 10;
  const int s0 = 64 * tsi + 4 * sb, d0 = 80 * tdi + 8 * dc;
#pragma unroll
  for (int r = 0; r < 7; ++r) {
    const int s = s0 - 3 + r;
    t.kr[r] = *(const u32x4*)(a.k + (size_t)(s < 0 ? 0 : s) * D + d0);
  }
#pragma unroll
  for (int r = 0; r < 4; ++r) t.vr[r] = *(const u32x4*)(a.v + (size_t)(s0 + r) * D + d0);
#pragma unroll
  for (int i = 0; i < 4; ++i) t.wk[i] = *(const u32x4*)(a.shk + (size_t)d0 * 4 + i * 8);
}
__device__ __forceinline__ float dot2(unsigned x, unsigned y, float c) {
  return __builtin_amdgcn_fdot2_f32_bf16(__builtin_bit_cast(bf16x2, x), __builtin_bit_cast(bf16x2, y), c, false);
}
__device__ __forceinline__ float dot2z(unsigned x, unsigned y) {
  float r;
  asm("v_dot2_f32_bf16 %0, %1, %2, 0" : "=v"(r) : "v"(x), "v"(y));
  return r;
}
__device__ __forceinline__ void kv_compute(KvTask& t, lptr lds, int task, int tsi) {
  const int dc = task % 10, sb = task / 10;
  if (tsi == 0 && sb == 0) {
#pragma unroll
    for (int r = 0; r < 3; ++r) t.kr[r] = (u32x4){0u, 0u, 0u, 0u};
  }
  const int sg = 15 - sb;
  lptr dst = lds + L_T + 8 * dc * 128 + (((sg >> 1) ^ (dc & 7)) << 4) + ((sg & 1) << 3);
#pragma unroll
  for (int c = 0; c < 8; ++c) {
    const unsigned w01 = t.wk[c >> 1][2 * (c & 1)], w23 = t.wk[c >> 1][2 * (c & 1) + 1];
    const unsigned sel = (c & 1) ? 0x03020706u : 0x01000504u;
    unsigned pr[6];
#pragma unroll
    for (int i = 0; i < 6; ++i) pr[i] = __builtin_amdgcn_perm(t.kr[i + 1][c >> 1], t.kr[i][c >> 1], sel);
    float o[4];
#pragma unroll
    for (int r = 0; r < 4; ++r) {
      const float ks = dot2(pr[r + 2], w01, dot2z(pr[r], w23));
      o[r] = ks * ((c & 1) ? bf_hi(t.vr[r][c >> 1]) : bf_lo(t.vr[r][c >> 1]));
    }
    *(LAS u32x2*)(dst + c * 128) = (u32x2){pack_bf16(o[3], o[2]), pack_bf16(o[1], o[0])};
  }
}

__device__ __forceinline__ void conv_wave(const Args& a, lptr lds, lptr kc, lptr ur, const Filter& flt, int ch, int nconv, int lane) {
#pragma unroll
  for (int it = 0; it < 5; ++it) {
    const int c = lane + 64 * it;
    if (c < 260) {
      const unsigned a0 = flt.d[it][0], a1 = flt.d[it][1], a2 = flt.d[it][2], a3 = flt.d[it][3], a4 = flt.d[it][4], a5 = flt.d[it][5];
      const unsigned e0 = __builtin_amdgcn_alignbit(a1, a0, 16), e1 = __builtin_amdgcn_alignbit(a2, a1, 16),
                     e2 = __builtin_amdgcn_alignbit(a3, a2, 16), e3 = __builtin_amdgcn_alignbit(a4, a3, 16),
                     e4 = __builtin_amdgcn_alignbit(a5, a4, 16);
      lptr w = kc + 16 * c;
      *(LAS u32x4*)(w) = (u32x4){a0, a1, a2, a3};
      *(LAS u32x4*)(w + KC_COPY) = (u32x4){e0, e1, e2, e3};
      *(LAS u32x4*)(w + 2 * KC_COPY) = (u32x4){a1, a2, a3, a4};
      *(LAS u32x4*)(w + 3 * KC_COPY) = (u32x4){e1, e2, e3, e4};
    }
  }
  const float dcoef = bf_lo(flt.dbits);
  WAIT_LGKM0();
  sy_wait(lds, SY_AREADY, 1u);
  sy_wait(lds, SY_EPI, 4u);
  __builtin_amdgcn_s_setprio(C_PRIO);
  STAMP(4);
  STAMPC(28);

  const int m = lane & 15, g = lane >> 4;
  const int rho = (m + 1) & 3;
  lptr abase = kc + rho * KC_COPY + 2 * (8 * g + m - 31 - rho);
  lptr bbase = ur + 2 * (2000 - 16 * m + 8 * g) - 7 * 512;
  f32x4 acc[8];
#pragma unroll
  for (int t = 0; t < 8; ++t) acc[t] = (f32x4){0.f, 0.f, 0.f, 0.f};
  struct CF { s16x4 a[8][2]; bf16x8 b[8]; };
  auto cread = [&](CF& f, int p, int i) {
    if (p < 16) f.a[p >> 1][p & 1] = *(LAS s16x4*)(abase + 64 * i + 512 * (p >> 1) + 8 * (p & 1));
    else f.b[p - 16] = lds_frag(bbase + 64 * i + 512 * (7 - (p - 16)));
  };
  auto cmma = [&](const CF& f, int p) {
    const int t = tri_row(p), u = p - tri_start(t);
    acc[t + u] = MFMA16(join(f.a[t][0], f.a[t][1]), f.b[u], acc[t + u]);
  };
  auto residue = [&](const CF& cur, CF& nxt, int inext) {
#pragma unroll
    for (int p = 0; p < 36; ++p) {
      cmma(cur, p); FENCE();
      if (p < 24) { cread(nxt, p, inext); FENCE(); }
    }
  };
  CF c0, c1;
#pragma unroll
  for (int p = 0; p < 24; ++p) cread(c0, p, 1);
#pragma unroll 1
  for (int i = 1; i <= 8; i += 2) {
    FENCE();
    residue(c0, c1, i + 1);
    residue(c1, c0, (i + 2 <= 8) ? i + 2 : 8);
  }
  STAMP(5);
  STAMPC(29);
  const __amdgpu_buffer_rsrc_t ry = make_rsrc(a.ws + WS_Y2T + (size_t)ch * S * 2, S * 2);
#pragma unroll
  for (int t = 0; t < 8; t += 2) {
    unsigned pk[2][2];
#pragma unroll
    for (int e = 0; e < 2; ++e) {
      const int s = 256 * (t + e) + 16 * m + 4 * g;
      const u32x2 kv = *(LAS u32x2*)(ur + 2 * (2044 - s));
      pk[e][0] = pack_bf16(acc[t + e][0] + bf_hi(kv[1]) * dcoef, acc[t + e][1] + bf_lo(kv[1]) * dcoef);
      pk[e][1] = pack_bf16(acc[t + e][2] + bf_hi(kv[0]) * dcoef, acc[t + e][3] + bf_lo(kv[0]) * dcoef);
    }
    const auto r0 = __builtin_amdgcn_permlane16_swap(pk[0][0], pk[1][0], false, false);
    const auto r1 = __builtin_amdgcn_permlane16_swap(pk[0][1], pk[1][1], false, false);
    const u32x4 o = {r0[0], r1[0], r0[1], r1[1]};
    const unsigned off = 2 * (256 * (t + (g & 1)) + 16 * m + 8 * (g >> 1));
    __builtin_amdgcn_raw_buffer_store_b128(o, ry, off, 0, SC1);
  }
  STAMP(6);
  WAIT_VM(0);
  STAMP(7);
  if (sy_arrive(lds, SY_CONV, lane) == (unsigned)(nconv - 1)) {
    const unsigned tag = get_tag(lds);
    if (lane == 0) flag_store(a.ws, WS_FLAGY + 4 * blockIdx.x, tag);
  }
}

constexpr int HD = G_HDEPTH;
__device__ constexpr int g_younger(int b) {
  int count = 0, last = 0;
  for (int k = 0; k < HD; ++k) { count += 4; if (k == b) last = count; }
  count += 1;
  for (int s = 0; s < b; ++s) {
    if (s + HD <= 11) { count += 4; if (s + HD == b) last = count; }
    if (s == 9) count += 5;
    if (s >= 2 && s <= 7) { count += 5; if (s + 4 == b + 1) last = count; }
  }
  return count - last;
}
__global__ __launch_bounds__(512, 2) void fused_kernel(Args a) {
  extern __shared__ __attribute__((aligned(16))) char smem[];
  unsigned long long probe_entry = 0;
  if (PROBE_ENTRY) { asm volatile("s_memrealtime %0\n\ts_waitcnt lgkmcnt(0)" : "=s"(probe_entry) :: "memory"); }
  lptr lds = (lptr)smem;
  const int tid = threadIdx.x, lane = tid & 63;
  const int wave = __builtin_amdgcn_readfirstlane(tid >> 6);
  const int bid = blockIdx.x;
  const int xcd = bid & 7, j = bid >> 3;
  const int mi = 8 * (xcd & 1) + (j & 7), fi = 4 * (xcd >> 1) + (j >> 3);
  const int si = 8 * (xcd & 3) + (j & 7), di = 4 * (xcd >> 2) + (j >> 3);
  const int c0 = (5 * bid) >> 1, nconv = 2 + (bid & 1);
  const unsigned lds0 = lds_addr(lds);
  STAMP_INIT();
  STAMP(0);

  if (wave == 7) {
    if (lane < 32) *sy(lds, lane) = 0u;
    WAIT_LGKM0();
  }
  BARRIER();

  if (wave == 7) {
    const unsigned epoch = __hip_atomic_load((GAS unsigned*)(a.ws + WS_EPOCH), __ATOMIC_RELAXED, __HIP_MEMORY_SCOPE_AGENT);
    {
      const int r0 = 128 * mi + 8 * fi, gr = lane >> 4, gq = lane & 15;
      u32x4 gx[2][5], gw[5];
#pragma unroll
      for (int i = 0; i < 5; ++i) {
        gw[i] = ((const u32x4*)a.gw)[gq + 16 * i];
        gx[0][i] = ((const u32x4*)(a.x + (size_t)(r0 + gr) * D))[gq + 16 * i];
        gx[1][i] = ((const u32x4*)(a.x + (size_t)(r0 + 4 + gr) * D))[gq + 16 * i];
      }
      FENCE();
      sy_set(lds, SY_TAG, epoch + 1u, lane);
      sy_set(lds, SY_TAGOK, 1u, lane);
      BARRIER();
#pragma unroll
      for (int pass = 0; pass < 2; ++pass) {
        float sum = 0.f;
#pragma unroll
        for (int i = 0; i < 5; ++i)
#pragma unroll
          for (int e = 0; e < 4; ++e) sum = dot2(gx[pass][i][e], gw[i][e], sum);
#pragma unroll
        for (int m = 8; m >= 1; m >>= 1) sum += __shfl_xor(sum, m);
        if (gq == 0)
          __hip_atomic_store((GAS unsigned*)(a.ws + WS_GATE) + r0 + 4 * pass + gr, __builtin_bit_cast(unsigned, sigmoidf_(sum)),
                             __ATOMIC_RELAXED, __HIP_MEMORY_SCOPE_AGENT);
      }
    }
    const unsigned tag = epoch + 1u;
    STAMP(1);
#pragma unroll 1
    for (int s = 0; s < 9; ++s) {
      BARRIER();
      if (s == 3 && lane == 0) flag_store(a.ws, WS_FLAGKV + 4 * (32 * (bid & 7) + (bid >> 3)), tag);
    }
    WAIT_VM(0);
    sy_set(lds, SY_GATE, 1u, lane);
    STAMP(2);
    {
      flag_wait(a.ws, WS_FLAGKV + 4 * (32 * (bid >> 5) + (lane & 31)), true, tag);
      STAMP(8);
      u32x4 uv[3][4];
#pragma unroll
      for (int cw = 0; cw < 3; ++cw) {
        const __amdgpu_buffer_rsrc_t ru = make_rsrc(a.ws + WS_KVR + (size_t)(c0 + (cw < nconv ? cw : 0)) * S * 2, S * 2);
#pragma unroll
        for (int i = 0; i < 4; ++i) uv[cw][i] = ld128(ru, (unsigned)(i * 1024 + lane * 16), 0, SC1);
      }
      FENCE();
#pragma unroll
      for (int cw = 0; cw < 3; ++cw) {
        lptr ur = lds + L_AUX + cw * UR_BYTES;
#pragma unroll
        for (int i = 0; i < 4; ++i) *(LAS u32x4*)(ur + i * 1024 + lane * 16) = uv[cw][i];
        if (lane < 32) *(LAS u32x4*)(ur + 4096 + lane * 16) = (u32x4){0u, 0u, 0u, 0u};
      }
      WAIT_LGKM0();
      sy_set(lds, SY_AREADY, 1u, lane);
    }
    STAMP(9);
    FENCE();
    {
      const char* base = (const char*)a.wd + 640 * (xcd >> 2);
#pragma unroll
      for (int t = 0; t < 4; ++t) {
        const int id = min(64 * t + lane, 239), r = id / 5, c = id - 5 * r;
        touch(base + (size_t)(48 * j + r) * (D * 2) + 128 * c, lds0 + L_DUMMY);
      }
    }
    if (bid == 0) {
#pragma unroll
      for (int i = 0; i < 4; ++i) flag_wait(a.ws, WS_FLAGKV + 4 * (lane + 64 * i), true, tag);
      if (lane == 0) flag_store(a.ws, WS_EPOCH, tag);
    }
    sy_wait(lds, SY_WPRE, 4u);
    flag_wait<HPOLL_SLEEP>(a.ws, WS_FLAGH + 4 * (16 * (si >> 1) + (lane & 15)), true, tag);
    sy_set(lds, SY_HREADY, 1u, lane);
    STAMP(10);
    sy_wait(lds, SY_CONV, nconv);
    sy_wait(lds, SY_GSTEP, 1u);
    flag_wait<YPOLL_SLEEP>(a.ws, WS_FLAGY + 4 * (32 * di + (lane & 31)), true, tag);
    STAMP(11);
    {
      const __amdgpu_buffer_rsrc_t ry = make_rsrc(a.ws + WS_Y2T, S * D * 2);
      u32x4 yv[10];
#pragma unroll
      for (int i = 0; i < 10; ++i) {
        const int id = lane + 64 * i;
        yv[i] = ld128(ry, (unsigned)((80 * di + (id >> 3)) * (S * 2) + 64 * si * 2 + (id & 7) * 16), 0, SC1);
      }
#pragma unroll
      for (int i = 0; i < 10; ++i) {
        const int id = lane + 64 * i, w = (id & 7) >> 1;
        const int so = w == 0 ? ystrip_off(0) : w == 1 ? ystrip_off(1) : w == 2 ? ystrip_off(2) : ystrip_off(3);
        *(LAS u32x4*)(lds + so + (id >> 3) * 32 + (id & 1) * 16) = yv[i];
      }
      WAIT_LGKM0();
      sy_set(lds, SY_YREADY, 1u, lane);
    }
    STAMP(12);
    WAIT_VM(0);
    STAMP_DUMP();
    return;
  }

  if (wave >= 4) {
    const int cw = wave - 4;
    const bool active = cw < nconv;
    const int ch = c0 + (active ? cw : 0);
    Filter flt;
    const int rot1 = ROT1 ? (3 * (j >> 3) + (j & 7)) % 10 : 0;
    if (cw == 0) loader<6>(a, lds, 0, 0, mi, fi, rot1, flt, ch, lane);
    else loader<5>(a, lds, cw, 1 + 5 * cw, mi, fi, rot1, flt, ch, lane);
    STAMP(3);
    if (active) {
      conv_wave(a, lds, lds + (cw == 2 ? 2 * STAGE : STAGE + cw * KC_BYTES), lds + L_AUX + cw * UR_BYTES, flt, ch, nconv, lane);
    }
    STAMP_DUMP();
    return;
  }

  unsigned long long probe_t0 = PROBE_ENTRY ? probe_entry : PROBE_AT ? __builtin_amdgcn_s_memrealtime() : 0ull;
  if (PROBE_GLOBAL && tid == 0)
    __hip_atomic_store((GAS unsigned long long*)(a.ws + (32u << 20)) + bid, probe_t0, __ATOMIC_RELAXED, __HIP_MEMORY_SCOPE_AGENT);
  (void)probe_t0;
  f32x4 ag[4][3], au[4][3];
  {
    const int tsi = bid >> 3, tdi = bid & 7;
    const int task = 40 * wave + (lane < 40 ? lane : 0);
    KvTask t0;
    kv_load(a, t0, task, tsi, tdi);
    FENCE();
#pragma unroll
    for (int t = 0; t < 4; ++t)
#pragma unroll
      for (int c = 0; c < 3; ++c) {
        ag[t][c] = (f32x4){0.f, 0.f, 0.f, 0.f}; au[t][c] = (f32x4){0.f, 0.f, 0.f, 0.f};
        asm volatile("" : "+v"(ag[t][c]), "+v"(au[t][c]));
      }
    FENCE();
    STAMP(1);
    if (lane < 40) kv_compute(t0, lds, task, tsi);
    sy_arrive(lds, SY_KV, lane);
    sy_wait(lds, SY_KV, 4u);
    STAMP(2);
    const __amdgpu_buffer_rsrc_t rkv = make_rsrc(a.ws + WS_KVR, S * D * 2);
#pragma unroll
    for (int i = 0; i < 3; ++i) {
      const int id = tid + 256 * i;
      if (id < 640) {
        const int dl = id >> 3, jj = id & 7;
        const u32x4 val = *(LAS u32x4*)(lds + L_T + dl * 128 + ((jj ^ ((dl >> 3) & 7)) << 4));
        __builtin_amdgcn_raw_buffer_store_b128(val, rkv, (unsigned)(((80 * tdi + dl) * S + (1984 - 64 * tsi) + 8 * jj) * 2), 0, SC1);
      }
    }
  }
  const int n = lane & 15, g = lane >> 4;
  {
    const int ws_ = wave & 1, wf = wave >> 1;
    const int q = n >> 2, p = lane & 3;
    const int xb0 = XB + (64 * ws_ + n) * 128 + ((g ^ (n >> 1)) << 4);
    int wb_[3];
#pragma unroll
    for (int c = 0; c < 3; ++c) wb_[c] = GB + (8 * g + q) * 192 + 8 * p + 32 * ((3 * wf + c) ^ (g & 1));
    lptr xa[3][2], wa[3][3];
#pragma unroll
    for (int b = 0; b < 3; ++b) {
#pragma unroll
      for (int kk = 0; kk < 2; ++kk) { xa[b][kk] = lds + b * STAGE + (xb0 ^ (kk << 6)); asm volatile("" : "+v"(xa[b][kk])); }
#pragma unroll
      for (int c = 0; c < 3; ++c) { wa[b][c] = lds + b * STAGE + wb_[c]; asm volatile("" : "+v"(wa[b][c])); }
    }
    auto read_piece = [&](Frags& f, int i, int b, int kk) {
      if (i < 4) {
        f.x[i] = lds_frag(xa[b][kk] + i * 2048);
      } else {
        const int c = (i - 4) >> 2, u = ((i - 4) >> 1) & 1, t = (i - 4) & 1;
        const s16x4 v = lds_tr(wa[b][c] + kk * (32 * 192) + u * (UB - GB) + t * (4 * 192));
        if (u == 0) f.g[c][t] = v; else f.u[c][t] = v;
      }
    };
    auto mma_piece = [&](const Frags& f, int i) {
      const int c = i >> 3, t = (i >> 1) & 3;
      if ((i & 1) == 0) ag[t][c] = MFMA16(join(f.g[c][0], f.g[c][1]), f.x[t], ag[t][c]);
      else au[t][c] = MFMA16(join(f.u[c][0], f.u[c][1]), f.x[t], au[t][c]);
    };
    auto half = [&](const Frags& cur, Frags& nxt, int nb, int nkk) {
#pragma unroll
      for (int i = 0; i < 24; ++i) {
        mma_piece(cur, i); FENCE();
        if (i < 16) { read_piece(nxt, i, nb, nkk); FENCE(); }
      }
    };
    BARRIER();
    PROBE(1);
    STAMP(3);
    STAMPC(28);
    __builtin_amdgcn_s_setprio(M_PRIO);
    Frags f0, f1;
#pragma unroll
    for (int i = 0; i < 16; ++i) read_piece(f0, i, 0, 0);
    FENCE();
    STAMPC(27);
    auto iteration = [&](int b, bool drain) {
      FENCE();
      half(f0, f1, b, 1);
      if (drain) WAIT_VM(0);
      WAIT_LGKM0();
      BARRIER();
      FENCE();
      half(f1, f0, (b + 1) % 3, 0);
    };
#pragma unroll 1
    for (int s3 = 0; s3 < 3; ++s3) {
      iteration(0, s3 == 1);
      iteration(1, false);
      iteration(2, false);
    }
    half(f0, f1, 0, 1);
    const __amdgpu_buffer_rsrc_t rhs = make_rsrc(a.ws + WS_HT, S * F * 2);
    const lptr exch = lds + L_FREE + ws_ * 2048 + lane * 8;
    u32x2 keep[4];
    auto epilogue = [&](int t) {
      u32x2 pk[3];
#pragma unroll
      for (int c = 0; c < 3; ++c) {
        const f32x2 s01 = (f32x2){ag[t][c][0], ag[t][c][1]} * -1.4426950408889634f, s23 = (f32x2){ag[t][c][2], ag[t][c][3]} * -1.4426950408889634f;
        const f32x2 d01 = (f32x2){__builtin_amdgcn_exp2f(s01[0]), __builtin_amdgcn_exp2f(s01[1])} + 1.0f;
        const f32x2 d23 = (f32x2){__builtin_amdgcn_exp2f(s23[0]), __builtin_amdgcn_exp2f(s23[1])} + 1.0f;
        const f32x2 h01 = (f32x2){__builtin_amdgcn_rcpf(d01[0]), __builtin_amdgcn_rcpf(d01[1])} * (f32x2){au[t][c][0], au[t][c][1]};
        const f32x2 h23 = (f32x2){__builtin_amdgcn_rcpf(d23[0]), __builtin_amdgcn_rcpf(d23[1])} * (f32x2){au[t][c][2], au[t][c][3]};
        pk[c] = (u32x2){pack_bf16(h01[0], h01[1]), pack_bf16(h23[0], h23[1])};
      }
      const unsigned base = (unsigned)(((8 * mi + 4 * ws_ + t) * 48 + 3 * fi) * 1024 + lane * 16);
      if (wf == 0) {
        __builtin_amdgcn_raw_buffer_store_b128((u32x4){pk[0][0], pk[0][1], pk[1][0], pk[1][1]}, rhs, base, 0, SC1);
        keep[t] = pk[2];
      } else {
        *(LAS u32x2*)(exch + t * 512) = pk[0];
        __builtin_amdgcn_raw_buffer_store_b128((u32x4){pk[1][0], pk[1][1], pk[2][0], pk[2][1]}, rhs, base + 2048, 0, SC1);
      }
    };
#pragma unroll
    for (int t = 0; t < 4; ++t) {
      FENCE();
#pragma unroll
      for (int c = 0; c < 3; ++c) {
        ag[t][c] = MFMA16(join(f1.g[c][0], f1.g[c][1]), f1.x[t], ag[t][c]);
        au[t][c] = MFMA16(join(f1.u[c][0], f1.u[c][1]), f1.x[t], au[t][c]);
      }
      FENCE();
      if (t == 0) { PROBE(2); STAMP(4); STAMPC(29); }
      if (t > 0) epilogue(t - 1);
    }
    FENCE();
    epilogue(3);
    if (wf == 0) {
      sy_wait(lds, SY_EX + ws_, 1u);
#pragma unroll
      for (int t = 0; t < 4; ++t) {
        const u32x2 o = *(LAS u32x2*)(exch + t * 512);
        __builtin_amdgcn_raw_buffer_store_b128((u32x4){keep[t][0], keep[t][1], o[0], o[1]}, rhs,
                                               (unsigned)(((8 * mi + 4 * ws_ + t) * 48 + 3 * fi + 1) * 1024 + lane * 16), 0, SC1);
      }
    } else {
      WAIT_LGKM0();
      sy_set(lds, SY_EX + ws_, 1u, lane);
    }
  }
  STAMP(5);

  const __amdgpu_buffer_rsrc_t rh = make_rsrc(a.ws + WS_HT, S * F * 2);
  const __amdgpu_buffer_rsrc_t rw = make_rsrc(a.wd, F * D * 2);
  const unsigned wr0 = wave == 0 ? wring_off(0) : wave == 1 ? wring_off(1) : wave == 2 ? wring_off(2) : wring_off(3);
  const int rot = 3 * (j >> 3);
  auto kstep = [&](int b) { const int t = b + rot; return 12 * wave + (t >= 12 ? t - 12 : t); };
  unsigned vwd[5];
#pragma unroll
  for (int i = 0; i < 5; ++i) {
    const int slot = 64 * i + lane, row = slot / 10, ch = slot - 10 * row;
    vwd[i] = (unsigned)(row * (D * 2) + 160 * di + ch * 16 - 1024 * (i & 3));
  }
  auto load_w = [&](int b) {
    const unsigned dst = lds0 + wr0 + (b & 3) * WSLOT, so = (unsigned)kstep(b) * (32 * D * 2);
    dma16xN<4>(rw, dst, vwd, so);
    dma16xN<1>(rw, dst + 4096, vwd + 4, so);
  };
  auto load_w1 = [&](int b, int i) {
    dma16(rw, lds0 + wr0 + (b & 3) * WSLOT + i * 1024, vwd[i] + 1024 * (i & 3), (unsigned)kstep(b) * (32 * D * 2));
  };
  struct HS4 { u32x4 f[4]; };
  HS4 H[HD + 1];
  const unsigned voff = lane * 16;
  auto load_h = [&](HS4& h, int b) {
    const unsigned so = (unsigned)(4 * si * 48 + kstep(b)) * 1024;
#define HPOL " sc1"
    asm volatile("buffer_load_dwordx4 %0, %4, %5, %6 offen" HPOL "\n\tbuffer_load_dwordx4 %1, %4, %5, %7 offen" HPOL "\n\t"
                 "buffer_load_dwordx4 %2, %4, %5, %8 offen" HPOL "\n\tbuffer_load_dwordx4 %3, %4, %5, %9 offen" HPOL
                 : "=&v"(h.f[0]), "=&v"(h.f[1]), "=&v"(h.f[2]), "=&v"(h.f[3])
                 : "v"(voff), "s"(rh), "s"(so), "s"(so + 48 * 1024), "s"(so + 96 * 1024), "s"(so + 144 * 1024) : "memory");
  };
  auto load_h1 = [&](HS4& h, int b, int i) {
    const unsigned so = (unsigned)((4 * si + i) * 48 + kstep(b)) * 1024;
    asm volatile("buffer_load_dwordx4 %0, %1, %2, %3 offen" HPOL : "=&v"(h.f[i]) : "v"(voff), "s"(rh), "s"(so) : "memory");
  };
  const int srow = 64 * si + 16 * wave + n;
  u32x2 qv[5];
  FENCE();
  __builtin_amdgcn_s_setprio(3);
  sy_arrive(lds, SY_EPI, lane);
  STAMP(22);
  FENCE();
  WAIT_VM(0);
  if (sy_arrive(lds, SY_HDONE, lane) == 3u) {
    sy_wait(lds, SY_GATE, 1u);
    const unsigned tag = get_tag(lds);
    if (lane == 0) flag_store(a.ws, WS_FLAGH + 4 * (16 * mi + fi), tag);
    WAIT_VM(0);
  }
  sy_wait(lds, SY_EPI, 4u);
  load_w(0); load_w(1); load_w(2); load_w(3);
  STAMP(23);
  f32x4 acc[4][5];
#pragma unroll
  for (int t = 0; t < 4; ++t)
#pragma unroll
    for (int c = 0; c < 5; ++c) { acc[t][c] = (f32x4){0.f, 0.f, 0.f, 0.f}; asm volatile("" : "+v"(acc[t][c])); }
  struct WF { s16x4 f[5][2]; };
  WF w0, w1;
  const lptr wrd = lds + wr0 + (4 * g + (n >> 2)) * 160 + 8 * (lane & 3);
  auto read_w = [&](WF& w, int p, int b) {
    w.f[p >> 1][p & 1] = lds_tr(wrd + (b & 3) * WSLOT + (p & 1) * (16 * 160) + (p >> 1) * 32);
  };
  STAMP(24);
  WAIT_VM(10);
#pragma unroll
  for (int p = 0; p < 10; ++p) read_w(w0, p, 0);
#pragma unroll
  for (int p = 0; p < 10; ++p) read_w(w1, p, 1);
  WAIT_LGKM0();
  load_w(4);
  load_w(5);
  sy_arrive(lds, SY_WPRE, lane);
  __builtin_amdgcn_s_setprio(G_PRIO);
  STAMP(6);
  sy_wait(lds, SY_HREADY, 1u);
  PROBE(3);
  STAMP(7);
#pragma unroll
  for (int b = 0; b < HD; ++b) load_h(H[b], b);
  const float gate = __builtin_bit_cast(float, __hip_atomic_load((GAS unsigned*)(a.ws + WS_GATE) + srow, __ATOMIC_RELAXED, __HIP_MEMORY_SCOPE_AGENT));
  FENCE();
  auto gstep = [&](const WF& cur, WF& nxt, HS4& h, HS4& hn, int b) {
    asm volatile("s_waitcnt vmcnt(%4) lgkmcnt(0)" : "+v"(h.f[0]), "+v"(h.f[1]), "+v"(h.f[2]), "+v"(h.f[3]) : "n"(g_younger(b)) : "memory");
    FENCE();
#pragma unroll
    for (int i = 0; i < 20; ++i) {
      acc[i & 3][i >> 2] = MFMA16(join(cur.f[i >> 2][0], cur.f[i >> 2][1]), __builtin_bit_cast(bf16x8, h.f[i & 3]), acc[i & 3][i >> 2]);
      FENCE();
      if (i < 8 && (i & 1) == 0 && b + HD < 12) { load_h1(hn, b + HD, i >> 1); FENCE(); }
      if (i == 7 && b == 9) {
#pragma unroll
        for (int c = 0; c < 5; ++c) qv[c] = *(const u32x2*)(a.x + (size_t)srow * D + 80 * di + 16 * c + 4 * g);
        asm volatile("" ::: "memory");
        FENCE();
      }
      if (i < 10 && b >= 1 && b + 1 < 12) { read_w(nxt, i, b + 1); FENCE(); }
      if (i >= 10 && (i & 1) == 0 && b >= 2 && b + 4 < 12) { load_w1(b + 4, (i - 10) >> 1); FENCE(); }
      if (b == YPOLL_STEP && i == 17 && wave == 0) { sy_post(lds0 + L_SYNC + 4 * SY_GSTEP, 1u); FENCE(); }
      if (b == YPOLL_STEP && i == 19 && wave == 0) { wake(); FENCE(); }
    }
  };
#pragma unroll
  for (int b = 0; b < 12; b += 2) {
    gstep(w0, w1, H[b % (HD + 1)], H[(b + HD) % (HD + 1)], b);
    if (b == 0) STAMPI(14);
    gstep(w1, w0, H[(b + 1) % (HD + 1)], H[(b + 1 + HD) % (HD + 1)], b + 1);
    STAMPI(15 + (b >> 1));
  }
  PROBE(4);
  STAMP(8);

  f32x4 bsum[5];
  {
#pragma unroll
    for (int t = 0; t < 4; ++t) {
      if (t != wave) {
#pragma unroll
        for (int c = 0; c < 5; ++c) *(LAS f32x4*)(lds + wr0 + (((t - wave - 1) & 3) * 5 + c) * 1024 + lane * 16) = acc[t][c];
      } else {
#pragma unroll
        for (int c = 0; c < 5; ++c) bsum[c] = acc[t][c];
      }
    }
    sy_arrive(lds, SY_RED, lane);
    sy_wait(lds, SY_RED, 4u);
#pragma unroll
    for (int w = 0; w < 4; ++w) {
      if (w != wave) {
#pragma unroll
        for (int c = 0; c < 5; ++c) bsum[c] += *(LAS f32x4*)(lds + wring_off(w) + (((wave - w - 1) & 3) * 5 + c) * 1024 + lane * 16);
      }
    }
  }
  STAMP(9);
  sy_wait(lds, SY_YREADY, 1u);
  STAMP(10);
  {
    const int q = n >> 2, p = lane & 3;
    const __amdgpu_buffer_rsrc_t ro = make_rsrc(a.out, S * D * 2);
    const lptr ystr = lds + (wave == 0 ? ystrip_off(0) : wave == 1 ? ystrip_off(1) : wave == 2 ? ystrip_off(2) : ystrip_off(3));
#pragma unroll
    for (int c = 0; c < 5; ++c) {
      const s16x4 yv = lds_tr(ystr + (16 * c + 4 * g + q) * 32 + 8 * p);
      const u32x2 y = __builtin_bit_cast(u32x2, yv);
      const float o0 = gate * (bf_lo(qv[c][0]) * bf_lo(y[0])) + (1.f - gate) * bsum[c][0];
      const float o1 = gate * (bf_hi(qv[c][0]) * bf_hi(y[0])) + (1.f - gate) * bsum[c][1];
      const float o2 = gate * (bf_lo(qv[c][1]) * bf_lo(y[1])) + (1.f - gate) * bsum[c][2];
      const float o3 = gate * (bf_hi(qv[c][1]) * bf_hi(y[1])) + (1.f - gate) * bsum[c][3];
      __builtin_amdgcn_raw_buffer_store_b64((u32x2){pack_bf16(o0, o1), pack_bf16(o2, o3)}, ro,
                                            (unsigned)((srow * D + 80 * di + 16 * c + 4 * g) * 2), 0, OUT_AUX);
    }
  }
  if (PROBE_GLOBAL) {
    unsigned long long r = PROBE_GLOBAL == 1 ? ~0ull : 0ull;
    for (int i = 0; i < 4; ++i) {
      const unsigned long long v = __hip_atomic_load((GAS unsigned long long*)(a.ws + (32u << 20)) + lane + 64 * i, __ATOMIC_RELAXED, __HIP_MEMORY_SCOPE_AGENT);
      r = PROBE_GLOBAL == 1 ? min(r, v) : max(r, v);
    }
    for (int m = 32; m >= 1; m >>= 1) { const unsigned long long o = __shfl_xor(r, m); r = PROBE_GLOBAL == 1 ? min(r, o) : max(r, o); }
    probe_t0 = __builtin_amdgcn_readfirstlane((unsigned)r) | ((unsigned long long)__builtin_amdgcn_readfirstlane((unsigned)(r >> 32)) << 32);
  }
  PROBE(5);
  STAMP(11);
}

}

extern "C" void kernel_launch(void* const* d_in, const int* in_sizes, int n_in, void* d_out, int out_size, void* d_ws,
                              size_t ws_size, hipStream_t stream) {
  (void)in_sizes; (void)n_in; (void)out_size; (void)ws_size;
  Args a;
  a.x = (const u16*)d_in[0];
  a.k = (const u16*)d_in[1];
  a.v = (const u16*)d_in[2];
  a.shk = (const u16*)d_in[3];
  a.ssmk = (const u16*)d_in[4];
  a.ssmd = (const u16*)d_in[5];
  a.wg = (const u16*)d_in[7];
  a.wu = (const u16*)d_in[8];
  a.wd = (const u16*)d_in[9];
  a.gw = (const u16*)d_in[10];
  a.ws = (char*)d_ws;
  a.out = (u16*)d_out;
  (void)hipFuncSetAttribute((const void*)fused_kernel, hipFuncAttributeMaxDynamicSharedMemorySize, LDS_BYTES);
  fused_kernel<<<256, 512, LDS_BYTES, stream>>>(a);
}
```

```cpp
#include <hip/hip_runtime.h>
#include <hip/hip_bf16.h>

namespace {

typedef unsigned short u16;
typedef __bf16 bf16x8 __attribute__((ext_vector_type(8)));
typedef __bf16 bf16x2 __attribute__((ext_vector_type(2)));
typedef short s16x4 __attribute__((ext_vector_type(4)));
typedef float f32x2 __attribute__((ext_vector_type(2)));
typedef float f32x4 __attribute__((ext_vector_type(4)));
typedef unsigned u32x2 __attribute__((ext_vector_type(2)));
typedef unsigned u32x4 __attribute__((ext_vector_type(4)));
#define LAS __attribute__((address_space(3)))
#define GAS __attribute__((address_space(1)))
typedef LAS char* lptr;

constexpr int S = 2048, D = 640, F = 1536;
constexpr int SC1 = 16;

constexpr size_t WS_HT = 0;
constexpr size_t WS_KVR = WS_HT + (size_t)S * F * 2;
constexpr size_t WS_Y2T = WS_KVR + (size_t)S * D * 2;
constexpr size_t WS_GATE = WS_Y2T + (size_t)S * D * 2;
constexpr size_t WS_FLAGKV = WS_GATE + (size_t)S * 4;
constexpr size_t WS_FLAGH = WS_FLAGKV + 1024;
constexpr size_t WS_FLAGY = WS_FLAGH + 1024;
constexpr size_t WS_EPOCH = WS_FLAGY + 1024;
constexpr size_t WS_END = WS_EPOCH + 256;

constexpr int STAGE = 40960, XB = 0, GB = 16384, UB = 28672;
constexpr int L_AUX = 3 * STAGE;
constexpr int L_T = L_AUX, UR_BYTES = 4608;
constexpr int L_FREE = L_AUX + 3 * UR_BYTES;
constexpr int L_SYNC = L_FREE + 20480 + 2 * 2560;
constexpr int L_DUMMY = L_SYNC + 128;
constexpr int L_END = L_DUMMY + 256;
constexpr int KC_COPY = 4160, KC_BYTES = 4 * KC_COPY;
constexpr int WSLOT = 5120;
__device__ constexpr int wring_off(int w) { return w == 0 ? 0 : w == 1 ? 20480 : w == 2 ? 2 * STAGE + KC_BYTES : L_FREE; }
__device__ constexpr int ystrip_off(int w) { return w < 2 ? STAGE + 2 * KC_BYTES + w * 2560 : L_FREE + 20480 + (w - 2) * 2560; }
static_assert(2 * STAGE + KC_BYTES + 20480 <= 3 * STAGE && STAGE + 2 * KC_BYTES + 2 * 2560 <= 2 * STAGE, "second-phase LDS map");
enum { SY_LANDED = 0, SY_CONSUMED = 4, SY_KV = 8, SY_SIDE = 9, SY_HDONE = 10, SY_RED = 11, SY_CONV = 12, SY_AREADY = 13,
       SY_HREADY = 14, SY_YREADY = 15, SY_TAG = 16, SY_TAGOK = 17, SY_GSTEP = 18, SY_GATE = 19, SY_EPI = 20, SY_WPRE = 21, SY_EX = 22 };

#define STAMP(i) do {} while (0)
#define STAMPC(i) do {} while (0)
#define STAMPI(i) do {} while (0)
#define STAMP_INIT() do {} while (0)
#define STAMP_DUMP() do {} while (0)
constexpr int LDS_BYTES = L_END;

#define PROBE_AT 0
#define PROBE_US 0
#define PROBE(k) do { if (PROBE_AT == (k)) { while (__builtin_amdgcn_s_memrealtime() - probe_t0 < 100ull * PROBE_US) __builtin_amdgcn_s_sleep(1); } } while (0)
#define PROBE_ENTRY 0
#define PROBE_GLOBAL 0
#define MFMA16(a, b, c) __builtin_amdgcn_mfma_f32_16x16x32_bf16((a), (b), (c), 0, 0, 0)
#define WAIT_VM(n) asm volatile("s_waitcnt vmcnt(" #n ")" ::: "memory")
#define WAIT_LGKM0() asm volatile("s_waitcnt lgkmcnt(0)" ::: "memory")
#define BARRIER() do { asm volatile("" ::: "memory"); __builtin_amdgcn_s_barrier(); asm volatile("" ::: "memory"); } while (0)
#define FENCE() __builtin_amdgcn_sched_barrier(0)
constexpr unsigned SPIN_LDS = 1u << 22, SPIN_MEM = 1u << 16;

__device__ __forceinline__ unsigned pack_bf16(float lo, float hi) {
  f32x2 v = {lo, hi};
  return __builtin_bit_cast(unsigned, __builtin_convertvector(v, bf16x2));
}
__device__ __forceinline__ float bf_lo(unsigned w) { return __builtin_bit_cast(float, w << 16); }
__device__ __forceinline__ float bf_hi(unsigned w) { return __builtin_bit_cast(float, w & 0xffff0000u); }
__device__ __forceinline__ float sigmoidf_(float x) {
  return __builtin_amdgcn_rcpf(1.0f + __builtin_amdgcn_exp2f(x * -1.4426950408889634f));
}
__device__ __forceinline__ __amdgpu_buffer_rsrc_t make_rsrc(const void* p, unsigned bytes) {
  return __builtin_amdgcn_make_buffer_rsrc(const_cast<void*>(p), 0, bytes, 0x00020000);
}
__device__ __forceinline__ unsigned lds_addr(lptr p) { return (unsigned)(size_t)p; }
__device__ __forceinline__ void dma16(__amdgpu_buffer_rsrc_t r, unsigned dst, unsigned voff, unsigned soff) {
  asm volatile("s_mov_b32 m0, %0\n\ts_nop 0\n\tbuffer_load_dwordx4 %1, %2, %3 offen lds"
               :: "s"(dst), "v"(voff), "s"(r), "s"(soff) : "memory");
}
template <int N>
__device__ __forceinline__ void dma16xN(__amdgpu_buffer_rsrc_t r, unsigned dst, const unsigned* v, unsigned soff) {
  static_assert(N >= 1 && N <= 4, "12-bit instruction offset");
  if constexpr (N == 1)
    asm volatile("s_mov_b32 m0, %0\n\ts_nop 0\n\tbuffer_load_dwordx4 %1, %2, %3 offen lds"
                 :: "s"(dst), "v"(v[0]), "s"(r), "s"(soff) : "memory");
  else if constexpr (N == 2)
    asm volatile("s_mov_b32 m0, %0\n\ts_nop 0\n\tbuffer_load_dwordx4 %1, %3, %4 offen lds\n\t"
                 "buffer_load_dwordx4 %2, %3, %4 offen offset:1024 lds"
                 :: "s"(dst), "v"(v[0]), "v"(v[1]), "s"(r), "s"(soff) : "memory");
  else if constexpr (N == 3)
    asm volatile("s_mov_b32 m0, %0\n\ts_nop 0\n\tbuffer_load_dwordx4 %1, %4, %5 offen lds\n\t"
                 "buffer_load_dwordx4 %2, %4, %5 offen offset:1024 lds\n\tbuffer_load_dwordx4 %3, %4, %5 offen offset:2048 lds"
                 :: "s"(dst), "v"(v[0]), "v"(v[1]), "v"(v[2]), "s"(r), "s"(soff) : "memory");
  else
    asm volatile("s_mov_b32 m0, %0\n\ts_nop 0\n\tbuffer_load_dwordx4 %1, %5, %6 offen lds\n\t"
                 "buffer_load_dwordx4 %2, %5, %6 offen offset:1024 lds\n\tbuffer_load_dwordx4 %3, %5, %6 offen offset:2048 lds\n\t"
                 "buffer_load_dwordx4 %4, %5, %6 offen offset:3072 lds"
                 :: "s"(dst), "v"(v[0]), "v"(v[1]), "v"(v[2]), "v"(v[3]), "s"(r), "s"(soff) : "memory");
}
__device__ __forceinline__ void touch(const void* p, unsigned dummy_lds) {
  asm volatile("s_mov_b32 m0, %0\n\ts_nop 0\n\tglobal_load_lds_dword %1, off" :: "s"(dummy_lds), "v"(p) : "memory");
}
__device__ __forceinline__ s16x4 lds_tr(lptr p) { return __builtin_amdgcn_ds_read_tr16_b64_v4i16((LAS s16x4*)p); }
__device__ __forceinline__ bf16x8 join(s16x4 a, s16x4 b) {
  return __builtin_bit_cast(bf16x8, __builtin_shufflevector(a, b, 0, 1, 2, 3, 4, 5, 6, 7));
}
__device__ __forceinline__ bf16x8 lds_frag(lptr p) { return *(LAS bf16x8*)p; }
__device__ __forceinline__ u32x4 ld128(__amdgpu_buffer_rsrc_t r, unsigned voff, unsigned soff, int aux) {
  return aux ? __builtin_bit_cast(u32x4, __builtin_amdgcn_raw_buffer_load_b128(r, voff, soff, SC1))
             : __builtin_bit_cast(u32x4, __builtin_amdgcn_raw_buffer_load_b128(r, voff, soff, 0));
}

__device__ __forceinline__ LAS unsigned* sy(lptr lds, int i) { return (LAS unsigned*)(lds + L_SYNC) + i; }
__device__ __forceinline__ unsigned sy_get(lptr lds, int i) {
  return __builtin_amdgcn_readfirstlane(__hip_atomic_load(sy(lds, i), __ATOMIC_RELAXED, __HIP_MEMORY_SCOPE_WORKGROUP));
}
__device__ __forceinline__ void wake() { asm volatile("s_wakeup" ::: "memory"); }
__device__ __forceinline__ void sy_set(lptr lds, int i, unsigned v, int lane) {
  if (lane == 0) __hip_atomic_store(sy(lds, i), v, __ATOMIC_RELAXED, __HIP_MEMORY_SCOPE_WORKGROUP);
  asm volatile("s_waitcnt lgkmcnt(0)\n\ts_wakeup" ::: "memory");
}
#define TOUCH_H 0
#define LOADER_POST_FIRST 1
#define HPOLL_SLEEP 4
#define YPOLL_SLEEP 8
#define YPOLL_STEP 8
#ifndef M_PRIO
#define M_PRIO 2
#endif
#define G_PRIO 0
#define ROT1 1
#define DMA_SPACE 0
#define C_PRIO 2
#define G_HDEPTH 4
#define OUT_AUX 0
#define H_ACQUIRE 0
#define CODE_LINES 0
#define CODE_WAVE 7
#define PUB_FIRST 1
#define WD_TOUCH_AT 7
#define LAND_RD 18
#define TOUCH_DELAY 8
#define TOUCH1 0
#define FILT_ITER 6
#define W7_DELAY 0
#define POLL_AFTER 8
#define LOADER_PRIO 0
#define SLEEP_LDS 4
__device__ __forceinline__ void sy_post(unsigned addr, unsigned v) {
  asm volatile("s_mov_b64 exec, 1\n\tds_write_b32 %0, %1\n\ts_mov_b64 exec, -1" :: "v"(addr), "v"(v) : "memory");
}
__device__ __forceinline__ void sy_wait(lptr lds, int i, unsigned v) {
#pragma unroll 1
  for (unsigned n = 0; n < SPIN_LDS; ++n) {
    if (sy_get(lds, i) >= v) break;
    __builtin_amdgcn_s_sleep(SLEEP_LDS);
  }
  asm volatile("" ::: "memory");
}
__device__ __forceinline__ unsigned sy_arrive(lptr lds, int i, int lane) {
  WAIT_LGKM0();
  unsigned old = 0;
  if (lane == 0) old = __hip_atomic_fetch_add(sy(lds, i), 1u, __ATOMIC_RELAXED, __HIP_MEMORY_SCOPE_WORKGROUP);
  old = __builtin_amdgcn_readfirstlane(old);
  wake();
  return old;
}
__device__ __forceinline__ unsigned min4(u32x4 v) { return min(min(v[0], v[1]), min(v[2], v[3])); }
__device__ __forceinline__ u32x4 sy_get4(lptr lds, int i) { return *(volatile LAS u32x4*)(lds + L_SYNC + 4 * i); }
__device__ __forceinline__ u32x2 sy_get2(lptr lds, int i) { return *(volatile LAS u32x2*)(lds + L_SYNC + 4 * i); }
__device__ __forceinline__ void sy_wait4(lptr lds, int i, unsigned v) {
#pragma unroll 1
  for (unsigned n = 0; n < SPIN_LDS; ++n) {
    if ((unsigned)__builtin_amdgcn_readfirstlane(min4(sy_get4(lds, i))) >= v) break;
    __builtin_amdgcn_s_sleep(SLEEP_LDS);
  }
  asm volatile("" ::: "memory");
}
#define LOADER_SLEEP 1
__device__ __forceinline__ void loader_wait(lptr lds, unsigned v) {
#pragma unroll 1
  for (unsigned n = 0; n < SPIN_LDS; ++n) {
    if ((unsigned)__builtin_amdgcn_readfirstlane(min4(sy_get4(lds, SY_CONSUMED))) >= v) break;
    if (LOADER_SLEEP) __builtin_amdgcn_s_sleep(LOADER_SLEEP);
  }
  asm volatile("" ::: "memory");
}
__device__ __forceinline__ void sy_wait4m(lptr lds, unsigned v) {
#pragma unroll 1
  for (unsigned n = 0; n < SPIN_LDS; ++n) {
    const u32x4 l = sy_get4(lds, SY_LANDED);
    if ((unsigned)__builtin_amdgcn_readfirstlane(min(min(l[0], l[1]), l[2])) >= v) break;
    __builtin_amdgcn_s_sleep(SLEEP_LDS);
  }
  asm volatile("" ::: "memory");
}
__device__ __forceinline__ unsigned get_tag(lptr lds) {
  sy_wait(lds, SY_TAGOK, 1u);
  return sy_get(lds, SY_TAG);
}
__device__ __forceinline__ void flag_store(char* ws, size_t off, unsigned tag) {
  __hip_atomic_store((GAS unsigned*)(ws + off), tag, __ATOMIC_RELAXED, __HIP_MEMORY_SCOPE_AGENT);
}
#define SIDE_PUB_ITER 3
#define POLL_SLEEP 32
template <int SLEEP = POLL_SLEEP>
__device__ __forceinline__ void flag_wait(char* ws, size_t off, bool on, unsigned tag) {
#pragma unroll 1
  for (unsigned n = 0; n < SPIN_MEM; ++n) {
    const unsigned v = __hip_atomic_load((GAS unsigned*)(ws + off), __ATOMIC_RELAXED, __HIP_MEMORY_SCOPE_AGENT);
    if (__all(!on || v == tag)) break;
    __builtin_amdgcn_s_sleep(SLEEP);
  }
  asm volatile("" ::: "memory");
}

struct Args {
  const u16* x; const u16* k; const u16* v; const u16* shk; const u16* ssmk; const u16* ssmd;
  const u16* wg; const u16* wu; const u16* wd; const u16* gw;
  char* ws; u16* out;
};

struct Frags { bf16x8 x[4]; s16x4 g[3][2]; s16x4 u[3][2]; };

__device__ constexpr int tri_start(int t) { return 8 * t - (t * (t - 1)) / 2; }
__device__ constexpr int tri_row(int p) {
  return p < 8 ? 0 : p < 15 ? 1 : p < 21 ? 2 : p < 26 ? 3 : p < 30 ? 4 : p < 33 ? 5 : p < 35 ? 6 : 7;
}

template <int N> __device__ __forceinline__ void wait_vm() { asm volatile("s_waitcnt vmcnt(%0)" ::"n"(N) : "memory"); }

struct Filter { unsigned d[5][6]; unsigned dbits; };
__device__ __forceinline__ void load_filter(const Args& a, Filter& f, int ch, int lane) {
  const __amdgpu_buffer_rsrc_t rk = make_rsrc(a.ssmk + (size_t)ch * S, S * 2);
#pragma unroll
  for (int it = 0; it < 5; ++it) {
    const unsigned off = (unsigned)(16 * (lane + 64 * it) - 32);
    const u32x4 v0 = __builtin_bit_cast(u32x4, __builtin_amdgcn_raw_buffer_load_b128(rk, off, 0, 0));
    const u32x2 v1 = __builtin_bit_cast(u32x2, __builtin_amdgcn_raw_buffer_load_b64(rk, off + 16, 0, 0));
    f.d[it][0] = v0[0]; f.d[it][1] = v0[1]; f.d[it][2] = v0[2]; f.d[it][3] = v0[3]; f.d[it][4] = v1[0]; f.d[it][5] = v1[1];
  }
  f.dbits = a.ssmd[ch];
}

template <int NX>
__device__ __forceinline__ void loader(const Args& a, lptr lds, int lw, int x0, int mi, int fi, int rot, Filter& flt, int ch, int lane) {
  const unsigned lds0 = lds_addr(lds);
  const __amdgpu_buffer_rsrc_t rx = make_rsrc(a.x, S * D * 2);
  const __amdgpu_buffer_rsrc_t rg = make_rsrc(a.wg, D * F * 2);
  const __amdgpu_buffer_rsrc_t ru = make_rsrc(a.wu, D * F * 2);
  unsigned vx[NX], vw[4];
#pragma unroll
  for (int i = 0; i < NX; ++i) {
    const int row = 8 * (x0 + i) + (lane >> 3), c = (lane & 7) ^ ((row >> 1) & 7);
    vx[i] = (unsigned)((128 * mi + row) * (D * 2) + c * 16 - 1024 * (i & 3));
  }
#pragma unroll
  for (int i = 0; i < 4; ++i) {
    const int slot = 64 * (4 * lw + i) + lane, k = slot / 12, chp = slot - 12 * k, c = chp ^ (2 * ((k >> 3) & 1));
    vw[i] = (unsigned)(k * (F * 2) + fi * 192 + c * 16 - 1024 * i);
  }
  const unsigned land_addr = lds0 + L_SYNC + 4 * (SY_LANDED + lw);
  auto slice = [&](int s) { const int k = s + rot; return k >= 10 ? k - 10 : k; };
  auto part_a = [&](int s) {
    const unsigned b = lds0 + (s % 3) * STAGE, so = (unsigned)slice(s) * (64 * F * 2);
    dma16xN<4>(rg, b + GB + 4 * lw * 1024, vw, so);
    if (DMA_SPACE) __builtin_amdgcn_s_sleep(DMA_SPACE);
    dma16xN<4>(ru, b + UB + 4 * lw * 1024, vw, so);
    if (DMA_SPACE) __builtin_amdgcn_s_sleep(DMA_SPACE);
  };
  auto part_b = [&](int s) {
    const unsigned b = lds0 + (s % 3) * STAGE, so = (unsigned)slice(s) * 128;
    dma16xN<4>(rx, b + XB + x0 * 1024, vx, so);
    if (DMA_SPACE) __builtin_amdgcn_s_sleep(DMA_SPACE);
    dma16xN<NX - 4>(rx, b + XB + (x0 + 4) * 1024, vx + 4, so);
  };
  constexpr int P = NX + 8;
  part_a(0); part_b(0);
  part_a(1); part_b(1);
  STAMP(1);
  wait_vm<P>();
  STAMP(2);
  BARRIER();
  part_a(2); part_b(2);
#pragma unroll
  for (int s = 0; s < 9; ++s) {
    if (s == 8) wait_vm<11>(); else wait_vm<P>();
    STAMPI(8 + 2 * s);
    BARRIER();
    STAMPI(9 + 2 * s);
    if (s < 7) { part_a(s + 3); part_b(s + 3); }
    if (s == 7) { FENCE(); load_filter(a, flt, ch, lane); FENCE(); asm volatile("" ::: "memory"); }
  }
}

struct KvTask { u32x4 kr[7], vr[4], wk[4]; };
__device__ __forceinline__ void kv_load(const Args& a, KvTask& t, int task, int tsi, int tdi) {
  const int dc = task % 10, sb = task / 10;
  const int s0 = 64 * tsi + 4 * sb, d0 = 80 * tdi + 8 * dc;
#pragma unroll
  for (int r = 0; r < 7; ++r) {
    const int s = s0 - 3 + r;
    t.kr[r] = *(const u32x4*)(a.k + (size_t)(s < 0 ? 0 : s) * D + d0);
  }
#pragma unroll
  for (int r = 0; r < 4; ++r) t.vr[r] = *(const u32x4*)(a.v + (size_t)(s0 + r) * D + d0);
#pragma unroll
  for (int i = 0; i < 4; ++i) t.wk[i] = *(const u32x4*)(a.shk + (size_t)d0 * 4 + i * 8);
}
__device__ __forceinline__ float dot2(unsigned x, unsigned y, float c) {
  return __builtin_amdgcn_fdot2_f32_bf16(__builtin_bit_cast(bf16x2, x), __builtin_bit_cast(bf16x2, y), c, false);
}
__device__ __forceinline__ float dot2z(unsigned x, unsigned y) {
  float r;
  asm("v_dot2_f32_bf16 %0, %1, %2, 0" : "=v"(r) : "v"(x), "v"(y));
  return r;
}
__device__ __forceinline__ void kv_compute(KvTask& t, lptr lds, int task, int tsi) {
  const int dc = task % 10, sb = task / 10;
  if (tsi == 0 && sb == 0) {
#pragma unroll
    for (int r = 0; r < 3; ++r) t.kr[r] = (u32x4){0u, 0u, 0u, 0u};
  }
  const int sg = 15 - sb;
  lptr dst = lds + L_T + 8 * dc * 128 + (((sg >> 1) ^ (dc & 7)) << 4) + ((sg & 1) << 3);
#pragma unroll
  for (int c = 0; c < 8; ++c) {
    const unsigned w01 = t.wk[c >> 1][2 * (c & 1)], w23 = t.wk[c >> 1][2 * (c & 1) + 1];
    const unsigned sel = (c & 1) ? 0x03020706u : 0x01000504u;
    unsigned pr[6];
#pragma unroll
    for (int i = 0; i < 6; ++i) pr[i] = __builtin_amdgcn_perm(t.kr[i + 1][c >> 1], t.kr[i][c >> 1], sel);
    float o[4];
#pragma unroll
    for (int r = 0; r < 4; ++r) {
      const float ks = dot2(pr[r + 2], w01, dot2z(pr[r], w23));
      o[r] = ks * ((c & 1) ? bf_hi(t.vr[r][c >> 1]) : bf_lo(t.vr[r][c >> 1]));
    }
    *(LAS u32x2*)(dst + c * 128) = (u32x2){pack_bf16(o[3], o[2]), pack_bf16(o[1], o[0])};
  }
}

__device__ __forceinline__ void conv_wave(const Args& a, lptr lds, lptr kc, lptr ur, const Filter& flt, int ch, int nconv, int lane) {
#pragma unroll
  for (int it = 0; it < 5; ++it) {
    const int c = lane + 64 * it;
    if (c < 260) {
      const unsigned a0 = flt.d[it][0], a1 = flt.d[it][1], a2 = flt.d[it][2], a3 = flt.d[it][3], a4 = flt.d[it][4], a5 = flt.d[it][5];
      const unsigned e0 = __builtin_amdgcn_alignbit(a1, a0, 16), e1 = __builtin_amdgcn_alignbit(a2, a1, 16),
                     e2 = __builtin_amdgcn_alignbit(a3, a2, 16), e3 = __builtin_amdgcn_alignbit(a4, a3, 16),
                     e4 = __builtin_amdgcn_alignbit(a5, a4, 16);
      lptr w = kc + 16 * c;
      *(LAS u32x4*)(w) = (u32x4){a0, a1, a2, a3};
      *(LAS u32x4*)(w + KC_COPY) = (u32x4){e0, e1, e2, e3};
      *(LAS u32x4*)(w + 2 * KC_COPY) = (u32x4){a1, a2, a3, a4};
      *(LAS u32x4*)(w + 3 * KC_COPY) = (u32x4){e1, e2, e3, e4};
    }
  }
  const float dcoef = bf_lo(flt.dbits);
  WAIT_LGKM0();
  sy_wait(lds, SY_AREADY, 1u);
  sy_wait(lds, SY_EPI, 4u);
  __builtin_amdgcn_s_setprio(C_PRIO);
  STAMP(4);
  STAMPC(28);

  const int m = lane & 15, g = lane >> 4;
  const int rho = (m + 1) & 3;
  lptr abase = kc + rho * KC_COPY + 2 * (8 * g + m - 31 - rho);
  lptr bbase = ur + 2 * (2000 - 16 * m + 8 * g) - 7 * 512;
  f32x4 acc[8];
#pragma unroll
  for (int t = 0; t < 8; ++t) acc[t] = (f32x4){0.f, 0.f, 0.f, 0.f};
  struct CF { s16x4 a[8][2]; bf16x8 b[8]; };
  auto cread = [&](CF& f, int p, int i) {
    if (p < 16) f.a[p >> 1][p & 1] = *(LAS s16x4*)(abase + 64 * i + 512 * (p >> 1) + 8 * (p & 1));
    else f.b[p - 16] = lds_frag(bbase + 64 * i + 512 * (7 - (p - 16)));
  };
  auto cmma = [&](const CF& f, int p) {
    const int t = tri_row(p), u = p - tri_start(t);
    acc[t + u] = MFMA16(join(f.a[t][0], f.a[t][1]), f.b[u], acc[t + u]);
  };
  auto residue = [&](const CF& cur, CF& nxt, int inext) {
#pragma unroll
    for (int p = 0; p < 36; ++p) {
      cmma(cur, p); FENCE();
      if (p < 24) { cread(nxt, p, inext); FENCE(); }
    }
  };
  CF c0, c1;
#pragma unroll
  for (int p = 0; p < 24; ++p) cread(c0, p, 1);
#pragma unroll 1
  for (int i = 1; i <= 8; i += 2) {
    FENCE();
    residue(c0, c1, i + 1);
    residue(c1, c0, (i + 2 <= 8) ? i + 2 : 8);
  }
  STAMP(5);
  STAMPC(29);
  const __amdgpu_buffer_rsrc_t ry = make_rsrc(a.ws + WS_Y2T + (size_t)ch * S * 2, S * 2);
#pragma unroll
  for (int t = 0; t < 8; t += 2) {
    unsigned pk[2][2];
#pragma unroll
    for (int e = 0; e < 2; ++e) {
      const int s = 256 * (t + e) + 16 * m + 4 * g;
      const u32x2 kv = *(LAS u32x2*)(ur + 2 * (2044 - s));
      pk[e][0] = pack_bf16(acc[t + e][0] + bf_hi(kv[1]) * dcoef, acc[t + e][1] + bf_lo(kv[1]) * dcoef);
      pk[e][1] = pack_bf16(acc[t + e][2] + bf_hi(kv[0]) * dcoef, acc[t + e][3] + bf_lo(kv[0]) * dcoef);
    }
    const auto r0 = __builtin_amdgcn_permlane16_swap(pk[0][0], pk[1][0], false, false);
    const auto r1 = __builtin_amdgcn_permlane16_swap(pk[0][1], pk[1][1], false, false);
    const u32x4 o = {r0[0], r1[0], r0[1], r1[1]};
    const unsigned off = 2 * (256 * (t + (g & 1)) + 16 * m + 8 * (g >> 1));
    __builtin_amdgcn_raw_buffer_store_b128(o, ry, off, 0, SC1);
  }
  STAMP(6);
  WAIT_VM(0);
  STAMP(7);
  if (sy_arrive(lds, SY_CONV, lane) == (unsigned)(nconv - 1)) {
    const unsigned tag = get_tag(lds);
    if (lane == 0) flag_store(a.ws, WS_FLAGY + 4 * blockIdx.x, tag);
  }
}

constexpr int HD = G_HDEPTH;
__device__ constexpr int g_younger(int b) {
  int count = 0, last = 0;
  for (int k = 0; k < HD; ++k) { count += 4; if (k == b) last = count; }
  count += 1;
  for (int s = 0; s < b; ++s) {
    if (s + HD <= 11) { count += 4; if (s + HD == b) last = count; }
    if (s == 9) count += 5;
    if (s >= 2 && s <= 7) { count += 5; if (s + 4 == b + 1) last = count; }
  }
  return count - last;
}
__global__ __launch_bounds__(512, 2) void fused_kernel(Args a) {
  extern __shared__ __attribute__((aligned(16))) char smem[];
  unsigned long long probe_entry = 0;
  if (PROBE_ENTRY) { asm volatile("s_memrealtime %0\n\ts_waitcnt lgkmcnt(0)" : "=s"(probe_entry) :: "memory"); }
  lptr lds = (lptr)smem;
  const int tid = threadIdx.x, lane = tid & 63;
  const int wave = __builtin_amdgcn_readfirstlane(tid >> 6);
  const int bid = blockIdx.x;
  const int xcd = bid & 7, j = bid >> 3;
  const int mi = 8 * (xcd & 1) + (j & 7), fi = 4 * (xcd >> 1) + (j >> 3);
  const int si = 8 * (xcd & 3) + (j & 7), di = 4 * (xcd >> 2) + (j >> 3);
  const int c0 = (5 * bid) >> 1, nconv = 2 + (bid & 1);
  const unsigned lds0 = lds_addr(lds);
  STAMP_INIT();
  STAMP(0);

  if (wave == 7) {
    if (lane < 32) *sy(lds, lane) = 0u;
    WAIT_LGKM0();
  }
  BARRIER();

  if (wave == 7) {
    const unsigned epoch = __hip_atomic_load((GAS unsigned*)(a.ws + WS_EPOCH), __ATOMIC_RELAXED, __HIP_MEMORY_SCOPE_AGENT);
    {
      const int r0 = 128 * mi + 8 * fi, gr = lane >> 4, gq = lane & 15;
      u32x4 gx[2][5], gw[5];
#pragma unroll
      for (int i = 0; i < 5; ++i) {
        gw[i] = ((const u32x4*)a.gw)[gq + 16 * i];
        gx[0][i] = ((const u32x4*)(a.x + (size_t)(r0 + gr) * D))[gq + 16 * i];
        gx[1][i] = ((const u32x4*)(a.x + (size_t)(r0 + 4 + gr) * D))[gq + 16 * i];
      }
      FENCE();
      sy_set(lds, SY_TAG, epoch + 1u, lane);
      sy_set(lds, SY_TAGOK, 1u, lane);
      BARRIER();
#pragma unroll
      for (int pass = 0; pass < 2; ++pass) {
        float sum = 0.f;
#pragma unroll
        for (int i = 0; i < 5; ++i)
#pragma unroll
          for (int e = 0; e < 4; ++e) sum = dot2(gx[pass][i][e], gw[i][e], sum);
#pragma unroll
        for (int m = 8; m >= 1; m >>= 1) sum += __shfl_xor(sum, m);
        if (gq == 0)
          __hip_atomic_store((GAS unsigned*)(a.ws + WS_GATE) + r0 + 4 * pass + gr, __builtin_bit_cast(unsigned, sigmoidf_(sum)),
                             __ATOMIC_RELAXED, __HIP_MEMORY_SCOPE_AGENT);
      }
    }
    const unsigned tag = epoch + 1u;
    STAMP(1);
#pragma unroll 1
    for (int s = 0; s < 9; ++s) {
      BARRIER();
      if (s == 3 && lane == 0) flag_store(a.ws, WS_FLAGKV + 4 * (32 * (bid & 7) + (bid >> 3)), tag);
      if (s == 5) {
        WAIT_VM(0);
        sy_set(lds, SY_GATE, 1u, lane);
      }
      if (s == WD_TOUCH_AT) {
        const char* base = (const char*)a.wd + 640 * (xcd >> 2);
#pragma unroll
        for (int t = 0; t < 4; ++t) {
          const int id = min(64 * t + lane, 239), r = id / 5, c = id - 5 * r;
          touch(base + (size_t)(48 * j + r) * (D * 2) + 128 * c, lds0 + L_DUMMY);
        }
      }
    }
    STAMP(2);
    {
      flag_wait(a.ws, WS_FLAGKV + 4 * (32 * (bid >> 5) + (lane & 31)), true, tag);
      STAMP(8);
      u32x4 uv[3][4];
#pragma unroll
      for (int cw = 0; cw < 3; ++cw) {
        const __amdgpu_buffer_rsrc_t ru = make_rsrc(a.ws + WS_KVR + (size_t)(c0 + (cw < nconv ? cw : 0)) * S * 2, S * 2);
#pragma unroll
        for (int i = 0; i < 4; ++i) uv[cw][i] = ld128(ru, (unsigned)(i * 1024 + lane * 16), 0, SC1);
      }
      FENCE();
#pragma unroll
      for (int cw = 0; cw < 3; ++cw) {
        lptr ur = lds + L_AUX + cw * UR_BYTES;
#pragma unroll
        for (int i = 0; i < 4; ++i) *(LAS u32x4*)(ur + i * 1024 + lane * 16) = uv[cw][i];
        if (lane < 32) *(LAS u32x4*)(ur + 4096 + lane * 16) = (u32x4){0u, 0u, 0u, 0u};
      }
      WAIT_LGKM0();
      sy_set(lds, SY_AREADY, 1u, lane);
    }
    STAMP(9);
    if (bid == 0) {
#pragma unroll
      for (int i = 0; i < 4; ++i) flag_wait(a.ws, WS_FLAGKV + 4 * (lane + 64 * i), true, tag);
      if (lane == 0) flag_store(a.ws, WS_EPOCH, tag);
    }
    sy_wait(lds, SY_WPRE, 4u);
    flag_wait<HPOLL_SLEEP>(a.ws, WS_FLAGH + 4 * (16 * (si >> 1) + (lane & 15)), true, tag);
    sy_set(lds, SY_HREADY, 1u, lane);
    STAMP(10);
    sy_wait(lds, SY_CONV, nconv);
    sy_wait(lds, SY_GSTEP, 1u);
    flag_wait<YPOLL_SLEEP>(a.ws, WS_FLAGY + 4 * (32 * di + (lane & 31)), true, tag);
    STAMP(11);
    {
      const __amdgpu_buffer_rsrc_t ry = make_rsrc(a.ws + WS_Y2T, S * D * 2);
      u32x4 yv[10];
#pragma unroll
      for (int i = 0; i < 10; ++i) {
        const int id = lane + 64 * i;
        yv[i] = ld128(ry, (unsigned)((80 * di + (id >> 3)) * (S * 2) + 64 * si * 2 + (id & 7) * 16), 0, SC1);
      }
#pragma unroll
      for (int i = 0; i < 10; ++i) {
        const int id = lane + 64 * i, w = (id & 7) >> 1;
        const int so = w == 0 ? ystrip_off(0) : w == 1 ? ystrip_off(1) : w == 2 ? ystrip_off(2) : ystrip_off(3);
        *(LAS u32x4*)(lds + so + (id >> 3) * 32 + (id & 1) * 16) = yv[i];
      }
      WAIT_LGKM0();
      sy_set(lds, SY_YREADY, 1u, lane);
    }
    STAMP(12);
    WAIT_VM(0);
    STAMP_DUMP();
    return;
  }

  if (wave >= 4) {
    const int cw = wave - 4;
    const bool active = cw < nconv;
    const int ch = c0 + (active ? cw : 0);
    Filter flt;
    const int rsum = (j >> 3) + (j & 7);
    const int rot1 = ROT1 == 1 ? (3 * (j >> 3) + (j & 7)) % 10 : ROT1 == 2 ? 5 * (rsum & 1) : ROT1 == 3 ? 3 * (rsum % 3) : ROT1 == 4 ? (5 * (rsum & 3)) >> 1 : 0;
    if (cw == 0) loader<6>(a, lds, 0, 0, mi, fi, rot1, flt, ch, lane);
    else loader<5>(a, lds, cw, 1 + 5 * cw, mi, fi, rot1, flt, ch, lane);
    STAMP(3);
    if (active) {
      conv_wave(a, lds, lds + (cw == 2 ? 2 * STAGE : STAGE + cw * KC_BYTES), lds + L_AUX + cw * UR_BYTES, flt, ch, nconv, lane);
    }
    STAMP_DUMP();
    return;
  }

  unsigned long long probe_t0 = PROBE_ENTRY ? probe_entry : PROBE_AT ? __builtin_amdgcn_s_memrealtime() : 0ull;
  if (PROBE_GLOBAL && tid == 0)
    __hip_atomic_store((GAS unsigned long long*)(a.ws + (32u << 20)) + bid, probe_t0, __ATOMIC_RELAXED, __HIP_MEMORY_SCOPE_AGENT);
  (void)probe_t0;
  f32x4 ag[4][3], au[4][3];
  {
    const int tsi = bid >> 3, tdi = bid & 7;
    const int task = 40 * wave + (lane < 40 ? lane : 0);
    KvTask t0;
    kv_load(a, t0, task, tsi, tdi);
    FENCE();
#pragma unroll
    for (int t = 0; t < 4; ++t)
#pragma unroll
      for (int c = 0; c < 3; ++c) {
        ag[t][c] = (f32x4){0.f, 0.f, 0.f, 0.f}; au[t][c] = (f32x4){0.f, 0.f, 0.f, 0.f};
        asm volatile("" : "+v"(ag[t][c]), "+v"(au[t][c]));
      }
    FENCE();
    STAMP(1);
    if (lane < 40) kv_compute(t0, lds, task, tsi);
    sy_arrive(lds, SY_KV, lane);
    sy_wait(lds, SY_KV, 4u);
    STAMP(2);
    const __amdgpu_buffer_rsrc_t rkv = make_rsrc(a.ws + WS_KVR, S * D * 2);
#pragma unroll
    for (int i = 0; i < 3; ++i) {
      const int id = tid + 256 * i;
      if (id < 640) {
        const int dl = id >> 3, jj = id & 7;
        const u32x4 val = *(LAS u32x4*)(lds + L_T + dl * 128 + ((jj ^ ((dl >> 3) & 7)) << 4));
        __builtin_amdgcn_raw_buffer_store_b128(val, rkv, (unsigned)(((80 * tdi + dl) * S + (1984 - 64 * tsi) + 8 * jj) * 2), 0, SC1);
      }
    }
  }
  const int n = lane & 15, g = lane >> 4;
  {
    const int ws_ = wave & 1, wf = wave >> 1;
    const int q = n >> 2, p = lane & 3;
    const int xb0 = XB + (64 * ws_ + n) * 128 + ((g ^ (n >> 1)) << 4);
    int wb_[3];
#pragma unroll
    for (int c = 0; c < 3; ++c) wb_[c] = GB + (8 * g + q) * 192 + 8 * p + 32 * ((3 * wf + c) ^ (g & 1));
    lptr xa[3][2], wa[3][3];
#pragma unroll
    for (int b = 0; b < 3; ++b) {
#pragma unroll
      for (int kk = 0; kk < 2; ++kk) { xa[b][kk] = lds + b * STAGE + (xb0 ^ (kk << 6)); asm volatile("" : "+v"(xa[b][kk])); }
#pragma unroll
      for (int c = 0; c < 3; ++c) { wa[b][c] = lds + b * STAGE + wb_[c]; asm volatile("" : "+v"(wa[b][c])); }
    }
    auto read_piece = [&](Frags& f, int i, int b, int kk) {
      if (i < 4) {
        f.x[i] = lds_frag(xa[b][kk] + i * 2048);
      } else {
        const int c = (i - 4) >> 2, u = ((i - 4) >> 1) & 1, t = (i - 4) & 1;
        const s16x4 v = lds_tr(wa[b][c] + kk * (32 * 192) + u * (UB - GB) + t * (4 * 192));
        if (u == 0) f.g[c][t] = v; else f.u[c][t] = v;
      }
    };
    auto mma_piece = [&](const Frags& f, int i) {
      const int c = i >> 3, t = (i >> 1) & 3;
      if ((i & 1) == 0) ag[t][c] = MFMA16(join(f.g[c][0], f.g[c][1]), f.x[t], ag[t][c]);
      else au[t][c] = MFMA16(join(f.u[c][0], f.u[c][1]), f.x[t], au[t][c]);
    };
    auto half = [&](const Frags& cur, Frags& nxt, int nb, int nkk) {
#pragma unroll
      for (int i = 0; i < 24; ++i) {
        mma_piece(cur, i); FENCE();
        if (i < 16) { read_piece(nxt, i, nb, nkk); FENCE(); }
      }
    };
    BARRIER();
    PROBE(1);
    STAMP(3);
    STAMPC(28);
    __builtin_amdgcn_s_setprio(M_PRIO);
    Frags f0, f1;
#pragma unroll
    for (int i = 0; i < 16; ++i) read_piece(f0, i, 0, 0);
    FENCE();
    STAMPC(27);
    auto iteration = [&](int b, bool drain) {
      FENCE();
      half(f0, f1, b, 1);
      if (drain) WAIT_VM(0);
      WAIT_LGKM0();
      BARRIER();
      FENCE();
      half(f1, f0, (b + 1) % 3, 0);
    };
#pragma unroll 1
    for (int s3 = 0; s3 < 3; ++s3) {
      iteration(0, s3 == 1);
      iteration(1, false);
      iteration(2, false);
    }
    half(f0, f1, 0, 1);
    const __amdgpu_buffer_rsrc_t rhs = make_rsrc(a.ws + WS_HT, S * F * 2);
    const lptr exch = lds + L_FREE + ws_ * 2048 + lane * 8;
    u32x2 keep[4];
    auto epilogue = [&](int t) {
      u32x2 pk[3];
#pragma unroll
      for (int c = 0; c < 3; ++c) {
        const f32x2 s01 = (f32x2){ag[t][c][0], ag[t][c][1]} * -1.4426950408889634f, s23 = (f32x2){ag[t][c][2], ag[t][c][3]} * -1.4426950408889634f;
        const f32x2 d01 = (f32x2){__builtin_amdgcn_exp2f(s01[0]), __builtin_amdgcn_exp2f(s01[1])} + 1.0f;
        const f32x2 d23 = (f32x2){__builtin_amdgcn_exp2f(s23[0]), __builtin_amdgcn_exp2f(s23[1])} + 1.0f;
        const f32x2 h01 = (f32x2){__builtin_amdgcn_rcpf(d01[0]), __builtin_amdgcn_rcpf(d01[1])} * (f32x2){au[t][c][0], au[t][c][1]};
        const f32x2 h23 = (f32x2){__builtin_amdgcn_rcpf(d23[0]), __builtin_amdgcn_rcpf(d23[1])} * (f32x2){au[t][c][2], au[t][c][3]};
        pk[c] = (u32x2){pack_bf16(h01[0], h01[1]), pack_bf16(h23[0], h23[1])};
      }
      const unsigned base = (unsigned)(((8 * mi + 4 * ws_ + t) * 48 + 3 * fi) * 1024 + lane * 16);
      if (wf == 0) {
        __builtin_amdgcn_raw_buffer_store_b128((u32x4){pk[0][0], pk[0][1], pk[1][0], pk[1][1]}, rhs, base, 0, SC1);
        keep[t] = pk[2];
      } else {
        *(LAS u32x2*)(exch + t * 512) = pk[0];
        __builtin_amdgcn_raw_buffer_store_b128((u32x4){pk[1][0], pk[1][1], pk[2][0], pk[2][1]}, rhs, base + 2048, 0, SC1);
      }
    };
#pragma unroll
    for (int t = 0; t < 4; ++t) {
      FENCE();
#pragma unroll
      for (int c = 0; c < 3; ++c) {
        ag[t][c] = MFMA16(join(f1.g[c][0], f1.g[c][1]), f1.x[t], ag[t][c]);
        au[t][c] = MFMA16(join(f1.u[c][0], f1.u[c][1]), f1.x[t], au[t][c]);
      }
      FENCE();
      if (t == 0) { PROBE(2); STAMP(4); STAMPC(29); }
      if (t > 0) epilogue(t - 1);
    }
    FENCE();
    epilogue(3);
    if (wf == 0) {
      sy_wait(lds, SY_EX + ws_, 1u);
#pragma unroll
      for (int t = 0; t < 4; ++t) {
        const u32x2 o = *(LAS u32x2*)(exch + t * 512);
        __builtin_amdgcn_raw_buffer_store_b128((u32x4){keep[t][0], keep[t][1], o[0], o[1]}, rhs,
                                               (unsigned)(((8 * mi + 4 * ws_ + t) * 48 + 3 * fi + 1) * 1024 + lane * 16), 0, SC1);
      }
    } else {
      WAIT_LGKM0();
      sy_set(lds, SY_EX + ws_, 1u, lane);
    }
  }
  STAMP(5);

  const __amdgpu_buffer_rsrc_t rh = make_rsrc(a.ws + WS_HT, S * F * 2);
  const __amdgpu_buffer_rsrc_t rw = make_rsrc(a.wd, F * D * 2);
  const unsigned wr0 = wave == 0 ? wring_off(0) : wave == 1 ? wring_off(1) : wave == 2 ? wring_off(2) : wring_off(3);
  const int rot = 3 * (j >> 3);
  auto kstep = [&](int b) { const int t = b + rot; return 12 * wave + (t >= 12 ? t - 12 : t); };
  unsigned vwd[5];
#pragma unroll
  for (int i = 0; i < 5; ++i) {
    const int slot = 64 * i + lane, row = slot / 10, ch = slot - 10 * row;
    vwd[i] = (unsigned)(row * (D * 2) + 160 * di + ch * 16 - 1024 * (i & 3));
  }
  auto load_w = [&](int b) {
    const unsigned dst = lds0 + wr0 + (b & 3) * WSLOT, so = (unsigned)kstep(b) * (32 * D * 2);
    dma16xN<4>(rw, dst, vwd, so);
    dma16xN<1>(rw, dst + 4096, vwd + 4, so);
  };
  auto load_w1 = [&](int b, int i) {
    dma16(rw, lds0 + wr0 + (b & 3) * WSLOT + i * 1024, vwd[i] + 1024 * (i & 3), (unsigned)kstep(b) * (32 * D * 2));
  };
  struct HS4 { u32x4 f[4]; };
  HS4 H[HD + 1];
  const unsigned voff = lane * 16;
  auto load_h = [&](HS4& h, int b) {
    const unsigned so = (unsigned)(4 * si * 48 + kstep(b)) * 1024;
#define HPOL " sc1"
    asm volatile("buffer_load_dwordx4 %0, %4, %5, %6 offen" HPOL "\n\tbuffer_load_dwordx4 %1, %4, %5, %7 offen" HPOL "\n\t"
                 "buffer_load_dwordx4 %2, %4, %5, %8 offen" HPOL "\n\tbuffer_load_dwordx4 %3, %4, %5, %9 offen" HPOL
                 : "=&v"(h.f[0]), "=&v"(h.f[1]), "=&v"(h.f[2]), "=&v"(h.f[3])
                 : "v"(voff), "s"(rh), "s"(so), "s"(so + 48 * 1024), "s"(so + 96 * 1024), "s"(so + 144 * 1024) : "memory");
  };
  auto load_h1 = [&](HS4& h, int b, int i) {
    const unsigned so = (unsigned)((4 * si + i) * 48 + kstep(b)) * 1024;
    asm volatile("buffer_load_dwordx4 %0, %1, %2, %3 offen" HPOL : "=&v"(h.f[i]) : "v"(voff), "s"(rh), "s"(so) : "memory");
  };
  const int srow = 64 * si + 16 * wave + n;
  u32x2 qv[5];
  FENCE();
  __builtin_amdgcn_s_setprio(3);
  sy_arrive(lds, SY_EPI, lane);
  STAMP(22);
  FENCE();
  WAIT_VM(0);
  if (sy_arrive(lds, SY_HDONE, lane) == 3u) {
    sy_wait(lds, SY_GATE, 1u);
    const unsigned tag = get_tag(lds);
    if (lane == 0) flag_store(a.ws, WS_FLAGH + 4 * (16 * mi + fi), tag);
    WAIT_VM(0);
  }
  sy_wait(lds, SY_EPI, 4u);
  load_w(0); load_w(1); load_w(2); load_w(3);
  STAMP(23);
  f32x4 acc[4][5];
#pragma unroll
  for (int t = 0; t < 4; ++t)
#pragma unroll
    for (int c = 0; c < 5; ++c) { acc[t][c] = (f32x4){0.f, 0.f, 0.f, 0.f}; asm volatile("" : "+v"(acc[t][c])); }
  struct WF { s16x4 f[5][2]; };
  WF w0, w1;
  const lptr wrd = lds + wr0 + (4 * g + (n >> 2)) * 160 + 8 * (lane & 3);
  auto read_w = [&](WF& w, int p, int b) {
    w.f[p >> 1][p & 1] = lds_tr(wrd + (b & 3) * WSLOT + (p & 1) * (16 * 160) + (p >> 1) * 32);
  };
  STAMP(24);
  WAIT_VM(10);
#pragma unroll
  for (int p = 0; p < 10; ++p) read_w(w0, p, 0);
#pragma unroll
  for (int p = 0; p < 10; ++p) read_w(w1, p, 1);
  WAIT_LGKM0();
  load_w(4);
  load_w(5);
  sy_arrive(lds, SY_WPRE, lane);
  __builtin_amdgcn_s_setprio(G_PRIO);
  STAMP(6);
  sy_wait(lds, SY_HREADY, 1u);
  PROBE(3);
  STAMP(7);
#pragma unroll
  for (int b = 0; b < HD; ++b) load_h(H[b], b);
  const float gate = __builtin_bit_cast(float, __hip_atomic_load((GAS unsigned*)(a.ws + WS_GATE) + srow, __ATOMIC_RELAXED, __HIP_MEMORY_SCOPE_AGENT));
  FENCE();
  auto gstep = [&](const WF& cur, WF& nxt, HS4& h, HS4& hn, int b) {
    asm volatile("s_waitcnt vmcnt(%4) lgkmcnt(0)" : "+v"(h.f[0]), "+v"(h.f[1]), "+v"(h.f[2]), "+v"(h.f[3]) : "n"(g_younger(b)) : "memory");
    FENCE();
#pragma unroll
    for (int i = 0; i < 20; ++i) {
      acc[i & 3][i >> 2] = MFMA16(join(cur.f[i >> 2][0], cur.f[i >> 2][1]), __builtin_bit_cast(bf16x8, h.f[i & 3]), acc[i & 3][i >> 2]);
      FENCE();
      if (i < 8 && (i & 1) == 0 && b + HD < 12) { load_h1(hn, b + HD, i >> 1); FENCE(); }
      if (i == 7 && b == 9) {
#pragma unroll
        for (int c = 0; c < 5; ++c) qv[c] = *(const u32x2*)(a.x + (size_t)srow * D + 80 * di + 16 * c + 4 * g);
        asm volatile("" ::: "memory");
        FENCE();
      }
      if (i < 10 && b >= 1 && b + 1 < 12) { read_w(nxt, i, b + 1); FENCE(); }
      if (i >= 10 && (i & 1) == 0 && b >= 2 && b + 4 < 12) { load_w1(b + 4, (i - 10) >> 1); FENCE(); }
      if (b == YPOLL_STEP && i == 17 && wave == 0) { sy_post(lds0 + L_SYNC + 4 * SY_GSTEP, 1u); FENCE(); }
      if (b == YPOLL_STEP && i == 19 && wave == 0) { wake(); FENCE(); }
    }
  };
#pragma unroll
  for (int b = 0; b < 12; b += 2) {
    gstep(w0, w1, H[b % (HD + 1)], H[(b + HD) % (HD + 1)], b);
    if (b == 0) STAMPI(14);
    gstep(w1, w0, H[(b + 1) % (HD + 1)], H[(b + 1 + HD) % (HD + 1)], b + 1);
    STAMPI(15 + (b >> 1));
  }
  PROBE(4);
  STAMP(8);

  f32x4 bsum[5];
  {
#pragma unroll
    for (int t = 0; t < 4; ++t) {
      if (t != wave) {
#pragma unroll
        for (int c = 0; c < 5; ++c) *(LAS f32x4*)(lds + wr0 + (((t - wave - 1) & 3) * 5 + c) * 1024 + lane * 16) = acc[t][c];
      } else {
#pragma unroll
        for (int c = 0; c < 5; ++c) bsum[c] = acc[t][c];
      }
    }
    sy_arrive(lds, SY_RED, lane);
    sy_wait(lds, SY_RED, 4u);
#pragma unroll
    for (int w = 0; w < 4; ++w) {
      if (w != wave) {
#pragma unroll
        for (int c = 0; c < 5; ++c) bsum[c] += *(LAS f32x4*)(lds + wring_off(w) + (((wave - w - 1) & 3) * 5 + c) * 1024 + lane * 16);
      }
    }
  }
  STAMP(9);
  sy_wait(lds, SY_YREADY, 1u);
  STAMP(10);
  {
    const int q = n >> 2, p = lane & 3;
    const __amdgpu_buffer_rsrc_t ro = make_rsrc(a.out, S * D * 2);
    const lptr ystr = lds + (wave == 0 ? ystrip_off(0) : wave == 1 ? ystrip_off(1) : wave == 2 ? ystrip_off(2) : ystrip_off(3));
#pragma unroll
    for (int c = 0; c < 5; ++c) {
      const s16x4 yv = lds_tr(ystr + (16 * c + 4 * g + q) * 32 + 8 * p);
      const u32x2 y = __builtin_bit_cast(u32x2, yv);
      const float o0 = gate * (bf_lo(qv[c][0]) * bf_lo(y[0])) + (1.f - gate) * bsum[c][0];
      const float o1 = gate * (bf_hi(qv[c][0]) * bf_hi(y[0])) + (1.f - gate) * bsum[c][1];
      const float o2 = gate * (bf_lo(qv[c][1]) * bf_lo(y[1])) + (1.f - gate) * bsum[c][2];
      const float o3 = gate * (bf_hi(qv[c][1]) * bf_hi(y[1])) + (1.f - gate) * bsum[c][3];
      __builtin_amdgcn_raw_buffer_store_b64((u32x2){pack_bf16(o0, o1), pack_bf16(o2, o3)}, ro,
                                            (unsigned)((srow * D + 80 * di + 16 * c + 4 * g) * 2), 0, OUT_AUX);
    }
  }
  if (PROBE_GLOBAL) {
    unsigned long long r = PROBE_GLOBAL == 1 ? ~0ull : 0ull;
    for (int i = 0; i < 4; ++i) {
      const unsigned long long v = __hip_atomic_load((GAS unsigned long long*)(a.ws + (32u << 20)) + lane + 64 * i, __ATOMIC_RELAXED, __HIP_MEMORY_SCOPE_AGENT);
      r = PROBE_GLOBAL == 1 ? min(r, v) : max(r, v);
    }
    for (int m = 32; m >= 1; m >>= 1) { const unsigned long long o = __shfl_xor(r, m); r = PROBE_GLOBAL == 1 ? min(r, o) : max(r, o); }
    probe_t0 = __builtin_amdgcn_readfirstlane((unsigned)r) | ((unsigned long long)__builtin_amdgcn_readfirstlane((unsigned)(r >> 32)) << 32);
  }
  PROBE(5);
  STAMP(11);
}

}

extern "C" void kernel_launch(void* const* d_in, const int* in_sizes, int n_in, void* d_out, int out_size, void* d_ws,
                              size_t ws_size, hipStream_t stream) {
  (void)in_sizes; (void)n_in; (void)out_size; (void)ws_size;
  Args a;
  a.x = (const u16*)d_in[0];
  a.k = (const u16*)d_in[1];
  a.v = (const u16*)d_in[2];
  a.shk = (const u16*)d_in[3];
  a.ssmk = (const u16*)d_in[4];
  a.ssmd = (const u16*)d_in[5];
  a.wg = (const u16*)d_in[7];
  a.wu = (const u16*)d_in[8];
  a.wd = (const u16*)d_in[9];
  a.gw = (const u16*)d_in[10];
  a.ws = (char*)d_ws;
  a.out = (u16*)d_out;
  (void)hipFuncSetAttribute((const void*)fused_kernel, hipFuncAttributeMaxDynamicSharedMemorySize, LDS_BYTES);
  fused_kernel<<<256, 512, LDS_BYTES, stream>>>(a);
}
```

```cpp
#include <hip/hip_runtime.h>
#include <hip/hip_bf16.h>

namespace {

typedef unsigned short u16;
typedef __bf16 bf16x8 __attribute__((ext_vector_type(8)));
typedef __bf16 bf16x2 __attribute__((ext_vector_type(2)));
typedef short s16x4 __attribute__((ext_vector_type(4)));
typedef float f32x2 __attribute__((ext_vector_type(2)));
typedef float f32x4 __attribute__((ext_vector_type(4)));
typedef unsigned u32x2 __attribute__((ext_vector_type(2)));
typedef unsigned u32x4 __attribute__((ext_vector_type(4)));
#define LAS __attribute__((address_space(3)))
#define GAS __attribute__((address_space(1)))
typedef LAS char* lptr;

constexpr int S = 2048, D = 640, F = 1536;
constexpr int SC1 = 16;

constexpr size_t WS_HT = 0;
constexpr size_t WS_KVR = WS_HT + (size_t)S * F * 2;
constexpr size_t WS_Y2T = WS_KVR + (size_t)S * D * 2;
constexpr size_t WS_GATE = WS_Y2T + (size_t)S * D * 2;
constexpr size_t WS_FLAGKV = WS_GATE + (size_t)S * 4;
constexpr size_t WS_FLAGH = WS_FLAGKV + 1024;
constexpr size_t WS_FLAGY = WS_FLAGH + 1024;
constexpr size_t WS_EPOCH = WS_FLAGY + 1024;
constexpr size_t WS_END = WS_EPOCH + 256;

constexpr int STAGE = 40960, XB = 0, GB = 16384, UB = 28672;
constexpr int L_AUX = 3 * STAGE;
constexpr int L_T = L_AUX, UR_BYTES = 4608;
constexpr int L_FREE = L_AUX + 3 * UR_BYTES;
constexpr int L_SYNC = L_FREE + 20480 + 2 * 2560;
constexpr int L_DUMMY = L_SYNC + 128;
constexpr int L_END = L_DUMMY + 256;
constexpr int KC_COPY = 4160, KC_BYTES = 4 * KC_COPY;
constexpr int WSLOT = 5120;
__device__ constexpr int wring_off(int w) { return w == 0 ? 0 : w == 1 ? 20480 : w == 2 ? 2 * STAGE + KC_BYTES : L_FREE; }
__device__ constexpr int ystrip_off(int w) { return w < 2 ? STAGE + 2 * KC_BYTES + w * 2560 : L_FREE + 20480 + (w - 2) * 2560; }
static_assert(2 * STAGE + KC_BYTES + 20480 <= 3 * STAGE && STAGE + 2 * KC_BYTES + 2 * 2560 <= 2 * STAGE, "second-phase LDS map");
enum { SY_LANDED = 0, SY_CONSUMED = 4, SY_KV = 8, SY_SIDE = 9, SY_HDONE = 10, SY_RED = 11, SY_CONV = 12, SY_AREADY = 13,
       SY_HREADY = 14, SY_YREADY = 15, SY_TAG = 16, SY_TAGOK = 17, SY_GSTEP = 18, SY_GATE = 19, SY_EPI = 20, SY_WPRE = 21, SY_EX = 22 };

#define STAMP(i) do {} while (0)
#define STAMPC(i) do {} while (0)
#define STAMPI(i) do {} while (0)
#define STAMP_INIT() do {} while (0)
#define STAMP_DUMP() do {} while (0)
constexpr int LDS_BYTES = L_END;

#define PROBE_AT 0
#define PROBE_US 0
#define PROBE(k) do { if (PROBE_AT == (k)) { while (__builtin_amdgcn_s_memrealtime() - probe_t0 < 100ull * PROBE_US) __builtin_amdgcn_s_sleep(1); } } while (0)
#define PROBE_ENTRY 0
#define PROBE_GLOBAL 0
#define MFMA16(a, b, c) __builtin_amdgcn_mfma_f32_16x16x32_bf16((a), (b), (c), 0, 0, 0)
#define WAIT_VM(n) asm volatile("s_waitcnt vmcnt(" #n ")" ::: "memory")
#define WAIT_LGKM0() asm volatile("s_waitcnt lgkmcnt(0)" ::: "memory")
#define BARRIER() do { asm volatile("" ::: "memory"); __builtin_amdgcn_s_barrier(); asm volatile("" ::: "memory"); } while (0)
#define FENCE() __builtin_amdgcn_sched_barrier(0)
constexpr unsigned SPIN_LDS = 1u << 22, SPIN_MEM = 1u << 16;

__device__ __forceinline__ unsigned pack_bf16(float lo, float hi) {
  f32x2 v = {lo, hi};
  return __builtin_bit_cast(unsigned, __builtin_convertvector(v, bf16x2));
}
__device__ __forceinline__ float bf_lo(unsigned w) { return __builtin_bit_cast(float, w << 16); }
__device__ __forceinline__ float bf_hi(unsigned w) { return __builtin_bit_cast(float, w & 0xffff0000u); }
__device__ __forceinline__ float sigmoidf_(float x) {
  return __builtin_amdgcn_rcpf(1.0f + __builtin_amdgcn_exp2f(x * -1.4426950408889634f));
}
__device__ __forceinline__ __amdgpu_buffer_rsrc_t make_rsrc(const void* p, unsigned bytes) {
  return __builtin_amdgcn_make_buffer_rsrc(const_cast<void*>(p), 0, bytes, 0x00020000);
}
__device__ __forceinline__ unsigned lds_addr(lptr p) { return (unsigned)(size_t)p; }
__device__ __forceinline__ void dma16(__amdgpu_buffer_rsrc_t r, unsigned dst, unsigned voff, unsigned soff) {
  asm volatile("s_mov_b32 m0, %0\n\ts_nop 0\n\tbuffer_load_dwordx4 %1, %2, %3 offen lds"
               :: "s"(dst), "v"(voff), "s"(r), "s"(soff) : "memory");
}
template <int N>
__device__ __forceinline__ void dma16xN(__amdgpu_buffer_rsrc_t r, unsigned dst, const unsigned* v, unsigned soff) {
  static_assert(N >= 1 && N <= 4, "12-bit instruction offset");
  if constexpr (N == 1)
    asm volatile("s_mov_b32 m0, %0\n\ts_nop 0\n\tbuffer_load_dwordx4 %1, %2, %3 offen lds"
                 :: "s"(dst), "v"(v[0]), "s"(r), "s"(soff) : "memory");
  else if constexpr (N == 2)
    asm volatile("s_mov_b32 m0, %0\n\ts_nop 0\n\tbuffer_load_dwordx4 %1, %3, %4 offen lds\n\t"
                 "buffer_load_dwordx4 %2, %3, %4 offen offset:1024 lds"
                 :: "s"(dst), "v"(v[0]), "v"(v[1]), "s"(r), "s"(soff) : "memory");
  else if constexpr (N == 3)
    asm volatile("s_mov_b32 m0, %0\n\ts_nop 0\n\tbuffer_load_dwordx4 %1, %4, %5 offen lds\n\t"
                 "buffer_load_dwordx4 %2, %4, %5 offen offset:1024 lds\n\tbuffer_load_dwordx4 %3, %4, %5 offen offset:2048 lds"
                 :: "s"(dst), "v"(v[0]), "v"(v[1]), "v"(v[2]), "s"(r), "s"(soff) : "memory");
  else
    asm volatile("s_mov_b32 m0, %0\n\ts_nop 0\n\tbuffer_load_dwordx4 %1, %5, %6 offen lds\n\t"
                 "buffer_load_dwordx4 %2, %5, %6 offen offset:1024 lds\n\tbuffer_load_dwordx4 %3, %5, %6 offen offset:2048 lds\n\t"
                 "buffer_load_dwordx4 %4, %5, %6 offen offset:3072 lds"
                 :: "s"(dst), "v"(v[0]), "v"(v[1]), "v"(v[2]), "v"(v[3]), "s"(r), "s"(soff) : "memory");
}
__device__ __forceinline__ void touch(const void* p, unsigned dummy_lds) {
  asm volatile("s_mov_b32 m0, %0\n\ts_nop 0\n\tglobal_load_lds_dword %1, off" :: "s"(dummy_lds), "v"(p) : "memory");
}
__device__ __forceinline__ s16x4 lds_tr(lptr p) { return __builtin_amdgcn_ds_read_tr16_b64_v4i16((LAS s16x4*)p); }
__device__ __forceinline__ bf16x8 join(s16x4 a, s16x4 b) {
  return __builtin_bit_cast(bf16x8, __builtin_shufflevector(a, b, 0, 1, 2, 3, 4, 5, 6, 7));
}
__device__ __forceinline__ bf16x8 lds_frag(lptr p) { return *(LAS bf16x8*)p; }
__device__ __forceinline__ u32x4 ld128(__amdgpu_buffer_rsrc_t r, unsigned voff, unsigned soff, int aux) {
  return aux ? __builtin_bit_cast(u32x4, __builtin_amdgcn_raw_buffer_load_b128(r, voff, soff, SC1))
             : __builtin_bit_cast(u32x4, __builtin_amdgcn_raw_buffer_load_b128(r, voff, soff, 0));
}

__device__ __forceinline__ LAS unsigned* sy(lptr lds, int i) { return (LAS unsigned*)(lds + L_SYNC) + i; }
__device__ __forceinline__ unsigned sy_get(lptr lds, int i) {
  return __builtin_amdgcn_readfirstlane(__hip_atomic_load(sy(lds, i), __ATOMIC_RELAXED, __HIP_MEMORY_SCOPE_WORKGROUP));
}
__device__ __forceinline__ void wake() { asm volatile("s_wakeup" ::: "memory"); }
__device__ __forceinline__ void sy_set(lptr lds, int i, unsigned v, int lane) {
  if (lane == 0) __hip_atomic_store(sy(lds, i), v, __ATOMIC_RELAXED, __HIP_MEMORY_SCOPE_WORKGROUP);
  asm volatile("s_waitcnt lgkmcnt(0)\n\ts_wakeup" ::: "memory");
}
#define TOUCH_H 0
#define LOADER_POST_FIRST 1
#define HPOLL_SLEEP 4
#define YPOLL_SLEEP 8
#define YPOLL_STEP 8
#ifndef M_PRIO
#define M_PRIO 2
#endif
#define G_PRIO 0
#define ROT1 4
#define DMA_SPACE 0
#define C_PRIO 2
#define G_HDEPTH 4
#define OUT_AUX 0
#define H_ACQUIRE 0
#define CODE_LINES 0
#define CODE_WAVE 7
#define PUB_FIRST 1
#define WD_TOUCH_AT 7
#define PROLOG 0
#define LAND_RD 18
#define TOUCH_DELAY 8
#define TOUCH1 0
#define FILT_ITER 6
#define W7_DELAY 0
#define POLL_AFTER 8
#define LOADER_PRIO 0
#define SLEEP_LDS 4
__device__ __forceinline__ void sy_post(unsigned addr, unsigned v) {
  asm volatile("s_mov_b64 exec, 1\n\tds_write_b32 %0, %1\n\ts_mov_b64 exec, -1" :: "v"(addr), "v"(v) : "memory");
}
__device__ __forceinline__ void sy_wait(lptr lds, int i, unsigned v) {
#pragma unroll 1
  for (unsigned n = 0; n < SPIN_LDS; ++n) {
    if (sy_get(lds, i) >= v) break;
    __builtin_amdgcn_s_sleep(SLEEP_LDS);
  }
  asm volatile("" ::: "memory");
}
__device__ __forceinline__ unsigned sy_arrive(lptr lds, int i, int lane) {
  WAIT_LGKM0();
  unsigned old = 0;
  if (lane == 0) old = __hip_atomic_fetch_add(sy(lds, i), 1u, __ATOMIC_RELAXED, __HIP_MEMORY_SCOPE_WORKGROUP);
  old = __builtin_amdgcn_readfirstlane(old);
  wake();
  return old;
}
__device__ __forceinline__ unsigned min4(u32x4 v) { return min(min(v[0], v[1]), min(v[2], v[3])); }
__device__ __forceinline__ u32x4 sy_get4(lptr lds, int i) { return *(volatile LAS u32x4*)(lds + L_SYNC + 4 * i); }
__device__ __forceinline__ u32x2 sy_get2(lptr lds, int i) { return *(volatile LAS u32x2*)(lds + L_SYNC + 4 * i); }
__device__ __forceinline__ void sy_wait4(lptr lds, int i, unsigned v) {
#pragma unroll 1
  for (unsigned n = 0; n < SPIN_LDS; ++n) {
    if ((unsigned)__builtin_amdgcn_readfirstlane(min4(sy_get4(lds, i))) >= v) break;
    __builtin_amdgcn_s_sleep(SLEEP_LDS);
  }
  asm volatile("" ::: "memory");
}
#define LOADER_SLEEP 1
__device__ __forceinline__ void loader_wait(lptr lds, unsigned v) {
#pragma unroll 1
  for (unsigned n = 0; n < SPIN_LDS; ++n) {
    if ((unsigned)__builtin_amdgcn_readfirstlane(min4(sy_get4(lds, SY_CONSUMED))) >= v) break;
    if (LOADER_SLEEP) __builtin_amdgcn_s_sleep(LOADER_SLEEP);
  }
  asm volatile("" ::: "memory");
}
__device__ __forceinline__ void sy_wait4m(lptr lds, unsigned v) {
#pragma unroll 1
  for (unsigned n = 0; n < SPIN_LDS; ++n) {
    const u32x4 l = sy_get4(lds, SY_LANDED);
    if ((unsigned)__builtin_amdgcn_readfirstlane(min(min(l[0], l[1]), l[2])) >= v) break;
    __builtin_amdgcn_s_sleep(SLEEP_LDS);
  }
  asm volatile("" ::: "memory");
}
__device__ __forceinline__ unsigned get_tag(lptr lds) {
  sy_wait(lds, SY_TAGOK, 1u);
  return sy_get(lds, SY_TAG);
}
__device__ __forceinline__ void flag_store(char* ws, size_t off, unsigned tag) {
  __hip_atomic_store((GAS unsigned*)(ws + off), tag, __ATOMIC_RELAXED, __HIP_MEMORY_SCOPE_AGENT);
}
#define SIDE_PUB_ITER 3
#define POLL_SLEEP 32
template <int SLEEP = POLL_SLEEP>
__device__ __forceinline__ void flag_wait(char* ws, size_t off, bool on, unsigned tag) {
#pragma unroll 1
  for (unsigned n = 0; n < SPIN_MEM; ++n) {
    const unsigned v = __hip_atomic_load((GAS unsigned*)(ws + off), __ATOMIC_RELAXED, __HIP_MEMORY_SCOPE_AGENT);
    if (__all(!on || v == tag)) break;
    __builtin_amdgcn_s_sleep(SLEEP);
  }
  asm volatile("" ::: "memory");
}

struct Args {
  const u16* x; const u16* k; const u16* v; const u16* shk; const u16* ssmk; const u16* ssmd;
  const u16* wg; const u16* wu; const u16* wd; const u16* gw;
  char* ws; u16* out;
};

struct Frags { bf16x8 x[4]; s16x4 g[3][2]; s16x4 u[3][2]; };

__device__ constexpr int tri_start(int t) { return 8 * t - (t * (t - 1)) / 2; }
__device__ constexpr int tri_row(int p) {
  return p < 8 ? 0 : p < 15 ? 1 : p < 21 ? 2 : p < 26 ? 3 : p < 30 ? 4 : p < 33 ? 5 : p < 35 ? 6 : 7;
}

template <int N> __device__ __forceinline__ void wait_vm() { asm volatile("s_waitcnt vmcnt(%0)" ::"n"(N) : "memory"); }

struct Filter { unsigned d[5][6]; unsigned dbits; };
__device__ __forceinline__ void load_filter(const Args& a, Filter& f, int ch, int lane) {
  const __amdgpu_buffer_rsrc_t rk = make_rsrc(a.ssmk + (size_t)ch * S, S * 2);
#pragma unroll
  for (int it = 0; it < 5; ++it) {
    const unsigned off = (unsigned)(16 * (lane + 64 * it) - 32);
    const u32x4 v0 = __builtin_bit_cast(u32x4, __builtin_amdgcn_raw_buffer_load_b128(rk, off, 0, 0));
    const u32x2 v1 = __builtin_bit_cast(u32x2, __builtin_amdgcn_raw_buffer_load_b64(rk, off + 16, 0, 0));
    f.d[it][0] = v0[0]; f.d[it][1] = v0[1]; f.d[it][2] = v0[2]; f.d[it][3] = v0[3]; f.d[it][4] = v1[0]; f.d[it][5] = v1[1];
  }
  f.dbits = a.ssmd[ch];
}

template <int NX>
__device__ __forceinline__ void loader(const Args& a, lptr lds, int lw, int x0, int mi, int fi, int rot, Filter& flt, int ch, int lane) {
  const unsigned lds0 = lds_addr(lds);
  const __amdgpu_buffer_rsrc_t rx = make_rsrc(a.x, S * D * 2);
  const __amdgpu_buffer_rsrc_t rg = make_rsrc(a.wg, D * F * 2);
  const __amdgpu_buffer_rsrc_t ru = make_rsrc(a.wu, D * F * 2);
  unsigned vx[NX], vw[4];
#pragma unroll
  for (int i = 0; i < NX; ++i) {
    const int row = 8 * (x0 + i) + (lane >> 3), c = (lane & 7) ^ ((row >> 1) & 7);
    vx[i] = (unsigned)((128 * mi + row) * (D * 2) + c * 16 - 1024 * (i & 3));
  }
#pragma unroll
  for (int i = 0; i < 4; ++i) {
    const int slot = 64 * (4 * lw + i) + lane, k = slot / 12, chp = slot - 12 * k, c = chp ^ (2 * ((k >> 3) & 1));
    vw[i] = (unsigned)(k * (F * 2) + fi * 192 + c * 16 - 1024 * i);
  }
  const unsigned land_addr = lds0 + L_SYNC + 4 * (SY_LANDED + lw);
  auto slice = [&](int s) { const int k = s + rot; return k >= 10 ? k - 10 : k; };
  auto part_a = [&](int s) {
    const unsigned b = lds0 + (s % 3) * STAGE, so = (unsigned)slice(s) * (64 * F * 2);
    dma16xN<4>(rg, b + GB + 4 * lw * 1024, vw, so);
    if (DMA_SPACE) __builtin_amdgcn_s_sleep(DMA_SPACE);
    dma16xN<4>(ru, b + UB + 4 * lw * 1024, vw, so);
    if (DMA_SPACE) __builtin_amdgcn_s_sleep(DMA_SPACE);
  };
  auto part_b = [&](int s) {
    const unsigned b = lds0 + (s % 3) * STAGE, so = (unsigned)slice(s) * 128;
    dma16xN<4>(rx, b + XB + x0 * 1024, vx, so);
    if (DMA_SPACE) __builtin_amdgcn_s_sleep(DMA_SPACE);
    dma16xN<NX - 4>(rx, b + XB + (x0 + 4) * 1024, vx + 4, so);
  };
  constexpr int P = NX + 8;
  part_a(0); part_b(0);
  if (PROLOG == 0) { part_a(1); part_b(1); }
  STAMP(1);
  if (PROLOG == 0) wait_vm<P>(); else wait_vm<0>();
  STAMP(2);
  if (PROLOG == 2) { part_a(1); part_b(1); }
  BARRIER();
  if (PROLOG == 1) { part_a(1); part_b(1); }
  part_a(2); part_b(2);
#pragma unroll
  for (int s = 0; s < 9; ++s) {
    if (s == 8) wait_vm<11>(); else wait_vm<P>();
    STAMPI(8 + 2 * s);
    BARRIER();
    STAMPI(9 + 2 * s);
    if (s < 7) { part_a(s + 3); part_b(s + 3); }
    if (s == 7) { FENCE(); load_filter(a, flt, ch, lane); FENCE(); asm volatile("" ::: "memory"); }
  }
}

struct KvTask { u32x4 kr[7], vr[4], wk[4]; };
__device__ __forceinline__ void kv_load(const Args& a, KvTask& t, int task, int tsi, int tdi) {
  const int dc = task % 10, sb = task / 10;
  const int s0 = 64 * tsi + 4 * sb, d0 = 80 * tdi + 8 * dc;
#pragma unroll
  for (int r = 0; r < 7; ++r) {
    const int s = s0 - 3 + r;
    t.kr[r] = *(const u32x4*)(a.k + (size_t)(s < 0 ? 0 : s) * D + d0);
  }
#pragma unroll
  for (int r = 0; r < 4; ++r) t.vr[r] = *(const u32x4*)(a.v + (size_t)(s0 + r) * D + d0);
#pragma unroll
  for (int i = 0; i < 4; ++i) t.wk[i] = *(const u32x4*)(a.shk + (size_t)d0 * 4 + i * 8);
}
__device__ __forceinline__ float dot2(unsigned x, unsigned y, float c) {
  return __builtin_amdgcn_fdot2_f32_bf16(__builtin_bit_cast(bf16x2, x), __builtin_bit_cast(bf16x2, y), c, false);
}
__device__ __forceinline__ float dot2z(unsigned x, unsigned y) {
  float r;
  asm("v_dot2_f32_bf16 %0, %1, %2, 0" : "=v"(r) : "v"(x), "v"(y));
  return r;
}
__device__ __forceinline__ void kv_compute(KvTask& t, lptr lds, int task, int tsi) {
  const int dc = task % 10, sb = task / 10;
  if (tsi == 0 && sb == 0) {
#pragma unroll
    for (int r = 0; r < 3; ++r) t.kr[r] = (u32x4){0u, 0u, 0u, 0u};
  }
  const int sg = 15 - sb;
  lptr dst = lds + L_T + 8 * dc * 128 + (((sg >> 1) ^ (dc & 7)) << 4) + ((sg & 1) << 3);
#pragma unroll
  for (int c = 0; c < 8; ++c) {
    const unsigned w01 = t.wk[c >> 1][2 * (c & 1)], w23 = t.wk[c >> 1][2 * (c & 1) + 1];
    const unsigned sel = (c & 1) ? 0x03020706u : 0x01000504u;
    unsigned pr[6];
#pragma unroll
    for (int i = 0; i < 6; ++i) pr[i] = __builtin_amdgcn_perm(t.kr[i + 1][c >> 1], t.kr[i][c >> 1], sel);
    float o[4];
#pragma unroll
    for (int r = 0; r < 4; ++r) {
      const float ks = dot2(pr[r + 2], w01, dot2z(pr[r], w23));
      o[r] = ks * ((c & 1) ? bf_hi(t.vr[r][c >> 1]) : bf_lo(t.vr[r][c >> 1]));
    }
    *(LAS u32x2*)(dst + c * 128) = (u32x2){pack_bf16(o[3], o[2]), pack_bf16(o[1], o[0])};
  }
}

__device__ __forceinline__ void conv_wave(const Args& a, lptr lds, lptr kc, lptr ur, const Filter& flt, int ch, int nconv, int lane) {
#pragma unroll
  for (int it = 0; it < 5; ++it) {
    const int c = lane + 64 * it;
    if (c < 260) {
      const unsigned a0 = flt.d[it][0], a1 = flt.d[it][1], a2 = flt.d[it][2], a3 = flt.d[it][3], a4 = flt.d[it][4], a5 = flt.d[it][5];
      const unsigned e0 = __builtin_amdgcn_alignbit(a1, a0, 16), e1 = __builtin_amdgcn_alignbit(a2, a1, 16),
                     e2 = __builtin_amdgcn_alignbit(a3, a2, 16), e3 = __builtin_amdgcn_alignbit(a4, a3, 16),
                     e4 = __builtin_amdgcn_alignbit(a5, a4, 16);
      lptr w = kc + 16 * c;
      *(LAS u32x4*)(w) = (u32x4){a0, a1, a2, a3};
      *(LAS u32x4*)(w + KC_COPY) = (u32x4){e0, e1, e2, e3};
      *(LAS u32x4*)(w + 2 * KC_COPY) = (u32x4){a1, a2, a3, a4};
      *(LAS u32x4*)(w + 3 * KC_COPY) = (u32x4){e1, e2, e3, e4};
    }
  }
  const float dcoef = bf_lo(flt.dbits);
  WAIT_LGKM0();
  sy_wait(lds, SY_AREADY, 1u);
  sy_wait(lds, SY_EPI, 4u);
  __builtin_amdgcn_s_setprio(C_PRIO);
  STAMP(4);
  STAMPC(28);

  const int m = lane & 15, g = lane >> 4;
  const int rho = (m + 1) & 3;
  lptr abase = kc + rho * KC_COPY + 2 * (8 * g + m - 31 - rho);
  lptr bbase = ur + 2 * (2000 - 16 * m + 8 * g) - 7 * 512;
  f32x4 acc[8];
#pragma unroll
  for (int t = 0; t < 8; ++t) acc[t] = (f32x4){0.f, 0.f, 0.f, 0.f};
  struct CF { s16x4 a[8][2]; bf16x8 b[8]; };
  auto cread = [&](CF& f, int p, int i) {
    if (p < 16) f.a[p >> 1][p & 1] = *(LAS s16x4*)(abase + 64 * i + 512 * (p >> 1) + 8 * (p & 1));
    else f.b[p - 16] = lds_frag(bbase + 64 * i + 512 * (7 - (p - 16)));
  };
  auto cmma = [&](const CF& f, int p) {
    const int t = tri_row(p), u = p - tri_start(t);
    acc[t + u] = MFMA16(join(f.a[t][0], f.a[t][1]), f.b[u], acc[t + u]);
  };
  auto residue = [&](const CF& cur, CF& nxt, int inext) {
#pragma unroll
    for (int p = 0; p < 36; ++p) {
      cmma(cur, p); FENCE();
      if (p < 24) { cread(nxt, p, inext); FENCE(); }
    }
  };
  CF c0, c1;
#pragma unroll
  for (int p = 0; p < 24; ++p) cread(c0, p, 1);
#pragma unroll 1
  for (int i = 1; i <= 8; i += 2) {
    FENCE();
    residue(c0, c1, i + 1);
    residue(c1, c0, (i + 2 <= 8) ? i + 2 : 8);
  }
  STAMP(5);
  STAMPC(29);
  const __amdgpu_buffer_rsrc_t ry = make_rsrc(a.ws + WS_Y2T + (size_t)ch * S * 2, S * 2);
#pragma unroll
  for (int t = 0; t < 8; t += 2) {
    unsigned pk[2][2];
#pragma unroll
    for (int e = 0; e < 2; ++e) {
      const int s = 256 * (t + e) + 16 * m + 4 * g;
      const u32x2 kv = *(LAS u32x2*)(ur + 2 * (2044 - s));
      pk[e][0] = pack_bf16(acc[t + e][0] + bf_hi(kv[1]) * dcoef, acc[t + e][1] + bf_lo(kv[1]) * dcoef);
      pk[e][1] = pack_bf16(acc[t + e][2] + bf_hi(kv[0]) * dcoef, acc[t + e][3] + bf_lo(kv[0]) * dcoef);
    }
    const auto r0 = __builtin_amdgcn_permlane16_swap(pk[0][0], pk[1][0], false, false);
    const auto r1 = __builtin_amdgcn_permlane16_swap(pk[0][1], pk[1][1], false, false);
    const u32x4 o = {r0[0], r1[0], r0[1], r1[1]};
    const unsigned off = 2 * (256 * (t + (g & 1)) + 16 * m + 8 * (g >> 1));
    __builtin_amdgcn_raw_buffer_store_b128(o, ry, off, 0, SC1);
  }
  STAMP(6);
  WAIT_VM(0);
  STAMP(7);
  if (sy_arrive(lds, SY_CONV, lane) == (unsigned)(nconv - 1)) {
    const unsigned tag = get_tag(lds);
    if (lane == 0) flag_store(a.ws, WS_FLAGY + 4 * blockIdx.x, tag);
  }
}

constexpr int HD = G_HDEPTH;
__device__ constexpr int g_younger(int b) {
  int count = 0, last = 0;
  for (int k = 0; k < HD; ++k) { count += 4; if (k == b) last = count; }
  count += 1;
  for (int s = 0; s < b; ++s) {
    if (s + HD <= 11) { count += 4; if (s + HD == b) last = count; }
    if (s == 9) count += 5;
    if (s >= 2 && s <= 7) { count += 5; if (s + 4 == b + 1) last = count; }
  }
  return count - last;
}
__global__ __launch_bounds__(512, 2) void fused_kernel(Args a) {
  extern __shared__ __attribute__((aligned(16))) char smem[];
  unsigned long long probe_entry = 0;
  if (PROBE_ENTRY) { asm volatile("s_memrealtime %0\n\ts_waitcnt lgkmcnt(0)" : "=s"(probe_entry) :: "memory"); }
  lptr lds = (lptr)smem;
  const int tid = threadIdx.x, lane = tid & 63;
  const int wave = __builtin_amdgcn_readfirstlane(tid >> 6);
  const int bid = blockIdx.x;
  const int xcd = bid & 7, j = bid >> 3;
  const int mi = 8 * (xcd & 1) + (j & 7), fi = 4 * (xcd >> 1) + (j >> 3);
  const int si = 8 * (xcd & 3) + (j & 7), di = 4 * (xcd >> 2) + (j >> 3);
  const int c0 = (5 * bid) >> 1, nconv = 2 + (bid & 1);
  const unsigned lds0 = lds_addr(lds);
  STAMP_INIT();
  STAMP(0);

  if (wave == 7) {
    if (lane < 32) *sy(lds, lane) = 0u;
    WAIT_LGKM0();
  }
  BARRIER();

  if (wave == 7) {
    const unsigned epoch = __hip_atomic_load((GAS unsigned*)(a.ws + WS_EPOCH), __ATOMIC_RELAXED, __HIP_MEMORY_SCOPE_AGENT);
    {
      const int r0 = 128 * mi + 8 * fi, gr = lane >> 4, gq = lane & 15;
      u32x4 gx[2][5], gw[5];
#pragma unroll
      for (int i = 0; i < 5; ++i) {
        gw[i] = ((const u32x4*)a.gw)[gq + 16 * i];
        gx[0][i] = ((const u32x4*)(a.x + (size_t)(r0 + gr) * D))[gq + 16 * i];
        gx[1][i] = ((const u32x4*)(a.x + (size_t)(r0 + 4 + gr) * D))[gq + 16 * i];
      }
      FENCE();
      sy_set(lds, SY_TAG, epoch + 1u, lane);
      sy_set(lds, SY_TAGOK, 1u, lane);
      BARRIER();
#pragma unroll
      for (int pass = 0; pass < 2; ++pass) {
        float sum = 0.f;
#pragma unroll
        for (int i = 0; i < 5; ++i)
#pragma unroll
          for (int e = 0; e < 4; ++e) sum = dot2(gx[pass][i][e], gw[i][e], sum);
#pragma unroll
        for (int m = 8; m >= 1; m >>= 1) sum += __shfl_xor(sum, m);
        if (gq == 0)
          __hip_atomic_store((GAS unsigned*)(a.ws + WS_GATE) + r0 + 4 * pass + gr, __builtin_bit_cast(unsigned, sigmoidf_(sum)),
                             __ATOMIC_RELAXED, __HIP_MEMORY_SCOPE_AGENT);
      }
    }
    const unsigned tag = epoch + 1u;
    STAMP(1);
#pragma unroll 1
    for (int s = 0; s < 9; ++s) {
      BARRIER();
      if (s == 3 && lane == 0) flag_store(a.ws, WS_FLAGKV + 4 * (32 * (bid & 7) + (bid >> 3)), tag);
      if (s == 5) {
        WAIT_VM(0);
        sy_set(lds, SY_GATE, 1u, lane);
      }
      if (s == WD_TOUCH_AT) {
        const char* base = (const char*)a.wd + 640 * (xcd >> 2);
#pragma unroll
        for (int t = 0; t < 4; ++t) {
          const int id = min(64 * t + lane, 239), r = id / 5, c = id - 5 * r;
          touch(base + (size_t)(48 * j + r) * (D * 2) + 128 * c, lds0 + L_DUMMY);
        }
      }
    }
    STAMP(2);
    {
      flag_wait(a.ws, WS_FLAGKV + 4 * (32 * (bid >> 5) + (lane & 31)), true, tag);
      STAMP(8);
      u32x4 uv[3][4];
#pragma unroll
      for (int cw = 0; cw < 3; ++cw) {
        const __amdgpu_buffer_rsrc_t ru = make_rsrc(a.ws + WS_KVR + (size_t)(c0 + (cw < nconv ? cw : 0)) * S * 2, S * 2);
#pragma unroll
        for (int i = 0; i < 4; ++i) uv[cw][i] = ld128(ru, (unsigned)(i * 1024 + lane * 16), 0, SC1);
      }
      FENCE();
#pragma unroll
      for (int cw = 0; cw < 3; ++cw) {
        lptr ur = lds + L_AUX + cw * UR_BYTES;
#pragma unroll
        for (int i = 0; i < 4; ++i) *(LAS u32x4*)(ur + i * 1024 + lane * 16) = uv[cw][i];
        if (lane < 32) *(LAS u32x4*)(ur + 4096 + lane * 16) = (u32x4){0u, 0u, 0u, 0u};
      }
      WAIT_LGKM0();
      sy_set(lds, SY_AREADY, 1u, lane);
    }
    STAMP(9);
    if (bid == 0) {
#pragma unroll
      for (int i = 0; i < 4; ++i) flag_wait(a.ws, WS_FLAGKV + 4 * (lane + 64 * i), true, tag);
      if (lane == 0) flag_store(a.ws, WS_EPOCH, tag);
    }
    sy_wait(lds, SY_WPRE, 4u);
    flag_wait<HPOLL_SLEEP>(a.ws, WS_FLAGH + 4 * (16 * (si >> 1) + (lane & 15)), true, tag);
    sy_set(lds, SY_HREADY, 1u, lane);
    STAMP(10);
    sy_wait(lds, SY_CONV, nconv);
    sy_wait(lds, SY_GSTEP, 1u);
    flag_wait<YPOLL_SLEEP>(a.ws, WS_FLAGY + 4 * (32 * di + (lane & 31)), true, tag);
    STAMP(11);
    {
      const __amdgpu_buffer_rsrc_t ry = make_rsrc(a.ws + WS_Y2T, S * D * 2);
      u32x4 yv[10];
#pragma unroll
      for (int i = 0; i < 10; ++i) {
        const int id = lane + 64 * i;
        yv[i] = ld128(ry, (unsigned)((80 * di + (id >> 3)) * (S * 2) + 64 * si * 2 + (id & 7) * 16), 0, SC1);
      }
#pragma unroll
      for (int i = 0; i < 10; ++i) {
        const int id = lane + 64 * i, w = (id & 7) >> 1;
        const int so = w == 0 ? ystrip_off(0) : w == 1 ? ystrip_off(1) : w == 2 ? ystrip_off(2) : ystrip_off(3);
        *(LAS u32x4*)(lds + so + (id >> 3) * 32 + (id & 1) * 16) = yv[i];
      }
      WAIT_LGKM0();
      sy_set(lds, SY_YREADY, 1u, lane);
    }
    STAMP(12);
    WAIT_VM(0);
    STAMP_DUMP();
    return;
  }

  if (wave >= 4) {
    const int cw = wave - 4;
    const bool active = cw < nconv;
    const int ch = c0 + (active ? cw : 0);
    Filter flt;
    const int rsum = (j >> 3) + (j & 7);
    const int rot1 = ROT1 == 1 ? (3 * (j >> 3) + (j & 7)) % 10 : ROT1 == 2 ? 5 * (rsum & 1) : ROT1 == 3 ? 3 * (rsum % 3) : ROT1 == 4 ? (5 * (rsum & 3)) >> 1 : 0;
    if (cw == 0) loader<6>(a, lds, 0, 0, mi, fi, rot1, flt, ch, lane);
    else loader<5>(a, lds, cw, 1 + 5 * cw, mi, fi, rot1, flt, ch, lane);
    STAMP(3);
    if (active) {
      conv_wave(a, lds, lds + (cw == 2 ? 2 * STAGE : STAGE + cw * KC_BYTES), lds + L_AUX + cw * UR_BYTES, flt, ch, nconv, lane);
    }
    STAMP_DUMP();
    return;
  }

  unsigned long long probe_t0 = PROBE_ENTRY ? probe_entry : PROBE_AT ? __builtin_amdgcn_s_memrealtime() : 0ull;
  if (PROBE_GLOBAL && tid == 0)
    __hip_atomic_store((GAS unsigned long long*)(a.ws + (32u << 20)) + bid, probe_t0, __ATOMIC_RELAXED, __HIP_MEMORY_SCOPE_AGENT);
  (void)probe_t0;
  f32x4 ag[4][3], au[4][3];
  {
    const int tsi = bid >> 3, tdi = bid & 7;
    const int task = 40 * wave + (lane < 40 ? lane : 0);
    KvTask t0;
    kv_load(a, t0, task, tsi, tdi);
    FENCE();
#pragma unroll
    for (int t = 0; t < 4; ++t)
#pragma unroll
      for (int c = 0; c < 3; ++c) {
        ag[t][c] = (f32x4){0.f, 0.f, 0.f, 0.f}; au[t][c] = (f32x4){0.f, 0.f, 0.f, 0.f};
        asm volatile("" : "+v"(ag[t][c]), "+v"(au[t][c]));
      }
    FENCE();
    STAMP(1);
    if (lane < 40) kv_compute(t0, lds, task, tsi);
    sy_arrive(lds, SY_KV, lane);
    sy_wait(lds, SY_KV, 4u);
    STAMP(2);
    const __amdgpu_buffer_rsrc_t rkv = make_rsrc(a.ws + WS_KVR, S * D * 2);
#pragma unroll
    for (int i = 0; i < 3; ++i) {
      const int id = tid + 256 * i;
      if (id < 640) {
        const int dl = id >> 3, jj = id & 7;
        const u32x4 val = *(LAS u32x4*)(lds + L_T + dl * 128 + ((jj ^ ((dl >> 3) & 7)) << 4));
        __builtin_amdgcn_raw_buffer_store_b128(val, rkv, (unsigned)(((80 * tdi + dl) * S + (1984 - 64 * tsi) + 8 * jj) * 2), 0, SC1);
      }
    }
  }
  const int n = lane & 15, g = lane >> 4;
  {
    const int ws_ = wave & 1, wf = wave >> 1;
    const int q = n >> 2, p = lane & 3;
    const int xb0 = XB + (64 * ws_ + n) * 128 + ((g ^ (n >> 1)) << 4);
    int wb_[3];
#pragma unroll
    for (int c = 0; c < 3; ++c) wb_[c] = GB + (8 * g + q) * 192 + 8 * p + 32 * ((3 * wf + c) ^ (g & 1));
    lptr xa[3][2], wa[3][3];
#pragma unroll
    for (int b = 0; b < 3; ++b) {
#pragma unroll
      for (int kk = 0; kk < 2; ++kk) { xa[b][kk] = lds + b * STAGE + (xb0 ^ (kk << 6)); asm volatile("" : "+v"(xa[b][kk])); }
#pragma unroll
      for (int c = 0; c < 3; ++c) { wa[b][c] = lds + b * STAGE + wb_[c]; asm volatile("" : "+v"(wa[b][c])); }
    }
    auto read_piece = [&](Frags& f, int i, int b, int kk) {
      if (i < 4) {
        f.x[i] = lds_frag(xa[b][kk] + i * 2048);
      } else {
        const int c = (i - 4) >> 2, u = ((i - 4) >> 1) & 1, t = (i - 4) & 1;
        const s16x4 v = lds_tr(wa[b][c] + kk * (32 * 192) + u * (UB - GB) + t * (4 * 192));
        if (u == 0) f.g[c][t] = v; else f.u[c][t] = v;
      }
    };
    auto mma_piece = [&](const Frags& f, int i) {
      const int c = i >> 3, t = (i >> 1) & 3;
      if ((i & 1) == 0) ag[t][c] = MFMA16(join(f.g[c][0], f.g[c][1]), f.x[t], ag[t][c]);
      else au[t][c] = MFMA16(join(f.u[c][0], f.u[c][1]), f.x[t], au[t][c]);
    };
    auto half = [&](const Frags& cur, Frags& nxt, int nb, int nkk) {
#pragma unroll
      for (int i = 0; i < 24; ++i) {
        mma_piece(cur, i); FENCE();
        if (i < 16) { read_piece(nxt, i, nb, nkk); FENCE(); }
      }
    };
    BARRIER();
    PROBE(1);
    STAMP(3);
    STAMPC(28);
    __builtin_amdgcn_s_setprio(M_PRIO);
    Frags f0, f1;
#pragma unroll
    for (int i = 0; i < 16; ++i) read_piece(f0, i, 0, 0);
    FENCE();
    STAMPC(27);
    auto iteration = [&](int b, bool drain) {
      FENCE();
      half(f0, f1, b, 1);
      if (drain) WAIT_VM(0);
      WAIT_LGKM0();
      BARRIER();
      FENCE();
      half(f1, f0, (b + 1) % 3, 0);
    };
#pragma unroll 1
    for (int s3 = 0; s3 < 3; ++s3) {
      iteration(0, s3 == 1);
      iteration(1, false);
      iteration(2, false);
    }
    half(f0, f1, 0, 1);
    const __amdgpu_buffer_rsrc_t rhs = make_rsrc(a.ws + WS_HT, S * F * 2);
    const lptr exch = lds + L_FREE + ws_ * 2048 + lane * 8;
    u32x2 keep[4];
    auto epilogue = [&](int t) {
      u32x2 pk[3];
#pragma unroll
      for (int c = 0; c < 3; ++c) {
        const f32x2 s01 = (f32x2){ag[t][c][0], ag[t][c][1]} * -1.4426950408889634f, s23 = (f32x2){ag[t][c][2], ag[t][c][3]} * -1.4426950408889634f;
        const f32x2 d01 = (f32x2){__builtin_amdgcn_exp2f(s01[0]), __builtin_amdgcn_exp2f(s01[1])} + 1.0f;
        const f32x2 d23 = (f32x2){__builtin_amdgcn_exp2f(s23[0]), __builtin_amdgcn_exp2f(s23[1])} + 1.0f;
        const f32x2 h01 = (f32x2){__builtin_amdgcn_rcpf(d01[0]), __builtin_amdgcn_rcpf(d01[1])} * (f32x2){au[t][c][0], au[t][c][1]};
        const f32x2 h23 = (f32x2){__builtin_amdgcn_rcpf(d23[0]), __builtin_amdgcn_rcpf(d23[1])} * (f32x2){au[t][c][2], au[t][c][3]};
        pk[c] = (u32x2){pack_bf16(h01[0], h01[1]), pack_bf16(h23[0], h23[1])};
      }
      const unsigned base = (unsigned)(((8 * mi + 4 * ws_ + t) * 48 + 3 * fi) * 1024 + lane * 16);
      if (wf == 0) {
        __builtin_amdgcn_raw_buffer_store_b128((u32x4){pk[0][0], pk[0][1], pk[1][0], pk[1][1]}, rhs, base, 0, SC1);
        keep[t] = pk[2];
      } else {
        *(LAS u32x2*)(exch + t * 512) = pk[0];
        __builtin_amdgcn_raw_buffer_store_b128((u32x4){pk[1][0], pk[1][1], pk[2][0], pk[2][1]}, rhs, base + 2048, 0, SC1);
      }
    };
#pragma unroll
    for (int t = 0; t < 4; ++t) {
      FENCE();
#pragma unroll
      for (int c = 0; c < 3; ++c) {
        ag[t][c] = MFMA16(join(f1.g[c][0], f1.g[c][1]), f1.x[t], ag[t][c]);
        au[t][c] = MFMA16(join(f1.u[c][0], f1.u[c][1]), f1.x[t], au[t][c]);
      }
      FENCE();
      if (t == 0) { PROBE(2); STAMP(4); STAMPC(29); }
      if (t > 0) epilogue(t - 1);
    }
    FENCE();
    epilogue(3);
    if (wf == 0) {
      sy_wait(lds, SY_EX + ws_, 1u);
#pragma unroll
      for (int t = 0; t < 4; ++t) {
        const u32x2 o = *(LAS u32x2*)(exch + t * 512);
        __builtin_amdgcn_raw_buffer_store_b128((u32x4){keep[t][0], keep[t][1], o[0], o[1]}, rhs,
                                               (unsigned)(((8 * mi + 4 * ws_ + t) * 48 + 3 * fi + 1) * 1024 + lane * 16), 0, SC1);
      }
    } else {
      WAIT_LGKM0();
      sy_set(lds, SY_EX + ws_, 1u, lane);
    }
  }
  STAMP(5);

  const __amdgpu_buffer_rsrc_t rh = make_rsrc(a.ws + WS_HT, S * F * 2);
  const __amdgpu_buffer_rsrc_t rw = make_rsrc(a.wd, F * D * 2);
  const unsigned wr0 = wave == 0 ? wring_off(0) : wave == 1 ? wring_off(1) : wave == 2 ? wring_off(2) : wring_off(3);
  const int rot = 3 * (j >> 3);
  auto kstep = [&](int b) { const int t = b + rot; return 12 * wave + (t >= 12 ? t - 12 : t); };
  unsigned vwd[5];
#pragma unroll
  for (int i = 0; i < 5; ++i) {
    const int slot = 64 * i + lane, row = slot / 10, ch = slot - 10 * row;
    vwd[i] = (unsigned)(row * (D * 2) + 160 * di + ch * 16 - 1024 * (i & 3));
  }
  auto load_w = [&](int b) {
    const unsigned dst = lds0 + wr0 + (b & 3) * WSLOT, so = (unsigned)kstep(b) * (32 * D * 2);
    dma16xN<4>(rw, dst, vwd, so);
    dma16xN<1>(rw, dst + 4096, vwd + 4, so);
  };
  auto load_w1 = [&](int b, int i) {
    dma16(rw, lds0 + wr0 + (b & 3) * WSLOT + i * 1024, vwd[i] + 1024 * (i & 3), (unsigned)kstep(b) * (32 * D * 2));
  };
  struct HS4 { u32x4 f[4]; };
  HS4 H[HD + 1];
  const unsigned voff = lane * 16;
  auto load_h = [&](HS4& h, int b) {
    const unsigned so = (unsigned)(4 * si * 48 + kstep(b)) * 1024;
#define HPOL " sc1"
    asm volatile("buffer_load_dwordx4 %0, %4, %5, %6 offen" HPOL "\n\tbuffer_load_dwordx4 %1, %4, %5, %7 offen" HPOL "\n\t"
                 "buffer_load_dwordx4 %2, %4, %5, %8 offen" HPOL "\n\tbuffer_load_dwordx4 %3, %4, %5, %9 offen" HPOL
                 : "=&v"(h.f[0]), "=&v"(h.f[1]), "=&v"(h.f[2]), "=&v"(h.f[3])
                 : "v"(voff), "s"(rh), "s"(so), "s"(so + 48 * 1024), "s"(so + 96 * 1024), "s"(so + 144 * 1024) : "memory");
  };
  auto load_h1 = [&](HS4& h, int b, int i) {
    const unsigned so = (unsigned)((4 * si + i) * 48 + kstep(b)) * 1024;
    asm volatile("buffer_load_dwordx4 %0, %1, %2, %3 offen" HPOL : "=&v"(h.f[i]) : "v"(voff), "s"(rh), "s"(so) : "memory");
  };
  const int srow = 64 * si + 16 * wave + n;
  u32x2 qv[5];
  FENCE();
  __builtin_amdgcn_s_setprio(3);
  sy_arrive(lds, SY_EPI, lane);
  STAMP(22);
  FENCE();
  WAIT_VM(0);
  if (sy_arrive(lds, SY_HDONE, lane) == 3u) {
    sy_wait(lds, SY_GATE, 1u);
    const unsigned tag = get_tag(lds);
    if (lane == 0) flag_store(a.ws, WS_FLAGH + 4 * (16 * mi + fi), tag);
    WAIT_VM(0);
  }
  sy_wait(lds, SY_EPI, 4u);
  load_w(0); load_w(1); load_w(2); load_w(3);
  STAMP(23);
  f32x4 acc[4][5];
#pragma unroll
  for (int t = 0; t < 4; ++t)
#pragma unroll
    for (int c = 0; c < 5; ++c) { acc[t][c] = (f32x4){0.f, 0.f, 0.f, 0.f}; asm volatile("" : "+v"(acc[t][c])); }
  struct WF { s16x4 f[5][2]; };
  WF w0, w1;
  const lptr wrd = lds + wr0 + (4 * g + (n >> 2)) * 160 + 8 * (lane & 3);
  auto read_w = [&](WF& w, int p, int b) {
    w.f[p >> 1][p & 1] = lds_tr(wrd + (b & 3) * WSLOT + (p & 1) * (16 * 160) + (p >> 1) * 32);
  };
  STAMP(24);
  WAIT_VM(10);
#pragma unroll
  for (int p = 0; p < 10; ++p) read_w(w0, p, 0);
#pragma unroll
  for (int p = 0; p < 10; ++p) read_w(w1, p, 1);
  WAIT_LGKM0();
  load_w(4);
  load_w(5);
  sy_arrive(lds, SY_WPRE, lane);
  __builtin_amdgcn_s_setprio(G_PRIO);
  STAMP(6);
  sy_wait(lds, SY_HREADY, 1u);
  PROBE(3);
  STAMP(7);
#pragma unroll
  for (int b = 0; b < HD; ++b) load_h(H[b], b);
  const float gate = __builtin_bit_cast(float, __hip_atomic_load((GAS unsigned*)(a.ws + WS_GATE) + srow, __ATOMIC_RELAXED, __HIP_MEMORY_SCOPE_AGENT));
  FENCE();
  auto gstep = [&](const WF& cur, WF& nxt, HS4& h, HS4& hn, int b) {
    asm volatile("s_waitcnt vmcnt(%4) lgkmcnt(0)" : "+v"(h.f[0]), "+v"(h.f[1]), "+v"(h.f[2]), "+v"(h.f[3]) : "n"(g_younger(b)) : "memory");
    FENCE();
#pragma unroll
    for (int i = 0; i < 20; ++i) {
      acc[i & 3][i >> 2] = MFMA16(join(cur.f[i >> 2][0], cur.f[i >> 2][1]), __builtin_bit_cast(bf16x8, h.f[i & 3]), acc[i & 3][i >> 2]);
      FENCE();
      if (i < 8 && (i & 1) == 0 && b + HD < 12) { load_h1(hn, b + HD, i >> 1); FENCE(); }
      if (i == 7 && b == 9) {
#pragma unroll
        for (int c = 0; c < 5; ++c) qv[c] = *(const u32x2*)(a.x + (size_t)srow * D + 80 * di + 16 * c + 4 * g);
        asm volatile("" ::: "memory");
        FENCE();
      }
      if (i < 10 && b >= 1 && b + 1 < 12) { read_w(nxt, i, b + 1); FENCE(); }
      if (i >= 10 && (i & 1) == 0 && b >= 2 && b + 4 < 12) { load_w1(b + 4, (i - 10) >> 1); FENCE(); }
      if (b == YPOLL_STEP && i == 17 && wave == 0) { sy_post(lds0 + L_SYNC + 4 * SY_GSTEP, 1u); FENCE(); }
      if (b == YPOLL_STEP && i == 19 && wave == 0) { wake(); FENCE(); }
    }
  };
#pragma unroll
  for (int b = 0; b < 12; b += 2) {
    gstep(w0, w1, H[b % (HD + 1)], H[(b + HD) % (HD + 1)], b);
    if (b == 0) STAMPI(14);
    gstep(w1, w0, H[(b + 1) % (HD + 1)], H[(b + 1 + HD) % (HD + 1)], b + 1);
    STAMPI(15 + (b >> 1));
  }
  PROBE(4);
  STAMP(8);

  f32x4 bsum[5];
  {
#pragma unroll
    for (int t = 0; t < 4; ++t) {
      if (t != wave) {
#pragma unroll
        for (int c = 0; c < 5; ++c) *(LAS f32x4*)(lds + wr0 + (((t - wave - 1) & 3) * 5 + c) * 1024 + lane * 16) = acc[t][c];
      } else {
#pragma unroll
        for (int c = 0; c < 5; ++c) bsum[c] = acc[t][c];
      }
    }
    sy_arrive(lds, SY_RED, lane);
    sy_wait(lds, SY_RED, 4u);
#pragma unroll
    for (int w = 0; w < 4; ++w) {
      if (w != wave) {
#pragma unroll
        for (int c = 0; c < 5; ++c) bsum[c] += *(LAS f32x4*)(lds + wring_off(w) + (((wave - w - 1) & 3) * 5 + c) * 1024 + lane * 16);
      }
    }
  }
  STAMP(9);
  sy_wait(lds, SY_YREADY, 1u);
  STAMP(10);
  {
    const int q = n >> 2, p = lane & 3;
    const __amdgpu_buffer_rsrc_t ro = make_rsrc(a.out, S * D * 2);
    const lptr ystr = lds + (wave == 0 ? ystrip_off(0) : wave == 1 ? ystrip_off(1) : wave == 2 ? ystrip_off(2) : ystrip_off(3));
#pragma unroll
    for (int c = 0; c < 5; ++c) {
      const s16x4 yv = lds_tr(ystr + (16 * c + 4 * g + q) * 32 + 8 * p);
      const u32x2 y = __builtin_bit_cast(u32x2, yv);
      const float o0 = gate * (bf_lo(qv[c][0]) * bf_lo(y[0])) + (1.f - gate) * bsum[c][0];
      const float o1 = gate * (bf_hi(qv[c][0]) * bf_hi(y[0])) + (1.f - gate) * bsum[c][1];
      const float o2 = gate * (bf_lo(qv[c][1]) * bf_lo(y[1])) + (1.f - gate) * bsum[c][2];
      const float o3 = gate * (bf_hi(qv[c][1]) * bf_hi(y[1])) + (1.f - gate) * bsum[c][3];
      __builtin_amdgcn_raw_buffer_store_b64((u32x2){pack_bf16(o0, o1), pack_bf16(o2, o3)}, ro,
                                            (unsigned)((srow * D + 80 * di + 16 * c + 4 * g) * 2), 0, OUT_AUX);
    }
  }
  if (PROBE_GLOBAL) {
    unsigned long long r = PROBE_GLOBAL == 1 ? ~0ull : 0ull;
    for (int i = 0; i < 4; ++i) {
      const unsigned long long v = __hip_atomic_load((GAS unsigned long long*)(a.ws + (32u << 20)) + lane + 64 * i, __ATOMIC_RELAXED, __HIP_MEMORY_SCOPE_AGENT);
      r = PROBE_GLOBAL == 1 ? min(r, v) : max(r, v);
    }
    for (int m = 32; m >= 1; m >>= 1) { const unsigned long long o = __shfl_xor(r, m); r = PROBE_GLOBAL == 1 ? min(r, o) : max(r, o); }
    probe_t0 = __builtin_amdgcn_readfirstlane((unsigned)r) | ((unsigned long long)__builtin_amdgcn_readfirstlane((unsigned)(r >> 32)) << 32);
  }
  PROBE(5);
  STAMP(11);
}

}

extern "C" void kernel_launch(void* const* d_in, const int* in_sizes, int n_in, void* d_out, int out_size, void* d_ws,
                              size_t ws_size, hipStream_t stream) {
  (void)in_sizes; (void)n_in; (void)out_size; (void)ws_size;
  Args a;
  a.x = (const u16*)d_in[0];
  a.k = (const u16*)d_in[1];
  a.v = (const u16*)d_in[2];
  a.shk = (const u16*)d_in[3];
  a.ssmk = (const u16*)d_in[4];
  a.ssmd = (const u16*)d_in[5];
  a.wg = (const u16*)d_in[7];
  a.wu = (const u16*)d_in[8];
  a.wd = (const u16*)d_in[9];
  a.gw = (const u16*)d_in[10];
  a.ws = (char*)d_ws;
  a.out = (u16*)d_out;
  (void)hipFuncSetAttribute((const void*)fused_kernel, hipFuncAttributeMaxDynamicSharedMemorySize, LDS_BYTES);
  fused_kernel<<<256, 512, LDS_BYTES, stream>>>(a);
}
```

```cpp
#include <hip/hip_runtime.h>
#include <hip/hip_bf16.h>

namespace {

typedef unsigned short u16;
typedef __bf16 bf16x8 __attribute__((ext_vector_type(8)));
typedef __bf16 bf16x2 __attribute__((ext_vector_type(2)));
typedef short s16x4 __attribute__((ext_vector_type(4)));
typedef float f32x2 __attribute__((ext_vector_type(2)));
typedef float f32x4 __attribute__((ext_vector_type(4)));
typedef unsigned u32x2 __attribute__((ext_vector_type(2)));
typedef unsigned u32x4 __attribute__((ext_vector_type(4)));
#define LAS __attribute__((address_space(3)))
#define GAS __attribute__((address_space(1)))
typedef LAS char* lptr;

constexpr int S = 2048, D = 640, F = 1536;
constexpr int SC1 = 16;

constexpr size_t WS_HT = 0;
constexpr size_t WS_KVR = WS_HT + (size_t)S * F * 2;
constexpr size_t WS_Y2T = WS_KVR + (size_t)S * D * 2;
constexpr size_t WS_GATE = WS_Y2T + (size_t)S * D * 2;
constexpr size_t WS_FLAGKV = WS_GATE + (size_t)S * 4;
constexpr size_t WS_FLAGH = WS_FLAGKV + 1024;
constexpr size_t WS_FLAGY = WS_FLAGH + 1024;
constexpr size_t WS_EPOCH = WS_FLAGY + 1024;
constexpr size_t WS_END = WS_EPOCH + 256;

constexpr int STAGE = 40960, XB = 0, GB = 16384, UB = 28672;
constexpr int L_AUX = 3 * STAGE;
constexpr int L_T = L_AUX, UR_BYTES = 4608;
constexpr int L_FREE = L_AUX + 3 * UR_BYTES;
constexpr int L_SYNC = L_FREE + 20480 + 2 * 2560;
constexpr int L_DUMMY = L_SYNC + 128;
constexpr int L_END = L_DUMMY + 256;
constexpr int KC_COPY = 4160, KC_BYTES = 4 * KC_COPY;
constexpr int WSLOT = 5120;
__device__ constexpr int wring_off(int w) { return w == 0 ? 0 : w == 1 ? 20480 : w == 2 ? 2 * STAGE + KC_BYTES : L_FREE; }
__device__ constexpr int ystrip_off(int w) { return w < 2 ? STAGE + 2 * KC_BYTES + w * 2560 : L_FREE + 20480 + (w - 2) * 2560; }
static_assert(2 * STAGE + KC_BYTES + 20480 <= 3 * STAGE && STAGE + 2 * KC_BYTES + 2 * 2560 <= 2 * STAGE, "second-phase LDS map");
enum { SY_LANDED = 0, SY_CONSUMED = 4, SY_KV = 8, SY_SIDE = 9, SY_HDONE = 10, SY_RED = 11, SY_CONV = 12, SY_AREADY = 13,
       SY_HREADY = 14, SY_YREADY = 15, SY_TAG = 16, SY_TAGOK = 17, SY_GSTEP = 18, SY_GATE = 19, SY_EPI = 20, SY_WPRE = 21, SY_EX = 22 };

#define STAMP(i) do {} while (0)
#define STAMPC(i) do {} while (0)
#define STAMPI(i) do {} while (0)
#define STAMP_INIT() do {} while (0)
#define STAMP_DUMP() do {} while (0)
constexpr int LDS_BYTES = L_END;

#define PROBE_AT 0
#define PROBE_US 0
#define PROBE(k) do { if (PROBE_AT == (k)) { while (__builtin_amdgcn_s_memrealtime() - probe_t0 < 100ull * PROBE_US) __builtin_amdgcn_s_sleep(1); } } while (0)
#define PROBE_ENTRY 0
#define PROBE_GLOBAL 0
#define MFMA16(a, b, c) __builtin_amdgcn_mfma_f32_16x16x32_bf16((a), (b), (c), 0, 0, 0)
#define WAIT_VM(n) asm volatile("s_waitcnt vmcnt(" #n ")" ::: "memory")
#define WAIT_LGKM0() asm volatile("s_waitcnt lgkmcnt(0)" ::: "memory")
#define BARRIER() do { asm volatile("" ::: "memory"); __builtin_amdgcn_s_barrier(); asm volatile("" ::: "memory"); } while (0)
#define FENCE() __builtin_amdgcn_sched_barrier(0)
constexpr unsigned SPIN_LDS = 1u << 22, SPIN_MEM = 1u << 16;

__device__ __forceinline__ unsigned pack_bf16(float lo, float hi) {
  f32x2 v = {lo, hi};
  return __builtin_bit_cast(unsigned, __builtin_convertvector(v, bf16x2));
}
__device__ __forceinline__ float bf_lo(unsigned w) { return __builtin_bit_cast(float, w << 16); }
__device__ __forceinline__ float bf_hi(unsigned w) { return __builtin_bit_cast(float, w & 0xffff0000u); }
__device__ __forceinline__ float sigmoidf_(float x) {
  return __builtin_amdgcn_rcpf(1.0f + __builtin_amdgcn_exp2f(x * -1.4426950408889634f));
}
__device__ __forceinline__ __amdgpu_buffer_rsrc_t make_rsrc(const void* p, unsigned bytes) {
  return __builtin_amdgcn_make_buffer_rsrc(const_cast<void*>(p), 0, bytes, 0x00020000);
}
__device__ __forceinline__ unsigned lds_addr(lptr p) { return (unsigned)(size_t)p; }
__device__ __forceinline__ void dma16(__amdgpu_buffer_rsrc_t r, unsigned dst, unsigned voff, unsigned soff) {
  asm volatile("s_mov_b32 m0, %0\n\ts_nop 0\n\tbuffer_load_dwordx4 %1, %2, %3 offen lds"
               :: "s"(dst), "v"(voff), "s"(r), "s"(soff) : "memory");
}
template <int N>
__device__ __forceinline__ void dma16xN(__amdgpu_buffer_rsrc_t r, unsigned dst, const unsigned* v, unsigned soff) {
  static_assert(N >= 1 && N <= 4, "12-bit instruction offset");
  if constexpr (N == 1)
    asm volatile("s_mov_b32 m0, %0\n\ts_nop 0\n\tbuffer_load_dwordx4 %1, %2, %3 offen lds"
                 :: "s"(dst), "v"(v[0]), "s"(r), "s"(soff) : "memory");
  else if constexpr (N == 2)
    asm volatile("s_mov_b32 m0, %0\n\ts_nop 0\n\tbuffer_load_dwordx4 %1, %3, %4 offen lds\n\t"
                 "buffer_load_dwordx4 %2, %3, %4 offen offset:1024 lds"
                 :: "s"(dst), "v"(v[0]), "v"(v[1]), "s"(r), "s"(soff) : "memory");
  else if constexpr (N == 3)
    asm volatile("s_mov_b32 m0, %0\n\ts_nop 0\n\tbuffer_load_dwordx4 %1, %4, %5 offen lds\n\t"
                 "buffer_load_dwordx4 %2, %4, %5 offen offset:1024 lds\n\tbuffer_load_dwordx4 %3, %4, %5 offen offset:2048 lds"
                 :: "s"(dst), "v"(v[0]), "v"(v[1]), "v"(v[2]), "s"(r), "s"(soff) : "memory");
  else
    asm volatile("s_mov_b32 m0, %0\n\ts_nop 0\n\tbuffer_load_dwordx4 %1, %5, %6 offen lds\n\t"
                 "buffer_load_dwordx4 %2, %5, %6 offen offset:1024 lds\n\tbuffer_load_dwordx4 %3, %5, %6 offen offset:2048 lds\n\t"
                 "buffer_load_dwordx4 %4, %5, %6 offen offset:3072 lds"
                 :: "s"(dst), "v"(v[0]), "v"(v[1]), "v"(v[2]), "v"(v[3]), "s"(r), "s"(soff) : "memory");
}
__device__ __forceinline__ void touch(const void* p, unsigned dummy_lds) {
  asm volatile("s_mov_b32 m0, %0\n\ts_nop 0\n\tglobal_load_lds_dword %1, off" :: "s"(dummy_lds), "v"(p) : "memory");
}
__device__ __forceinline__ s16x4 lds_tr(lptr p) { return __builtin_amdgcn_ds_read_tr16_b64_v4i16((LAS s16x4*)p); }
__device__ __forceinline__ bf16x8 join(s16x4 a, s16x4 b) {
  return __builtin_bit_cast(bf16x8, __builtin_shufflevector(a, b, 0, 1, 2, 3, 4, 5, 6, 7));
}
__device__ __forceinline__ bf16x8 lds_frag(lptr p) { return *(LAS bf16x8*)p; }
__device__ __forceinline__ u32x4 ld128(__amdgpu_buffer_rsrc_t r, unsigned voff, unsigned soff, int aux) {
  return aux ? __builtin_bit_cast(u32x4, __builtin_amdgcn_raw_buffer_load_b128(r, voff, soff, SC1))
             : __builtin_bit_cast(u32x4, __builtin_amdgcn_raw_buffer_load_b128(r, voff, soff, 0));
}

__device__ __forceinline__ LAS unsigned* sy(lptr lds, int i) { return (LAS unsigned*)(lds + L_SYNC) + i; }
__device__ __forceinline__ unsigned sy_get(lptr lds, int i) {
  return __builtin_amdgcn_readfirstlane(__hip_atomic_load(sy(lds, i), __ATOMIC_RELAXED, __HIP_MEMORY_SCOPE_WORKGROUP));
}
__device__ __forceinline__ void wake() { asm volatile("s_wakeup" ::: "memory"); }
__device__ __forceinline__ void sy_set(lptr lds, int i, unsigned v, int lane) {
  if (lane == 0) __hip_atomic_store(sy(lds, i), v, __ATOMIC_RELAXED, __HIP_MEMORY_SCOPE_WORKGROUP);
  asm volatile("s_waitcnt lgkmcnt(0)\n\ts_wakeup" ::: "memory");
}
#define TOUCH_H 0
#define LOADER_POST_FIRST 1
#define HPOLL_SLEEP 4
#define YPOLL_SLEEP 8
#define YPOLL_STEP 7
#ifndef M_PRIO
#define M_PRIO 2
#endif
#define G_PRIO 0
#define ROT1 4
#define DMA_SPACE 0
#define C_PRIO 2
#define G_HDEPTH 3
#define OUT_AUX 0
#define H_ACQUIRE 0
#define CODE_LINES 0
#define CODE_WAVE 7
#define PUB_FIRST 1
#define WD_TOUCH_AT 7
#define PROLOG 0
#define LOADER_DELAY 0
#define LAND_RD 18
#define TOUCH_DELAY 8
#define TOUCH1 0
#define FILT_ITER 6
#define W7_DELAY 0
#define POLL_AFTER 8
#define LOADER_PRIO 0
#define SLEEP_LDS 4
__device__ __forceinline__ void sy_post(unsigned addr, unsigned v) {
  asm volatile("s_mov_b64 exec, 1\n\tds_write_b32 %0, %1\n\ts_mov_b64 exec, -1" :: "v"(addr), "v"(v) : "memory");
}
__device__ __forceinline__ void sy_wait(lptr lds, int i, unsigned v) {
#pragma unroll 1
  for (unsigned n = 0; n < SPIN_LDS; ++n) {
    if (sy_get(lds, i) >= v) break;
    __builtin_amdgcn_s_sleep(SLEEP_LDS);
  }
  asm volatile("" ::: "memory");
}
__device__ __forceinline__ unsigned sy_arrive(lptr lds, int i, int lane) {
  WAIT_LGKM0();
  unsigned old = 0;
  if (lane == 0) old = __hip_atomic_fetch_add(sy(lds, i), 1u, __ATOMIC_RELAXED, __HIP_MEMORY_SCOPE_WORKGROUP);
  old = __builtin_amdgcn_readfirstlane(old);
  wake();
  return old;
}
__device__ __forceinline__ unsigned min4(u32x4 v) { return min(min(v[0], v[1]), min(v[2], v[3])); }
__device__ __forceinline__ u32x4 sy_get4(lptr lds, int i) { return *(volatile LAS u32x4*)(lds + L_SYNC + 4 * i); }
__device__ __forceinline__ u32x2 sy_get2(lptr lds, int i) { return *(volatile LAS u32x2*)(lds + L_SYNC + 4 * i); }
__device__ __forceinline__ void sy_wait4(lptr lds, int i, unsigned v) {
#pragma unroll 1
  for (unsigned n = 0; n < SPIN_LDS; ++n) {
    if ((unsigned)__builtin_amdgcn_readfirstlane(min4(sy_get4(lds, i))) >= v) break;
    __builtin_amdgcn_s_sleep(SLEEP_LDS);
  }
  asm volatile("" ::: "memory");
}
#define LOADER_SLEEP 1
__device__ __forceinline__ void loader_wait(lptr lds, unsigned v) {
#pragma unroll 1
  for (unsigned n = 0; n < SPIN_LDS; ++n) {
    if ((unsigned)__builtin_amdgcn_readfirstlane(min4(sy_get4(lds, SY_CONSUMED))) >= v) break;
    if (LOADER_SLEEP) __builtin_amdgcn_s_sleep(LOADER_SLEEP);
  }
  asm volatile("" ::: "memory");
}
__device__ __forceinline__ void sy_wait4m(lptr lds, unsigned v) {
#pragma unroll 1
  for (unsigned n = 0; n < SPIN_LDS; ++n) {
    const u32x4 l = sy_get4(lds, SY_LANDED);
    if ((unsigned)__builtin_amdgcn_readfirstlane(min(min(l[0], l[1]), l[2])) >= v) break;
    __builtin_amdgcn_s_sleep(SLEEP_LDS);
  }
  asm volatile("" ::: "memory");
}
__device__ __forceinline__ unsigned get_tag(lptr lds) {
  sy_wait(lds, SY_TAGOK, 1u);
  return sy_get(lds, SY_TAG);
}
__device__ __forceinline__ void flag_store(char* ws, size_t off, unsigned tag) {
  __hip_atomic_store((GAS unsigned*)(ws + off), tag, __ATOMIC_RELAXED, __HIP_MEMORY_SCOPE_AGENT);
}
#define SIDE_PUB_ITER 3
#define POLL_SLEEP 32
template <int SLEEP = POLL_SLEEP>
__device__ __forceinline__ void flag_wait(char* ws, size_t off, bool on, unsigned tag) {
#pragma unroll 1
  for (unsigned n = 0; n < SPIN_MEM; ++n) {
    const unsigned v = __hip_atomic_load((GAS unsigned*)(ws + off), __ATOMIC_RELAXED, __HIP_MEMORY_SCOPE_AGENT);
    if (__all(!on || v == tag)) break;
    __builtin_amdgcn_s_sleep(SLEEP);
  }
  asm volatile("" ::: "memory");
}

struct Args {
  const u16* x; const u16* k; const u16* v; const u16* shk; const u16* ssmk; const u16* ssmd;
  const u16* wg; const u16* wu; const u16* wd; const u16* gw;
  char* ws; u16* out;
};

struct Frags { bf16x8 x[4]; s16x4 g[3][2]; s16x4 u[3][2]; };

__device__ constexpr int tri_start(int t) { return 8 * t - (t * (t - 1)) / 2; }
__device__ constexpr int tri_row(int p) {
  return p < 8 ? 0 : p < 15 ? 1 : p < 21 ? 2 : p < 26 ? 3 : p < 30 ? 4 : p < 33 ? 5 : p < 35 ? 6 : 7;
}

template <int N> __device__ __forceinline__ void wait_vm() { asm volatile("s_waitcnt vmcnt(%0)" ::"n"(N) : "memory"); }

struct Filter { unsigned d[5][6]; unsigned dbits; };
__device__ __forceinline__ void load_filter(const Args& a, Filter& f, int ch, int lane) {
  const __amdgpu_buffer_rsrc_t rk = make_rsrc(a.ssmk + (size_t)ch * S, S * 2);
#pragma unroll
  for (int it = 0; it < 5; ++it) {
    const unsigned off = (unsigned)(16 * (lane + 64 * it) - 32);
    const u32x4 v0 = __builtin_bit_cast(u32x4, __builtin_amdgcn_raw_buffer_load_b128(rk, off, 0, 0));
    const u32x2 v1 = __builtin_bit_cast(u32x2, __builtin_amdgcn_raw_buffer_load_b64(rk, off + 16, 0, 0));
    f.d[it][0] = v0[0]; f.d[it][1] = v0[1]; f.d[it][2] = v0[2]; f.d[it][3] = v0[3]; f.d[it][4] = v1[0]; f.d[it][5] = v1[1];
  }
  f.dbits = a.ssmd[ch];
}

template <int NX>
__device__ __forceinline__ void loader(const Args& a, lptr lds, int lw, int x0, int mi, int fi, int rot, Filter& flt, int ch, int lane) {
  const unsigned lds0 = lds_addr(lds);
  const __amdgpu_buffer_rsrc_t rx = make_rsrc(a.x, S * D * 2);
  const __amdgpu_buffer_rsrc_t rg = make_rsrc(a.wg, D * F * 2);
  const __amdgpu_buffer_rsrc_t ru = make_rsrc(a.wu, D * F * 2);
  unsigned vx[NX], vw[4];
#pragma unroll
  for (int i = 0; i < NX; ++i) {
    const int row = 8 * (x0 + i) + (lane >> 3), c = (lane & 7) ^ ((row >> 1) & 7);
    vx[i] = (unsigned)((128 * mi + row) * (D * 2) + c * 16 - 1024 * (i & 3));
  }
#pragma unroll
  for (int i = 0; i < 4; ++i) {
    const int slot = 64 * (4 * lw + i) + lane, k = slot / 12, chp = slot - 12 * k, c = chp ^ (2 * ((k >> 3) & 1));
    vw[i] = (unsigned)(k * (F * 2) + fi * 192 + c * 16 - 1024 * i);
  }
  const unsigned land_addr = lds0 + L_SYNC + 4 * (SY_LANDED + lw);
  auto slice = [&](int s) { const int k = s + rot; return k >= 10 ? k - 10 : k; };
  auto part_a = [&](int s) {
    const unsigned b = lds0 + (s % 3) * STAGE, so = (unsigned)slice(s) * (64 * F * 2);
    dma16xN<4>(rg, b + GB + 4 * lw * 1024, vw, so);
    if (DMA_SPACE) __builtin_amdgcn_s_sleep(DMA_SPACE);
    dma16xN<4>(ru, b + UB + 4 * lw * 1024, vw, so);
    if (DMA_SPACE) __builtin_amdgcn_s_sleep(DMA_SPACE);
  };
  auto part_b = [&](int s) {
    const unsigned b = lds0 + (s % 3) * STAGE, so = (unsigned)slice(s) * 128;
    dma16xN<4>(rx, b + XB + x0 * 1024, vx, so);
    if (DMA_SPACE) __builtin_amdgcn_s_sleep(DMA_SPACE);
    dma16xN<NX - 4>(rx, b + XB + (x0 + 4) * 1024, vx + 4, so);
  };
  constexpr int P = NX + 8;
  if (LOADER_DELAY) __builtin_amdgcn_s_sleep(LOADER_DELAY);
  part_a(0); part_b(0);
  if (PROLOG == 0) { part_a(1); part_b(1); }
  STAMP(1);
  if (PROLOG == 0) wait_vm<P>(); else wait_vm<0>();
  STAMP(2);
  if (PROLOG == 2) { part_a(1); part_b(1); }
  BARRIER();
  if (PROLOG == 1) { part_a(1); part_b(1); }
  part_a(2); part_b(2);
#pragma unroll
  for (int s = 0; s < 9; ++s) {
    if (s == 8) wait_vm<11>(); else wait_vm<P>();
    STAMPI(8 + 2 * s);
    BARRIER();
    STAMPI(9 + 2 * s);
    if (s < 7) { part_a(s + 3); part_b(s + 3); }
    if (s == 7) { FENCE(); load_filter(a, flt, ch, lane); FENCE(); asm volatile("" ::: "memory"); }
  }
}

struct KvTask { u32x4 kr[7], vr[4], wk[4]; };
__device__ __forceinline__ void kv_load(const Args& a, KvTask& t, int task, int tsi, int tdi) {
  const int dc = task % 10, sb = task / 10;
  const int s0 = 64 * tsi + 4 * sb, d0 = 80 * tdi + 8 * dc;
#pragma unroll
  for (int r = 0; r < 7; ++r) {
    const int s = s0 - 3 + r;
    t.kr[r] = *(const u32x4*)(a.k + (size_t)(s < 0 ? 0 : s) * D + d0);
  }
#pragma unroll
  for (int r = 0; r < 4; ++r) t.vr[r] = *(const u32x4*)(a.v + (size_t)(s0 + r) * D + d0);
#pragma unroll
  for (int i = 0; i < 4; ++i) t.wk[i] = *(const u32x4*)(a.shk + (size_t)d0 * 4 + i * 8);
}
__device__ __forceinline__ float dot2(unsigned x, unsigned y, float c) {
  return __builtin_amdgcn_fdot2_f32_bf16(__builtin_bit_cast(bf16x2, x), __builtin_bit_cast(bf16x2, y), c, false);
}
__device__ __forceinline__ float dot2z(unsigned x, unsigned y) {
  float r;
  asm("v_dot2_f32_bf16 %0, %1, %2, 0" : "=v"(r) : "v"(x), "v"(y));
  return r;
}
__device__ __forceinline__ void kv_compute(KvTask& t, lptr lds, int task, int tsi) {
  const int dc = task % 10, sb = task / 10;
  if (tsi == 0 && sb == 0) {
#pragma unroll
    for (int r = 0; r < 3; ++r) t.kr[r] = (u32x4){0u, 0u, 0u, 0u};
  }
  const int sg = 15 - sb;
  lptr dst = lds + L_T + 8 * dc * 128 + (((sg >> 1) ^ (dc & 7)) << 4) + ((sg & 1) << 3);
#pragma unroll
  for (int c = 0; c < 8; ++c) {
    const unsigned w01 = t.wk[c >> 1][2 * (c & 1)], w23 = t.wk[c >> 1][2 * (c & 1) + 1];
    const unsigned sel = (c & 1) ? 0x03020706u : 0x01000504u;
    unsigned pr[6];
#pragma unroll
    for (int i = 0; i < 6; ++i) pr[i] = __builtin_amdgcn_perm(t.kr[i + 1][c >> 1], t.kr[i][c >> 1], sel);
    float o[4];
#pragma unroll
    for (int r = 0; r < 4; ++r) {
      const float ks = dot2(pr[r + 2], w01, dot2z(pr[r], w23));
      o[r] = ks * ((c & 1) ? bf_hi(t.vr[r][c >> 1]) : bf_lo(t.vr[r][c >> 1]));
    }
    *(LAS u32x2*)(dst + c * 128) = (u32x2){pack_bf16(o[3], o[2]), pack_bf16(o[1], o[0])};
  }
}

__device__ __forceinline__ void conv_wave(const Args& a, lptr lds, lptr kc, lptr ur, const Filter& flt, int ch, int nconv, int lane) {
#pragma unroll
  for (int it = 0; it < 5; ++it) {
    const int c = lane + 64 * it;
    if (c < 260) {
      const unsigned a0 = flt.d[it][0], a1 = flt.d[it][1], a2 = flt.d[it][2], a3 = flt.d[it][3], a4 = flt.d[it][4], a5 = flt.d[it][5];
      const unsigned e0 = __builtin_amdgcn_alignbit(a1, a0, 16), e1 = __builtin_amdgcn_alignbit(a2, a1, 16),
                     e2 = __builtin_amdgcn_alignbit(a3, a2, 16), e3 = __builtin_amdgcn_alignbit(a4, a3, 16),
                     e4 = __builtin_amdgcn_alignbit(a5, a4, 16);
      lptr w = kc + 16 * c;
      *(LAS u32x4*)(w) = (u32x4){a0, a1, a2, a3};
      *(LAS u32x4*)(w + KC_COPY) = (u32x4){e0, e1, e2, e3};
      *(LAS u32x4*)(w + 2 * KC_COPY) = (u32x4){a1, a2, a3, a4};
      *(LAS u32x4*)(w + 3 * KC_COPY) = (u32x4){e1, e2, e3, e4};
    }
  }
  const float dcoef = bf_lo(flt.dbits);
  WAIT_LGKM0();
  sy_wait(lds, SY_AREADY, 1u);
  sy_wait(lds, SY_EPI, 4u);
  __builtin_amdgcn_s_setprio(C_PRIO);
  STAMP(4);
  STAMPC(28);

  const int m = lane & 15, g = lane >> 4;
  const int rho = (m + 1) & 3;
  lptr abase = kc + rho * KC_COPY + 2 * (8 * g + m - 31 - rho);
  lptr bbase = ur + 2 * (2000 - 16 * m + 8 * g) - 7 * 512;
  f32x4 acc[8];
#pragma unroll
  for (int t = 0; t < 8; ++t) acc[t] = (f32x4){0.f, 0.f, 0.f, 0.f};
  struct CF { s16x4 a[8][2]; bf16x8 b[8]; };
  auto cread = [&](CF& f, int p, int i) {
    if (p < 16) f.a[p >> 1][p & 1] = *(LAS s16x4*)(abase + 64 * i + 512 * (p >> 1) + 8 * (p & 1));
    else f.b[p - 16] = lds_frag(bbase + 64 * i + 512 * (7 - (p - 16)));
  };
  auto cmma = [&](const CF& f, int p) {
    const int t = tri_row(p), u = p - tri_start(t);
    acc[t + u] = MFMA16(join(f.a[t][0], f.a[t][1]), f.b[u], acc[t + u]);
  };
  auto residue = [&](const CF& cur, CF& nxt, int inext) {
#pragma unroll
    for (int p = 0; p < 36; ++p) {
      cmma(cur, p); FENCE();
      if (p < 24) { cread(nxt, p, inext); FENCE(); }
    }
  };
  CF c0, c1;
#pragma unroll
  for (int p = 0; p < 24; ++p) cread(c0, p, 1);
#pragma unroll 1
  for (int i = 1; i <= 8; i += 2) {
    FENCE();
    residue(c0, c1, i + 1);
    residue(c1, c0, (i + 2 <= 8) ? i + 2 : 8);
  }
  STAMP(5);
  STAMPC(29);
  const __amdgpu_buffer_rsrc_t ry = make_rsrc(a.ws + WS_Y2T + (size_t)ch * S * 2, S * 2);
#pragma unroll
  for (int t = 0; t < 8; t += 2) {
    unsigned pk[2][2];
#pragma unroll
    for (int e = 0; e < 2; ++e) {
      const int s = 256 * (t + e) + 16 * m + 4 * g;
      const u32x2 kv = *(LAS u32x2*)(ur + 2 * (2044 - s));
      pk[e][0] = pack_bf16(acc[t + e][0] + bf_hi(kv[1]) * dcoef, acc[t + e][1] + bf_lo(kv[1]) * dcoef);
      pk[e][1] = pack_bf16(acc[t + e][2] + bf_hi(kv[0]) * dcoef, acc[t + e][3] + bf_lo(kv[0]) * dcoef);
    }
    const auto r0 = __builtin_amdgcn_permlane16_swap(pk[0][0], pk[1][0], false, false);
    const auto r1 = __builtin_amdgcn_permlane16_swap(pk[0][1], pk[1][1], false, false);
    const u32x4 o = {r0[0], r1[0], r0[1], r1[1]};
    const unsigned off = 2 * (256 * (t + (g & 1)) + 16 * m + 8 * (g >> 1));
    __builtin_amdgcn_raw_buffer_store_b128(o, ry, off, 0, SC1);
  }
  STAMP(6);
  WAIT_VM(0);
  STAMP(7);
  if (sy_arrive(lds, SY_CONV, lane) == (unsigned)(nconv - 1)) {
    const unsigned tag = get_tag(lds);
    if (lane == 0) flag_store(a.ws, WS_FLAGY + 4 * blockIdx.x, tag);
  }
}

constexpr int HD = G_HDEPTH;
__device__ constexpr int g_younger(int b) {
  int count = 0, last = 0;
  for (int k = 0; k < HD; ++k) { count += 4; if (k == b) last = count; }
  count += 1;
  for (int s = 0; s < b; ++s) {
    if (s + HD <= 11) { count += 4; if (s + HD == b) last = count; }
    if (s == 9) count += 5;
    if (s >= 2 && s <= 7) { count += 5; if (s + 4 == b + 1) last = count; }
  }
  return count - last;
}
__global__ __launch_bounds__(512, 2) void fused_kernel(Args a) {
  extern __shared__ __attribute__((aligned(16))) char smem[];
  unsigned long long probe_entry = 0;
  if (PROBE_ENTRY) { asm volatile("s_memrealtime %0\n\ts_waitcnt lgkmcnt(0)" : "=s"(probe_entry) :: "memory"); }
  lptr lds = (lptr)smem;
  const int tid = threadIdx.x, lane = tid & 63;
  const int wave = __builtin_amdgcn_readfirstlane(tid >> 6);
  const int bid = blockIdx.x;
  const int xcd = bid & 7, j = bid >> 3;
  const int mi = 8 * (xcd & 1) + (j & 7), fi = 4 * (xcd >> 1) + (j >> 3);
  const int si = 8 * (xcd & 3) + (j & 7), di = 4 * (xcd >> 2) + (j >> 3);
  const int c0 = (5 * bid) >> 1, nconv = 2 + (bid & 1);
  const unsigned lds0 = lds_addr(lds);
  STAMP_INIT();
  STAMP(0);

  if (wave == 7) {
    if (lane < 32) *sy(lds, lane) = 0u;
    WAIT_LGKM0();
  }
  BARRIER();

  if (wave == 7) {
    const unsigned epoch = __hip_atomic_load((GAS unsigned*)(a.ws + WS_EPOCH), __ATOMIC_RELAXED, __HIP_MEMORY_SCOPE_AGENT);
    {
      const int r0 = 128 * mi + 8 * fi, gr = lane >> 4, gq = lane & 15;
      u32x4 gx[2][5], gw[5];
#pragma unroll
      for (int i = 0; i < 5; ++i) {
        gw[i] = ((const u32x4*)a.gw)[gq + 16 * i];
        gx[0][i] = ((const u32x4*)(a.x + (size_t)(r0 + gr) * D))[gq + 16 * i];
        gx[1][i] = ((const u32x4*)(a.x + (size_t)(r0 + 4 + gr) * D))[gq + 16 * i];
      }
      FENCE();
      sy_set(lds, SY_TAG, epoch + 1u, lane);
      sy_set(lds, SY_TAGOK, 1u, lane);
      BARRIER();
#pragma unroll
      for (int pass = 0; pass < 2; ++pass) {
        float sum = 0.f;
#pragma unroll
        for (int i = 0; i < 5; ++i)
#pragma unroll
          for (int e = 0; e < 4; ++e) sum = dot2(gx[pass][i][e], gw[i][e], sum);
#pragma unroll
        for (int m = 8; m >= 1; m >>= 1) sum += __shfl_xor(sum, m);
        if (gq == 0)
          __hip_atomic_store((GAS unsigned*)(a.ws + WS_GATE) + r0 + 4 * pass + gr, __builtin_bit_cast(unsigned, sigmoidf_(sum)),
                             __ATOMIC_RELAXED, __HIP_MEMORY_SCOPE_AGENT);
      }
    }
    const unsigned tag = epoch + 1u;
    STAMP(1);
#pragma unroll 1
    for (int s = 0; s < 9; ++s) {
      BARRIER();
      if (s == 3 && lane == 0) flag_store(a.ws, WS_FLAGKV + 4 * (32 * (bid & 7) + (bid >> 3)), tag);
      if (s == 5) {
        WAIT_VM(0);
        sy_set(lds, SY_GATE, 1u, lane);
      }
      if (s == WD_TOUCH_AT) {
        const char* base = (const char*)a.wd + 640 * (xcd >> 2);
#pragma unroll
        for (int t = 0; t < 4; ++t) {
          const int id = min(64 * t + lane, 239), r = id / 5, c = id - 5 * r;
          touch(base + (size_t)(48 * j + r) * (D * 2) + 128 * c, lds0 + L_DUMMY);
        }
      }
    }
    STAMP(2);
    {
      flag_wait(a.ws, WS_FLAGKV + 4 * (32 * (bid >> 5) + (lane & 31)), true, tag);
      STAMP(8);
      u32x4 uv[3][4];
#pragma unroll
      for (int cw = 0; cw < 3; ++cw) {
        const __amdgpu_buffer_rsrc_t ru = make_rsrc(a.ws + WS_KVR + (size_t)(c0 + (cw < nconv ? cw : 0)) * S * 2, S * 2);
#pragma unroll
        for (int i = 0; i < 4; ++i) uv[cw][i] = ld128(ru, (unsigned)(i * 1024 + lane * 16), 0, SC1);
      }
      FENCE();
#pragma unroll
      for (int cw = 0; cw < 3; ++cw) {
        lptr ur = lds + L_AUX + cw * UR_BYTES;
#pragma unroll
        for (int i = 0; i < 4; ++i) *(LAS u32x4*)(ur + i * 1024 + lane * 16) = uv[cw][i];
        if (lane < 32) *(LAS u32x4*)(ur + 4096 + lane * 16) = (u32x4){0u, 0u, 0u, 0u};
      }
      WAIT_LGKM0();
      sy_set(lds, SY_AREADY, 1u, lane);
    }
    STAMP(9);
    if (bid == 0) {
#pragma unroll
      for (int i = 0; i < 4; ++i) flag_wait(a.ws, WS_FLAGKV + 4 * (lane + 64 * i), true, tag);
      if (lane == 0) flag_store(a.ws, WS_EPOCH, tag);
    }
    sy_wait(lds, SY_WPRE, 4u);
    flag_wait<HPOLL_SLEEP>(a.ws, WS_FLAGH + 4 * (16 * (si >> 1) + (lane & 15)), true, tag);
    sy_set(lds, SY_HREADY, 1u, lane);
    STAMP(10);
    sy_wait(lds, SY_CONV, nconv);
    sy_wait(lds, SY_GSTEP, 1u);
    flag_wait<YPOLL_SLEEP>(a.ws, WS_FLAGY + 4 * (32 * di + (lane & 31)), true, tag);
    STAMP(11);
    {
      const __amdgpu_buffer_rsrc_t ry = make_rsrc(a.ws + WS_Y2T, S * D * 2);
      u32x4 yv[10];
#pragma unroll
      for (int i = 0; i < 10; ++i) {
        const int id = lane + 64 * i;
        yv[i] = ld128(ry, (unsigned)((80 * di + (id >> 3)) * (S * 2) + 64 * si * 2 + (id & 7) * 16), 0, SC1);
      }
#pragma unroll
      for (int i = 0; i < 10; ++i) {
        const int id = lane + 64 * i, w = (id & 7) >> 1;
        const int so = w == 0 ? ystrip_off(0) : w == 1 ? ystrip_off(1) : w == 2 ? ystrip_off(2) : ystrip_off(3);
        *(LAS u32x4*)(lds + so + (id >> 3) * 32 + (id & 1) * 16) = yv[i];
      }
      WAIT_LGKM0();
      sy_set(lds, SY_YREADY, 1u, lane);
    }
    STAMP(12);
    WAIT_VM(0);
    STAMP_DUMP();
    return;
  }

  if (wave >= 4) {
    const int cw = wave - 4;
    const bool active = cw < nconv;
    const int ch = c0 + (active ? cw : 0);
    Filter flt;
    const int rsum = (j >> 3) + (j & 7);
    const int rot1 = ROT1 == 1 ? (3 * (j >> 3) + (j & 7)) % 10 : ROT1 == 2 ? 5 * (rsum & 1) : ROT1 == 3 ? 3 * (rsum % 3) : ROT1 == 4 ? (5 * (rsum & 3)) >> 1 : 0;
    if (cw == 0) loader<6>(a, lds, 0, 0, mi, fi, rot1, flt, ch, lane);
    else loader<5>(a, lds, cw, 1 + 5 * cw, mi, fi, rot1, flt, ch, lane);
    STAMP(3);
    if (active) {
      conv_wave(a, lds, lds + (cw == 2 ? 2 * STAGE : STAGE + cw * KC_BYTES), lds + L_AUX + cw * UR_BYTES, flt, ch, nconv, lane);
    }
    STAMP_DUMP();
    return;
  }

  unsigned long long probe_t0 = PROBE_ENTRY ? probe_entry : PROBE_AT ? __builtin_amdgcn_s_memrealtime() : 0ull;
  if (PROBE_GLOBAL && tid == 0)
    __hip_atomic_store((GAS unsigned long long*)(a.ws + (32u << 20)) + bid, probe_t0, __ATOMIC_RELAXED, __HIP_MEMORY_SCOPE_AGENT);
  (void)probe_t0;
  f32x4 ag[4][3], au[4][3];
  {
    const int tsi = bid >> 3, tdi = bid & 7;
    const int task = 40 * wave + (lane < 40 ? lane : 0);
    KvTask t0;
    kv_load(a, t0, task, tsi, tdi);
    FENCE();
#pragma unroll
    for (int t = 0; t < 4; ++t)
#pragma unroll
      for (int c = 0; c < 3; ++c) {
        ag[t][c] = (f32x4){0.f, 0.f, 0.f, 0.f}; au[t][c] = (f32x4){0.f, 0.f, 0.f, 0.f};
        asm volatile("" : "+v"(ag[t][c]), "+v"(au[t][c]));
      }
    FENCE();
    STAMP(1);
    if (lane < 40) kv_compute(t0, lds, task, tsi);
    sy_arrive(lds, SY_KV, lane);
    sy_wait(lds, SY_KV, 4u);
    STAMP(2);
    const __amdgpu_buffer_rsrc_t rkv = make_rsrc(a.ws + WS_KVR, S * D * 2);
#pragma unroll
    for (int i = 0; i < 3; ++i) {
      const int id = tid + 256 * i;
      if (id < 640) {
        const int dl = id >> 3, jj = id & 7;
        const u32x4 val = *(LAS u32x4*)(lds + L_T + dl * 128 + ((jj ^ ((dl >> 3) & 7)) << 4));
        __builtin_amdgcn_raw_buffer_store_b128(val, rkv, (unsigned)(((80 * tdi + dl) * S + (1984 - 64 * tsi) + 8 * jj) * 2), 0, SC1);
      }
    }
  }
  const int n = lane & 15, g = lane >> 4;
  {
    const int ws_ = wave & 1, wf = wave >> 1;
    const int q = n >> 2, p = lane & 3;
    const int xb0 = XB + (64 * ws_ + n) * 128 + ((g ^ (n >> 1)) << 4);
    int wb_[3];
#pragma unroll
    for (int c = 0; c < 3; ++c) wb_[c] = GB + (8 * g + q) * 192 + 8 * p + 32 * ((3 * wf + c) ^ (g & 1));
    lptr xa[3][2], wa[3][3];
#pragma unroll
    for (int b = 0; b < 3; ++b) {
#pragma unroll
      for (int kk = 0; kk < 2; ++kk) { xa[b][kk] = lds + b * STAGE + (xb0 ^ (kk << 6)); asm volatile("" : "+v"(xa[b][kk])); }
#pragma unroll
      for (int c = 0; c < 3; ++c) { wa[b][c] = lds + b * STAGE + wb_[c]; asm volatile("" : "+v"(wa[b][c])); }
    }
    auto read_piece = [&](Frags& f, int i, int b, int kk) {
      if (i < 4) {
        f.x[i] = lds_frag(xa[b][kk] + i * 2048);
      } else {
        const int c = (i - 4) >> 2, u = ((i - 4) >> 1) & 1, t = (i - 4) & 1;
        const s16x4 v = lds_tr(wa[b][c] + kk * (32 * 192) + u * (UB - GB) + t * (4 * 192));
        if (u == 0) f.g[c][t] = v; else f.u[c][t] = v;
      }
    };
    auto mma_piece = [&](const Frags& f, int i) {
      const int c = i >> 3, t = (i >> 1) & 3;
      if ((i & 1) == 0) ag[t][c] = MFMA16(join(f.g[c][0], f.g[c][1]), f.x[t], ag[t][c]);
      else au[t][c] = MFMA16(join(f.u[c][0], f.u[c][1]), f.x[t], au[t][c]);
    };
    auto half = [&](const Frags& cur, Frags& nxt, int nb, int nkk) {
#pragma unroll
      for (int i = 0; i < 24; ++i) {
        mma_piece(cur, i); FENCE();
        if (i < 16) { read_piece(nxt, i, nb, nkk); FENCE(); }
      }
    };
    BARRIER();
    PROBE(1);
    STAMP(3);
    STAMPC(28);
    __builtin_amdgcn_s_setprio(M_PRIO);
    Frags f0, f1;
#pragma unroll
    for (int i = 0; i < 16; ++i) read_piece(f0, i, 0, 0);
    FENCE();
    STAMPC(27);
    auto iteration = [&](int b, bool drain) {
      FENCE();
      half(f0, f1, b, 1);
      if (drain) WAIT_VM(0);
      WAIT_LGKM0();
      BARRIER();
      FENCE();
      half(f1, f0, (b + 1) % 3, 0);
    };
#pragma unroll 1
    for (int s3 = 0; s3 < 3; ++s3) {
      iteration(0, s3 == 1);
      iteration(1, false);
      iteration(2, false);
    }
    half(f0, f1, 0, 1);
    const __amdgpu_buffer_rsrc_t rhs = make_rsrc(a.ws + WS_HT, S * F * 2);
    const lptr exch = lds + L_FREE + ws_ * 2048 + lane * 8;
    u32x2 keep[4];
    auto epilogue = [&](int t) {
      u32x2 pk[3];
#pragma unroll
      for (int c = 0; c < 3; ++c) {
        const f32x2 s01 = (f32x2){ag[t][c][0], ag[t][c][1]} * -1.4426950408889634f, s23 = (f32x2){ag[t][c][2], ag[t][c][3]} * -1.4426950408889634f;
        const f32x2 d01 = (f32x2){__builtin_amdgcn_exp2f(s01[0]), __builtin_amdgcn_exp2f(s01[1])} + 1.0f;
        const f32x2 d23 = (f32x2){__builtin_amdgcn_exp2f(s23[0]), __builtin_amdgcn_exp2f(s23[1])} + 1.0f;
        const f32x2 h01 = (f32x2){__builtin_amdgcn_rcpf(d01[0]), __builtin_amdgcn_rcpf(d01[1])} * (f32x2){au[t][c][0], au[t][c][1]};
        const f32x2 h23 = (f32x2){__builtin_amdgcn_rcpf(d23[0]), __builtin_amdgcn_rcpf(d23[1])} * (f32x2){au[t][c][2], au[t][c][3]};
        pk[c] = (u32x2){pack_bf16(h01[0], h01[1]), pack_bf16(h23[0], h23[1])};
      }
      const unsigned base = (unsigned)(((8 * mi + 4 * ws_ + t) * 48 + 3 * fi) * 1024 + lane * 16);
      if (wf == 0) {
        __builtin_amdgcn_raw_buffer_store_b128((u32x4){pk[0][0], pk[0][1], pk[1][0], pk[1][1]}, rhs, base, 0, SC1);
        keep[t] = pk[2];
      } else {
        *(LAS u32x2*)(exch + t * 512) = pk[0];
        __builtin_amdgcn_raw_buffer_store_b128((u32x4){pk[1][0], pk[1][1], pk[2][0], pk[2][1]}, rhs, base + 2048, 0, SC1);
      }
    };
#pragma unroll
    for (int t = 0; t < 4; ++t) {
      FENCE();
#pragma unroll
      for (int c = 0; c < 3; ++c) {
        ag[t][c] = MFMA16(join(f1.g[c][0], f1.g[c][1]), f1.x[t], ag[t][c]);
        au[t][c] = MFMA16(join(f1.u[c][0], f1.u[c][1]), f1.x[t], au[t][c]);
      }
      FENCE();
      if (t == 0) { PROBE(2); STAMP(4); STAMPC(29); }
      if (t > 0) epilogue(t - 1);
    }
    FENCE();
    epilogue(3);
    if (wf == 0) {
      sy_wait(lds, SY_EX + ws_, 1u);
#pragma unroll
      for (int t = 0; t < 4; ++t) {
        const u32x2 o = *(LAS u32x2*)(exch + t * 512);
        __builtin_amdgcn_raw_buffer_store_b128((u32x4){keep[t][0], keep[t][1], o[0], o[1]}, rhs,
                                               (unsigned)(((8 * mi + 4 * ws_ + t) * 48 + 3 * fi + 1) * 1024 + lane * 16), 0, SC1);
      }
    } else {
      WAIT_LGKM0();
      sy_set(lds, SY_EX + ws_, 1u, lane);
    }
  }
  STAMP(5);

  const __amdgpu_buffer_rsrc_t rh = make_rsrc(a.ws + WS_HT, S * F * 2);
  const __amdgpu_buffer_rsrc_t rw = make_rsrc(a.wd, F * D * 2);
  const unsigned wr0 = wave == 0 ? wring_off(0) : wave == 1 ? wring_off(1) : wave == 2 ? wring_off(2) : wring_off(3);
  const int rot = 3 * (j >> 3);
  auto kstep = [&](int b) { const int t = b + rot; return 12 * wave + (t >= 12 ? t - 12 : t); };
  unsigned vwd[5];
#pragma unroll
  for (int i = 0; i < 5; ++i) {
    const int slot = 64 * i + lane, row = slot / 10, ch = slot - 10 * row;
    vwd[i] = (unsigned)(row * (D * 2) + 160 * di + ch * 16 - 1024 * (i & 3));
  }
  auto load_w = [&](int b) {
    const unsigned dst = lds0 + wr0 + (b & 3) * WSLOT, so = (unsigned)kstep(b) * (32 * D * 2);
    dma16xN<4>(rw, dst, vwd, so);
    dma16xN<1>(rw, dst + 4096, vwd + 4, so);
  };
  auto load_w1 = [&](int b, int i) {
    dma16(rw, lds0 + wr0 + (b & 3) * WSLOT + i * 1024, vwd[i] + 1024 * (i & 3), (unsigned)kstep(b) * (32 * D * 2));
  };
  struct HS4 { u32x4 f[4]; };
  HS4 H[HD + 1];
  const unsigned voff = lane * 16;
  auto load_h = [&](HS4& h, int b) {
    const unsigned so = (unsigned)(4 * si * 48 + kstep(b)) * 1024;
#define HPOL " sc1"
    asm volatile("buffer_load_dwordx4 %0, %4, %5, %6 offen" HPOL "\n\tbuffer_load_dwordx4 %1, %4, %5, %7 offen" HPOL "\n\t"
                 "buffer_load_dwordx4 %2, %4, %5, %8 offen" HPOL "\n\tbuffer_load_dwordx4 %3, %4, %5, %9 offen" HPOL
                 : "=&v"(h.f[0]), "=&v"(h.f[1]), "=&v"(h.f[2]), "=&v"(h.f[3])
                 : "v"(voff), "s"(rh), "s"(so), "s"(so + 48 * 1024), "s"(so + 96 * 1024), "s"(so + 144 * 1024) : "memory");
  };
  auto load_h1 = [&](HS4& h, int b, int i) {
    const unsigned so = (unsigned)((4 * si + i) * 48 + kstep(b)) * 1024;
    asm volatile("buffer_load_dwordx4 %0, %1, %2, %3 offen" HPOL : "=&v"(h.f[i]) : "v"(voff), "s"(rh), "s"(so) : "memory");
  };
  const int srow = 64 * si + 16 * wave + n;
  u32x2 qv[5];
  FENCE();
  __builtin_amdgcn_s_setprio(3);
  sy_arrive(lds, SY_EPI, lane);
  STAMP(22);
  FENCE();
  WAIT_VM(0);
  if (sy_arrive(lds, SY_HDONE, lane) == 3u) {
    sy_wait(lds, SY_GATE, 1u);
    const unsigned tag = get_tag(lds);
    if (lane == 0) flag_store(a.ws, WS_FLAGH + 4 * (16 * mi + fi), tag);
    WAIT_VM(0);
  }
  sy_wait(lds, SY_EPI, 4u);
  load_w(0); load_w(1); load_w(2); load_w(3);
  STAMP(23);
  f32x4 acc[4][5];
#pragma unroll
  for (int t = 0; t < 4; ++t)
#pragma unroll
    for (int c = 0; c < 5; ++c) { acc[t][c] = (f32x4){0.f, 0.f, 0.f, 0.f}; asm volatile("" : "+v"(acc[t][c])); }
  struct WF { s16x4 f[5][2]; };
  WF w0, w1;
  const lptr wrd = lds + wr0 + (4 * g + (n >> 2)) * 160 + 8 * (lane & 3);
  auto read_w = [&](WF& w, int p, int b) {
    w.f[p >> 1][p & 1] = lds_tr(wrd + (b & 3) * WSLOT + (p & 1) * (16 * 160) + (p >> 1) * 32);
  };
  STAMP(24);
  WAIT_VM(10);
#pragma unroll
  for (int p = 0; p < 10; ++p) read_w(w0, p, 0);
#pragma unroll
  for (int p = 0; p < 10; ++p) read_w(w1, p, 1);
  WAIT_LGKM0();
  load_w(4);
  load_w(5);
  sy_arrive(lds, SY_WPRE, lane);
  __builtin_amdgcn_s_setprio(G_PRIO);
  STAMP(6);
  sy_wait(lds, SY_HREADY, 1u);
  PROBE(3);
  STAMP(7);
#pragma unroll
  for (int b = 0; b < HD; ++b) load_h(H[b], b);
  const float gate = __builtin_bit_cast(float, __hip_atomic_load((GAS unsigned*)(a.ws + WS_GATE) + srow, __ATOMIC_RELAXED, __HIP_MEMORY_SCOPE_AGENT));
  FENCE();
  auto gstep = [&](const WF& cur, WF& nxt, HS4& h, HS4& hn, int b) {
    asm volatile("s_waitcnt vmcnt(%4) lgkmcnt(0)" : "+v"(h.f[0]), "+v"(h.f[1]), "+v"(h.f[2]), "+v"(h.f[3]) : "n"(g_younger(b)) : "memory");
    FENCE();
#pragma unroll
    for (int i = 0; i < 20; ++i) {
      acc[i & 3][i >> 2] = MFMA16(join(cur.f[i >> 2][0], cur.f[i >> 2][1]), __builtin_bit_cast(bf16x8, h.f[i & 3]), acc[i & 3][i >> 2]);
      FENCE();
      if (i < 8 && (i & 1) == 0 && b + HD < 12) { load_h1(hn, b + HD, i >> 1); FENCE(); }
      if (i == 7 && b == 9) {
#pragma unroll
        for (int c = 0; c < 5; ++c) qv[c] = *(const u32x2*)(a.x + (size_t)srow * D + 80 * di + 16 * c + 4 * g);
        asm volatile("" ::: "memory");
        FENCE();
      }
      if (i < 10 && b >= 1 && b + 1 < 12) { read_w(nxt, i, b + 1); FENCE(); }
      if (i >= 10 && (i & 1) == 0 && b >= 2 && b + 4 < 12) { load_w1(b + 4, (i - 10) >> 1); FENCE(); }
      if (b == YPOLL_STEP && i == 17 && wave == 0) { sy_post(lds0 + L_SYNC + 4 * SY_GSTEP, 1u); FENCE(); }
      if (b == YPOLL_STEP && i == 19 && wave == 0) { wake(); FENCE(); }
    }
  };
#pragma unroll
  for (int b = 0; b < 10; b += 2) {
    gstep(w0, w1, H[b % (HD + 1)], H[(b + HD) % (HD + 1)], b);
    if (b == 0) STAMPI(14);
    gstep(w1, w0, H[(b + 1) % (HD + 1)], H[(b + 1 + HD) % (HD + 1)], b + 1);
    STAMPI(15 + (b >> 1));
  }
  gstep(w0, w1, H[10 % (HD + 1)], H[0], 10);
  {
    HS4& h = H[11 % (HD + 1)];
    asm volatile("s_waitcnt vmcnt(%4) lgkmcnt(0)" : "+v"(h.f[0]), "+v"(h.f[1]), "+v"(h.f[2]), "+v"(h.f[3]) : "n"(g_younger(11)) : "memory");
    auto put = [&](int t) {
      if (t != wave) {
#pragma unroll
        for (int c = 0; c < 5; ++c) *(LAS f32x4*)(lds + wr0 + (((t - wave - 1) & 3) * 5 + c) * 1024 + lane * 16) = acc[t][c];
      }
    };
#pragma unroll
    for (int t = 0; t < 4; ++t) {
      FENCE();
#pragma unroll
      for (int c = 0; c < 5; ++c) acc[t][c] = MFMA16(join(w1.f[c][0], w1.f[c][1]), __builtin_bit_cast(bf16x8, h.f[t]), acc[t][c]);
      FENCE();
      if (t > 0) put(t - 1);
    }
    FENCE();
    put(3);
  }
  PROBE(4);
  STAMP(8);

  f32x4 bsum[5];
  {
#pragma unroll
    for (int c = 0; c < 5; ++c) bsum[c] = wave == 0 ? acc[0][c] : wave == 1 ? acc[1][c] : wave == 2 ? acc[2][c] : acc[3][c];
    sy_arrive(lds, SY_RED, lane);
    sy_wait(lds, SY_RED, 4u);
#pragma unroll
    for (int w = 0; w < 4; ++w) {
      if (w != wave) {
#pragma unroll
        for (int c = 0; c < 5; ++c) bsum[c] += *(LAS f32x4*)(lds + wring_off(w) + (((wave - w - 1) & 3) * 5 + c) * 1024 + lane * 16);
      }
    }
  }
  STAMP(9);
  sy_wait(lds, SY_YREADY, 1u);
  STAMP(10);
  {
    const int q = n >> 2, p = lane & 3;
    const __amdgpu_buffer_rsrc_t ro = make_rsrc(a.out, S * D * 2);
    const lptr ystr = lds + (wave == 0 ? ystrip_off(0) : wave == 1 ? ystrip_off(1) : wave == 2 ? ystrip_off(2) : ystrip_off(3));
#pragma unroll
    for (int c = 0; c < 5; ++c) {
      const s16x4 yv = lds_tr(ystr + (16 * c + 4 * g + q) * 32 + 8 * p);
      const u32x2 y = __builtin_bit_cast(u32x2, yv);
      const float o0 = gate * (bf_lo(qv[c][0]) * bf_lo(y[0])) + (1.f - gate) * bsum[c][0];
      const float o1 = gate * (bf_hi(qv[c][0]) * bf_hi(y[0])) + (1.f - gate) * bsum[c][1];
      const float o2 = gate * (bf_lo(qv[c][1]) * bf_lo(y[1])) + (1.f - gate) * bsum[c][2];
      const float o3 = gate * (bf_hi(qv[c][1]) * bf_hi(y[1])) + (1.f - gate) * bsum[c][3];
      __builtin_amdgcn_raw_buffer_store_b64((u32x2){pack_bf16(o0, o1), pack_bf16(o2, o3)}, ro,
                                            (unsigned)((srow * D + 80 * di + 16 * c + 4 * g) * 2), 0, OUT_AUX);
    }
  }
  if (PROBE_GLOBAL) {
    unsigned long long r = PROBE_GLOBAL == 1 ? ~0ull : 0ull;
    for (int i = 0; i < 4; ++i) {
      const unsigned long long v = __hip_atomic_load((GAS unsigned long long*)(a.ws + (32u << 20)) + lane + 64 * i, __ATOMIC_RELAXED, __HIP_MEMORY_SCOPE_AGENT);
      r = PROBE_GLOBAL == 1 ? min(r, v) : max(r, v);
    }
    for (int m = 32; m >= 1; m >>= 1) { const unsigned long long o = __shfl_xor(r, m); r = PROBE_GLOBAL == 1 ? min(r, o) : max(r, o); }
    probe_t0 = __builtin_amdgcn_readfirstlane((unsigned)r) | ((unsigned long long)__builtin_amdgcn_readfirstlane((unsigned)(r >> 32)) << 32);
  }
  PROBE(5);
  STAMP(11);
}

}

extern "C" void kernel_launch(void* const* d_in, const int* in_sizes, int n_in, void* d_out, int out_size, void* d_ws,
                              size_t ws_size, hipStream_t stream) {
  (void)in_sizes; (void)n_in; (void)out_size; (void)ws_size;
  Args a;
  a.x = (const u16*)d_in[0];
  a.k = (const u16*)d_in[1];
  a.v = (const u16*)d_in[2];
  a.shk = (const u16*)d_in[3];
  a.ssmk = (const u16*)d_in[4];
  a.ssmd = (const u16*)d_in[5];
  a.wg = (const u16*)d_in[7];
  a.wu = (const u16*)d_in[8];
  a.wd = (const u16*)d_in[9];
  a.gw = (const u16*)d_in[10];
  a.ws = (char*)d_ws;
  a.out = (u16*)d_out;
  (void)hipFuncSetAttribute((const void*)fused_kernel, hipFuncAttributeMaxDynamicSharedMemorySize, LDS_BYTES);
  fused_kernel<<<256, 512, LDS_BYTES, stream>>>(a);
}
```

```cpp
#include <hip/hip_runtime.h>
#include <hip/hip_bf16.h>

namespace {

typedef unsigned short u16;
typedef __bf16 bf16x8 __attribute__((ext_vector_type(8)));
typedef __bf16 bf16x2 __attribute__((ext_vector_type(2)));
typedef short s16x4 __attribute__((ext_vector_type(4)));
typedef float f32x2 __attribute__((ext_vector_type(2)));
typedef float f32x4 __attribute__((ext_vector_type(4)));
typedef unsigned u32x2 __attribute__((ext_vector_type(2)));
typedef unsigned u32x4 __attribute__((ext_vector_type(4)));
#define LAS __attribute__((address_space(3)))
#define GAS __attribute__((address_space(1)))
typedef LAS char* lptr;

constexpr int S = 2048, D = 640, F = 1536;
constexpr int SC1 = 16;

constexpr size_t WS_HT = 0;
constexpr size_t WS_KVR = WS_HT + (size_t)S * F * 2;
constexpr size_t WS_Y2T = WS_KVR + (size_t)S * D * 2;
constexpr size_t WS_GATE = WS_Y2T + (size_t)S * D * 2;
constexpr size_t WS_FLAGKV = WS_GATE + (size_t)S * 4;
constexpr size_t WS_FLAGH = WS_FLAGKV + 1024;
constexpr size_t WS_FLAGY = WS_FLAGH + 1024;
constexpr size_t WS_EPOCH = WS_FLAGY + 1024;
constexpr size_t WS_END = WS_EPOCH + 256;

constexpr int STAGE = 40960, XB = 0, GB = 16384, UB = 28672;
constexpr int L_AUX = 3 * STAGE;
constexpr int L_T = L_AUX, UR_BYTES = 4608;
constexpr int L_FREE = L_AUX + 3 * UR_BYTES;
constexpr int L_SYNC = L_FREE + 20480 + 2 * 2560;
constexpr int L_DUMMY = L_SYNC + 128;
constexpr int L_END = L_DUMMY + 256;
constexpr int KC_COPY = 4160, KC_BYTES = 4 * KC_COPY;
constexpr int WSLOT = 5120;
__device__ constexpr int wring_off(int w) { return w == 0 ? 0 : w == 1 ? 20480 : w == 2 ? 2 * STAGE + KC_BYTES : L_FREE; }
__device__ constexpr int ystrip_off(int w) { return w < 2 ? STAGE + 2 * KC_BYTES + w * 2560 : L_FREE + 20480 + (w - 2) * 2560; }
static_assert(2 * STAGE + KC_BYTES + 20480 <= 3 * STAGE && STAGE + 2 * KC_BYTES + 2 * 2560 <= 2 * STAGE, "second-phase LDS map");
enum { SY_KV = 8, SY_HDONE = 10, SY_RED = 11, SY_CONV = 12, SY_AREADY = 13, SY_HREADY = 14, SY_YREADY = 15, SY_TAG = 16,
       SY_TAGOK = 17, SY_GSTEP = 18, SY_GATE = 19, SY_EPI = 20, SY_WPRE = 21, SY_EX = 22 };

constexpr int LDS_BYTES = L_END;

#define MFMA16(a, b, c) __builtin_amdgcn_mfma_f32_16x16x32_bf16((a), (b), (c), 0, 0, 0)
#define WAIT_VM(n) asm volatile("s_waitcnt vmcnt(" #n ")" ::: "memory")
#define WAIT_LGKM0() asm volatile("s_waitcnt lgkmcnt(0)" ::: "memory")
#define BARRIER() do { asm volatile("" ::: "memory"); __builtin_amdgcn_s_barrier(); asm volatile("" ::: "memory"); } while (0)
#define FENCE() __builtin_amdgcn_sched_barrier(0)
constexpr unsigned SPIN_LDS = 1u << 22, SPIN_MEM = 1u << 19;

__device__ __forceinline__ unsigned pack_bf16(float lo, float hi) {
  f32x2 v = {lo, hi};
  return __builtin_bit_cast(unsigned, __builtin_convertvector(v, bf16x2));
}
__device__ __forceinline__ float bf_lo(unsigned w) { return __builtin_bit_cast(float, w << 16); }
__device__ __forceinline__ float bf_hi(unsigned w) { return __builtin_bit_cast(float, w & 0xffff0000u); }
__device__ __forceinline__ float sigmoidf_(float x) {
  return __builtin_amdgcn_rcpf(1.0f + __builtin_amdgcn_exp2f(x * -1.4426950408889634f));
}
__device__ __forceinline__ __amdgpu_buffer_rsrc_t make_rsrc(const void* p, unsigned bytes) {
  return __builtin_amdgcn_make_buffer_rsrc(const_cast<void*>(p), 0, bytes, 0x00020000);
}
__device__ __forceinline__ unsigned lds_addr(lptr p) { return (unsigned)(size_t)p; }
__device__ __forceinline__ void dma16(__amdgpu_buffer_rsrc_t r, unsigned dst, unsigned voff, unsigned soff) {
  asm volatile("s_mov_b32 m0, %0\n\ts_nop 0\n\tbuffer_load_dwordx4 %1, %2, %3 offen lds"
               :: "s"(dst), "v"(voff), "s"(r), "s"(soff) : "memory");
}
template <int N>
__device__ __forceinline__ void dma16xN(__amdgpu_buffer_rsrc_t r, unsigned dst, const unsigned* v, unsigned soff) {
  static_assert(N >= 1 && N <= 4, "12-bit instruction offset");
  if constexpr (N == 1)
    asm volatile("s_mov_b32 m0, %0\n\ts_nop 0\n\tbuffer_load_dwordx4 %1, %2, %3 offen lds"
                 :: "s"(dst), "v"(v[0]), "s"(r), "s"(soff) : "memory");
  else if constexpr (N == 2)
    asm volatile("s_mov_b32 m0, %0\n\ts_nop 0\n\tbuffer_load_dwordx4 %1, %3, %4 offen lds\n\t"
                 "buffer_load_dwordx4 %2, %3, %4 offen offset:1024 lds"
                 :: "s"(dst), "v"(v[0]), "v"(v[1]), "s"(r), "s"(soff) : "memory");
  else if constexpr (N == 3)
    asm volatile("s_mov_b32 m0, %0\n\ts_nop 0\n\tbuffer_load_dwordx4 %1, %4, %5 offen lds\n\t"
                 "buffer_load_dwordx4 %2, %4, %5 offen offset:1024 lds\n\tbuffer_load_dwordx4 %3, %4, %5 offen offset:2048 lds"
                 :: "s"(dst), "v"(v[0]), "v"(v[1]), "v"(v[2]), "s"(r), "s"(soff) : "memory");
  else
    asm volatile("s_mov_b32 m0, %0\n\ts_nop 0\n\tbuffer_load_dwordx4 %1, %5, %6 offen lds\n\t"
                 "buffer_load_dwordx4 %2, %5, %6 offen offset:1024 lds\n\tbuffer_load_dwordx4 %3, %5, %6 offen offset:2048 lds\n\t"
                 "buffer_load_dwordx4 %4, %5, %6 offen offset:3072 lds"
                 :: "s"(dst), "v"(v[0]), "v"(v[1]), "v"(v[2]), "v"(v[3]), "s"(r), "s"(soff) : "memory");
}
__device__ __forceinline__ void touch(const void* p, unsigned dummy_lds) {
  asm volatile("s_mov_b32 m0, %0\n\ts_nop 0\n\tglobal_load_lds_dword %1, off" :: "s"(dummy_lds), "v"(p) : "memory");
}
__device__ __forceinline__ s16x4 lds_tr(lptr p) { return __builtin_amdgcn_ds_read_tr16_b64_v4i16((LAS s16x4*)p); }
__device__ __forceinline__ bf16x8 join(s16x4 a, s16x4 b) {
  return __builtin_bit_cast(bf16x8, __builtin_shufflevector(a, b, 0, 1, 2, 3, 4, 5, 6, 7));
}
__device__ __forceinline__ bf16x8 lds_frag(lptr p) { return *(LAS bf16x8*)p; }
__device__ __forceinline__ u32x4 ld128(__amdgpu_buffer_rsrc_t r, unsigned voff, unsigned soff, int aux) {
  return aux ? __builtin_bit_cast(u32x4, __builtin_amdgcn_raw_buffer_load_b128(r, voff, soff, SC1))
             : __builtin_bit_cast(u32x4, __builtin_amdgcn_raw_buffer_load_b128(r, voff, soff, 0));
}

__device__ __forceinline__ LAS unsigned* sy(lptr lds, int i) { return (LAS unsigned*)(lds + L_SYNC) + i; }
__device__ __forceinline__ unsigned sy_get(lptr lds, int i) {
  return __builtin_amdgcn_readfirstlane(__hip_atomic_load(sy(lds, i), __ATOMIC_RELAXED, __HIP_MEMORY_SCOPE_WORKGROUP));
}
__device__ __forceinline__ void wake() { asm volatile("s_wakeup" ::: "memory"); }
__device__ __forceinline__ void sy_set(lptr lds, int i, unsigned v, int lane) {
  if (lane == 0) __hip_atomic_store(sy(lds, i), v, __ATOMIC_RELAXED, __HIP_MEMORY_SCOPE_WORKGROUP);
  asm volatile("s_waitcnt lgkmcnt(0)\n\ts_wakeup" ::: "memory");
}
constexpr int SLEEP_LDS = 4;
constexpr int POLL_SLEEP = 32, HPOLL_SLEEP = 4, YPOLL_SLEEP = 8;
constexpr int YPOLL_STEP = 7;
constexpr int M_PRIO = 2, C_PRIO = 2, G_PRIO = 0;
constexpr int G_HDEPTH = 3;
__device__ __forceinline__ void sy_post(unsigned addr, unsigned v) {
  asm volatile("s_mov_b64 exec, 1\n\tds_write_b32 %0, %1\n\ts_mov_b64 exec, -1" :: "v"(addr), "v"(v) : "memory");
}
__device__ __forceinline__ void sy_wait(lptr lds, int i, unsigned v) {
#pragma unroll 1
  for (unsigned n = 0; n < SPIN_LDS; ++n) {
    if (sy_get(lds, i) >= v) break;
    __builtin_amdgcn_s_sleep(SLEEP_LDS);
  }
  asm volatile("" ::: "memory");
}
__device__ __forceinline__ unsigned sy_arrive(lptr lds, int i, int lane) {
  WAIT_LGKM0();
  unsigned old = 0;
  if (lane == 0) old = __hip_atomic_fetch_add(sy(lds, i), 1u, __ATOMIC_RELAXED, __HIP_MEMORY_SCOPE_WORKGROUP);
  old = __builtin_amdgcn_readfirstlane(old);
  wake();
  return old;
}
__device__ __forceinline__ unsigned get_tag(lptr lds) {
  sy_wait(lds, SY_TAGOK, 1u);
  return sy_get(lds, SY_TAG);
}
__device__ __forceinline__ void flag_store(char* ws, size_t off, unsigned tag) {
  __hip_atomic_store((GAS unsigned*)(ws + off), tag, __ATOMIC_RELAXED, __HIP_MEMORY_SCOPE_AGENT);
}
template <int SLEEP = POLL_SLEEP>
__device__ __forceinline__ void flag_wait(char* ws, size_t off, unsigned tag) {
#pragma unroll 1
  for (unsigned n = 0; n < SPIN_MEM; ++n) {
    const unsigned v = __hip_atomic_load((GAS unsigned*)(ws + off), __ATOMIC_RELAXED, __HIP_MEMORY_SCOPE_AGENT);
    if (__all(v == tag)) break;
    __builtin_amdgcn_s_sleep(SLEEP);
  }
  asm volatile("" ::: "memory");
}

struct Args {
  const u16* x; const u16* k; const u16* v; const u16* shk; const u16* ssmk; const u16* ssmd;
  const u16* wg; const u16* wu; const u16* wd; const u16* gw;
  char* ws; u16* out;
};

struct Frags { bf16x8 x[4]; s16x4 g[3][2]; s16x4 u[3][2]; };

__device__ constexpr int tri_start(int t) { return 8 * t - (t * (t - 1)) / 2; }
__device__ constexpr int tri_row(int p) {
  return p < 8 ? 0 : p < 15 ? 1 : p < 21 ? 2 : p < 26 ? 3 : p < 30 ? 4 : p < 33 ? 5 : p < 35 ? 6 : 7;
}

template <int N> __device__ __forceinline__ void wait_vm() { asm volatile("s_waitcnt vmcnt(%0)" ::"n"(N) : "memory"); }

struct Filter { unsigned d[5][6]; unsigned dbits; };
__device__ __forceinline__ void load_filter(const Args& a, Filter& f, int ch, int lane) {
  const __amdgpu_buffer_rsrc_t rk = make_rsrc(a.ssmk + (size_t)ch * S, S * 2);
#pragma unroll
  for (int it = 0; it < 5; ++it) {
    const unsigned off = (unsigned)(16 * (lane + 64 * it) - 32);
    const u32x4 v0 = __builtin_bit_cast(u32x4, __builtin_amdgcn_raw_buffer_load_b128(rk, off, 0, 0));
    const u32x2 v1 = __builtin_bit_cast(u32x2, __builtin_amdgcn_raw_buffer_load_b64(rk, off + 16, 0, 0));
    f.d[it][0] = v0[0]; f.d[it][1] = v0[1]; f.d[it][2] = v0[2]; f.d[it][3] = v0[3]; f.d[it][4] = v1[0]; f.d[it][5] = v1[1];
  }
  f.dbits = a.ssmd[ch];
}

template <int NX>
__device__ __forceinline__ void loader(const Args& a, lptr lds, int lw, int x0, int mi, int fi, int rot, Filter& flt, int ch, int lane) {
  const unsigned lds0 = lds_addr(lds);
  const __amdgpu_buffer_rsrc_t rx = make_rsrc(a.x, S * D * 2);
  const __amdgpu_buffer_rsrc_t rg = make_rsrc(a.wg, D * F * 2);
  const __amdgpu_buffer_rsrc_t ru = make_rsrc(a.wu, D * F * 2);
  unsigned vx[NX], vw[4];
#pragma unroll
  for (int i = 0; i < NX; ++i) {
    const int row = 8 * (x0 + i) + (lane >> 3), c = (lane & 7) ^ ((row >> 1) & 7);
    vx[i] = (unsigned)((128 * mi + row) * (D * 2) + c * 16 - 1024 * (i & 3));
  }
#pragma unroll
  for (int i = 0; i < 4; ++i) {
    const int slot = 64 * (4 * lw + i) + lane, k = slot / 12, chp = slot - 12 * k, c = chp ^ (2 * ((k >> 3) & 1));
    vw[i] = (unsigned)(k * (F * 2) + fi * 192 + c * 16 - 1024 * i);
  }
  auto slice = [&](int s) { const int k = s + rot; return k >= 10 ? k - 10 : k; };
  auto part_a = [&](int s) {
    const unsigned b = lds0 + (s % 3) * STAGE, so = (unsigned)slice(s) * (64 * F * 2);
    dma16xN<4>(rg, b + GB + 4 * lw * 1024, vw, so);
    dma16xN<4>(ru, b + UB + 4 * lw * 1024, vw, so);
  };
  auto part_b = [&](int s) {
    const unsigned b = lds0 + (s % 3) * STAGE, so = (unsigned)slice(s) * 128;
    dma16xN<4>(rx, b + XB + x0 * 1024, vx, so);
    dma16xN<NX - 4>(rx, b + XB + (x0 + 4) * 1024, vx + 4, so);
  };
  constexpr int P = NX + 8;
  part_a(0); part_b(0);
  part_a(1); part_b(1);
  wait_vm<P>();
  BARRIER();
  part_a(2); part_b(2);
#pragma unroll
  for (int s = 0; s < 9; ++s) {
    if (s == 8) wait_vm<11>(); else wait_vm<P>();
    BARRIER();
    if (s < 7) { part_a(s + 3); part_b(s + 3); }
    if (s == 7) { FENCE(); load_filter(a, flt, ch, lane); FENCE(); asm volatile("" ::: "memory"); }
  }
}

struct KvTask { u32x4 kr[7], vr[4], wk[4]; };
__device__ __forceinline__ void kv_load(const Args& a, KvTask& t, int task, int tsi, int tdi) {
  const int dc = task % 10, sb = task / 10;
  const int s0 = 64 * tsi + 4 * sb, d0 = 80 * tdi + 8 * dc;
#pragma unroll
  for (int r = 0; r < 7; ++r) {
    const int s = s0 - 3 + r;
    t.kr[r] = *(const u32x4*)(a.k + (size_t)(s < 0 ? 0 : s) * D + d0);
  }
#pragma unroll
  for (int r = 0; r < 4; ++r) t.vr[r] = *(const u32x4*)(a.v + (size_t)(s0 + r) * D + d0);
#pragma unroll
  for (int i = 0; i < 4; ++i) t.wk[i] = *(const u32x4*)(a.shk + (size_t)d0 * 4 + i * 8);
}
__device__ __forceinline__ float dot2(unsigned x, unsigned y, float c) {
  return __builtin_amdgcn_fdot2_f32_bf16(__builtin_bit_cast(bf16x2, x), __builtin_bit_cast(bf16x2, y), c, false);
}
__device__ __forceinline__ float dot2z(unsigned x, unsigned y) {
  float r;
  asm("v_dot2_f32_bf16 %0, %1, %2, 0" : "=v"(r) : "v"(x), "v"(y));
  return r;
}
__device__ __forceinline__ void kv_compute(KvTask& t, lptr lds, int task, int tsi) {
  const int dc = task % 10, sb = task / 10;
  if (tsi == 0 && sb == 0) {
#pragma unroll
    for (int r = 0; r < 3; ++r) t.kr[r] = (u32x4){0u, 0u, 0u, 0u};
  }
  const int sg = 15 - sb;
  lptr dst = lds + L_T + 8 * dc * 128 + (((sg >> 1) ^ (dc & 7)) << 4) + ((sg & 1) << 3);
#pragma unroll
  for (int c = 0; c < 8; ++c) {
    const unsigned w01 = t.wk[c >> 1][2 * (c & 1)], w23 = t.wk[c >> 1][2 * (c & 1) + 1];
    const unsigned sel = (c & 1) ? 0x03020706u : 0x01000504u;
    unsigned pr[6];
#pragma unroll
    for (int i = 0; i < 6; ++i) pr[i] = __builtin_amdgcn_perm(t.kr[i + 1][c >> 1], t.kr[i][c >> 1], sel);
    float o[4];
#pragma unroll
    for (int r = 0; r < 4; ++r) {
      const float ks = dot2(pr[r + 2], w01, dot2z(pr[r], w23));
      o[r] = ks * ((c & 1) ? bf_hi(t.vr[r][c >> 1]) : bf_lo(t.vr[r][c >> 1]));
    }
    *(LAS u32x2*)(dst + c * 128) = (u32x2){pack_bf16(o[3], o[2]), pack_bf16(o[1], o[0])};
  }
}

__device__ __forceinline__ void conv_wave(const Args& a, lptr lds, lptr kc, lptr ur, const Filter& flt, int ch, int nconv, int lane) {
#pragma unroll
  for (int it = 0; it < 5; ++it) {
    const int c = lane + 64 * it;
    if (c < 260) {
      const unsigned a0 = flt.d[it][0], a1 = flt.d[it][1], a2 = flt.d[it][2], a3 = flt.d[it][3], a4 = flt.d[it][4], a5 = flt.d[it][5];
      const unsigned e0 = __builtin_amdgcn_alignbit(a1, a0, 16), e1 = __builtin_amdgcn_alignbit(a2, a1, 16),
                     e2 = __builtin_amdgcn_alignbit(a3, a2, 16), e3 = __builtin_amdgcn_alignbit(a4, a3, 16),
                     e4 = __builtin_amdgcn_alignbit(a5, a4, 16);
      lptr w = kc + 16 * c;
      *(LAS u32x4*)(w) = (u32x4){a0, a1, a2, a3};
      *(LAS u32x4*)(w + KC_COPY) = (u32x4){e0, e1, e2, e3};
      *(LAS u32x4*)(w + 2 * KC_COPY) = (u32x4){a1, a2, a3, a4};
      *(LAS u32x4*)(w + 3 * KC_COPY) = (u32x4){e1, e2, e3, e4};
    }
  }
  const float dcoef = bf_lo(flt.dbits);
  WAIT_LGKM0();
  sy_wait(lds, SY_AREADY, 1u);
  sy_wait(lds, SY_EPI, 4u);
  __builtin_amdgcn_s_setprio(C_PRIO);

  const int m = lane & 15, g = lane >> 4;
  const int rho = (m + 1) & 3;
  lptr abase = kc + rho * KC_COPY + 2 * (8 * g + m - 31 - rho);
  lptr bbase = ur + 2 * (2000 - 16 * m + 8 * g) - 7 * 512;
  f32x4 acc[8];
#pragma unroll
  for (int t = 0; t < 8; ++t) acc[t] = (f32x4){0.f, 0.f, 0.f, 0.f};
  struct CF { s16x4 a[8][2]; bf16x8 b[8]; };
  auto cread = [&](CF& f, int p, int i) {
    if (p < 16) f.a[p >> 1][p & 1] = *(LAS s16x4*)(abase + 64 * i + 512 * (p >> 1) + 8 * (p & 1));
    else f.b[p - 16] = lds_frag(bbase + 64 * i + 512 * (7 - (p - 16)));
  };
  auto cmma = [&](const CF& f, int p) {
    const int t = tri_row(p), u = p - tri_start(t);
    acc[t + u] = MFMA16(join(f.a[t][0], f.a[t][1]), f.b[u], acc[t + u]);
  };
  auto residue = [&](const CF& cur, CF& nxt, int inext) {
#pragma unroll
    for (int p = 0; p < 36; ++p) {
      cmma(cur, p); FENCE();
      if (p < 24) { cread(nxt, p, inext); FENCE(); }
    }
  };
  CF c0, c1;
#pragma unroll
  for (int p = 0; p < 24; ++p) cread(c0, p, 1);
#pragma unroll 1
  for (int i = 1; i <= 8; i += 2) {
    FENCE();
    residue(c0, c1, i + 1);
    residue(c1, c0, (i + 2 <= 8) ? i + 2 : 8);
  }
  const __amdgpu_buffer_rsrc_t ry = make_rsrc(a.ws + WS_Y2T + (size_t)ch * S * 2, S * 2);
#pragma unroll
  for (int t = 0; t < 8; t += 2) {
    unsigned pk[2][2];
#pragma unroll
    for (int e = 0; e < 2; ++e) {
      const int s = 256 * (t + e) + 16 * m + 4 * g;
      const u32x2 kv = *(LAS u32x2*)(ur + 2 * (2044 - s));
      pk[e][0] = pack_bf16(acc[t + e][0] + bf_hi(kv[1]) * dcoef, acc[t + e][1] + bf_lo(kv[1]) * dcoef);
      pk[e][1] = pack_bf16(acc[t + e][2] + bf_hi(kv[0]) * dcoef, acc[t + e][3] + bf_lo(kv[0]) * dcoef);
    }
    const auto r0 = __builtin_amdgcn_permlane16_swap(pk[0][0], pk[1][0], false, false);
    const auto r1 = __builtin_amdgcn_permlane16_swap(pk[0][1], pk[1][1], false, false);
    const u32x4 o = {r0[0], r1[0], r0[1], r1[1]};
    const unsigned off = 2 * (256 * (t + (g & 1)) + 16 * m + 8 * (g >> 1));
    __builtin_amdgcn_raw_buffer_store_b128(o, ry, off, 0, SC1);
  }
  WAIT_VM(0);
  if (sy_arrive(lds, SY_CONV, lane) == (unsigned)(nconv - 1)) {
    const unsigned tag = get_tag(lds);
    if (lane == 0) flag_store(a.ws, WS_FLAGY + 4 * blockIdx.x, tag);
  }
}

constexpr int HD = G_HDEPTH;
__device__ constexpr int g_younger(int b) {
  int count = 0, last = 0;
  for (int k = 0; k < HD; ++k) { count += 4; if (k == b) last = count; }
  count += 1;
  for (int s = 0; s < b; ++s) {
    if (s + HD <= 11) { count += 4; if (s + HD == b) last = count; }
    if (s == 9) count += 5;
    if (s >= 2 && s <= 7) { count += 5; if (s + 4 == b + 1) last = count; }
  }
  return count - last;
}
__global__ __launch_bounds__(512, 2) void fused_kernel(Args a) {
  extern __shared__ __attribute__((aligned(16))) char smem[];
  lptr lds = (lptr)smem;
  const int tid = threadIdx.x, lane = tid & 63;
  const int wave = __builtin_amdgcn_readfirstlane(tid >> 6);
  const int bid = blockIdx.x;
  const int xcd = bid & 7, j = bid >> 3;
  const int mi = 8 * (xcd & 1) + (j & 7), fi = 4 * (xcd >> 1) + (j >> 3);
  const int si = 8 * (xcd & 3) + (j & 7), di = 4 * (xcd >> 2) + (j >> 3);
  const int c0 = (5 * bid) >> 1, nconv = 2 + (bid & 1);
  const unsigned lds0 = lds_addr(lds);

  if (wave == 7) {
    if (lane < 32) *sy(lds, lane) = 0u;
    WAIT_LGKM0();
  }
  BARRIER();

  if (wave == 7) {
    const unsigned epoch = __hip_atomic_load((GAS unsigned*)(a.ws + WS_EPOCH), __ATOMIC_RELAXED, __HIP_MEMORY_SCOPE_AGENT);
    {
      const int r0 = 128 * mi + 8 * fi, gr = lane >> 4, gq = lane & 15;
      u32x4 gx[2][5], gw[5];
#pragma unroll
      for (int i = 0; i < 5; ++i) {
        gw[i] = ((const u32x4*)a.gw)[gq + 16 * i];
        gx[0][i] = ((const u32x4*)(a.x + (size_t)(r0 + gr) * D))[gq + 16 * i];
        gx[1][i] = ((const u32x4*)(a.x + (size_t)(r0 + 4 + gr) * D))[gq + 16 * i];
      }
      FENCE();
      sy_set(lds, SY_TAG, epoch + 1u, lane);
      sy_set(lds, SY_TAGOK, 1u, lane);
      BARRIER();
#pragma unroll
      for (int pass = 0; pass < 2; ++pass) {
        float sum = 0.f;
#pragma unroll
        for (int i = 0; i < 5; ++i)
#pragma unroll
          for (int e = 0; e < 4; ++e) sum = dot2(gx[pass][i][e], gw[i][e], sum);
#pragma unroll
        for (int m = 8; m >= 1; m >>= 1) sum += __shfl_xor(sum, m);
        if (gq == 0)
          __hip_atomic_store((GAS unsigned*)(a.ws + WS_GATE) + r0 + 4 * pass + gr, __builtin_bit_cast(unsigned, sigmoidf_(sum)),
                             __ATOMIC_RELAXED, __HIP_MEMORY_SCOPE_AGENT);
      }
    }
    const unsigned tag = epoch + 1u;
#pragma unroll 1
    for (int s = 0; s < 9; ++s) {
      BARRIER();
      if (s == 3 && lane == 0) flag_store(a.ws, WS_FLAGKV + 4 * (32 * (bid & 7) + (bid >> 3)), tag);
      if (s == 5) {
        WAIT_VM(0);
        sy_set(lds, SY_GATE, 1u, lane);
      }
      if (s == 7) {
        const char* base = (const char*)a.wd + 640 * (xcd >> 2);
#pragma unroll
        for (int t = 0; t < 4; ++t) {
          const int id = min(64 * t + lane, 239), r = id / 5, c = id - 5 * r;
          touch(base + (size_t)(48 * j + r) * (D * 2) + 128 * c, lds0 + L_DUMMY);
        }
      }
    }
    {
      flag_wait(a.ws, WS_FLAGKV + 4 * (32 * (bid >> 5) + (lane & 31)), tag);
      u32x4 uv[3][4];
#pragma unroll
      for (int cw = 0; cw < 3; ++cw) {
        const __amdgpu_buffer_rsrc_t ru = make_rsrc(a.ws + WS_KVR + (size_t)(c0 + (cw < nconv ? cw : 0)) * S * 2, S * 2);
#pragma unroll
        for (int i = 0; i < 4; ++i) uv[cw][i] = ld128(ru, (unsigned)(i * 1024 + lane * 16), 0, SC1);
      }
      FENCE();
#pragma unroll
      for (int cw = 0; cw < 3; ++cw) {
        lptr ur = lds + L_AUX + cw * UR_BYTES;
#pragma unroll
        for (int i = 0; i < 4; ++i) *(LAS u32x4*)(ur + i * 1024 + lane * 16) = uv[cw][i];
        if (lane < 32) *(LAS u32x4*)(ur + 4096 + lane * 16) = (u32x4){0u, 0u, 0u, 0u};
      }
      WAIT_LGKM0();
      sy_set(lds, SY_AREADY, 1u, lane);
    }
    if (bid == 0) {
#pragma unroll
      for (int i = 0; i < 4; ++i) flag_wait(a.ws, WS_FLAGKV + 4 * (lane + 64 * i), tag);
      if (lane == 0) flag_store(a.ws, WS_EPOCH, tag);
    }
    sy_wait(lds, SY_WPRE, 4u);
    flag_wait<HPOLL_SLEEP>(a.ws, WS_FLAGH + 4 * (16 * (si >> 1) + (lane & 15)), tag);
    sy_set(lds, SY_HREADY, 1u, lane);
    sy_wait(lds, SY_CONV, nconv);
    sy_wait(lds, SY_GSTEP, 1u);
    flag_wait<YPOLL_SLEEP>(a.ws, WS_FLAGY + 4 * (32 * di + (lane & 31)), tag);
    {
      const __amdgpu_buffer_rsrc_t ry = make_rsrc(a.ws + WS_Y2T, S * D * 2);
      u32x4 yv[10];
#pragma unroll
      for (int i = 0; i < 10; ++i) {
        const int id = lane + 64 * i;
        yv[i] = ld128(ry, (unsigned)((80 * di + (id >> 3)) * (S * 2) + 64 * si * 2 + (id & 7) * 16), 0, SC1);
      }
#pragma unroll
      for (int i = 0; i < 10; ++i) {
        const int id = lane + 64 * i, w = (id & 7) >> 1;
        const int so = w == 0 ? ystrip_off(0) : w == 1 ? ystrip_off(1) : w == 2 ? ystrip_off(2) : ystrip_off(3);
        *(LAS u32x4*)(lds + so + (id >> 3) * 32 + (id & 1) * 16) = yv[i];
      }
      WAIT_LGKM0();
      sy_set(lds, SY_YREADY, 1u, lane);
    }
    WAIT_VM(0);
    return;
  }

  if (wave >= 4) {
    const int cw = wave - 4;
    const bool active = cw < nconv;
    const int ch = c0 + (active ? cw : 0);
    Filter flt;
    const int rot1 = (5 * (((j >> 3) + (j & 7)) & 3)) >> 1;
    if (cw == 0) loader<6>(a, lds, 0, 0, mi, fi, rot1, flt, ch, lane);
    else loader<5>(a, lds, cw, 1 + 5 * cw, mi, fi, rot1, flt, ch, lane);
    if (active) {
      conv_wave(a, lds, lds + (cw == 2 ? 2 * STAGE : STAGE + cw * KC_BYTES), lds + L_AUX + cw * UR_BYTES, flt, ch, nconv, lane);
    }
    return;
  }

  f32x4 ag[4][3], au[4][3];
  {
    const int tsi = bid >> 3, tdi = bid & 7;
    const int task = 40 * wave + (lane < 40 ? lane : 0);
    KvTask t0;
    kv_load(a, t0, task, tsi, tdi);
    FENCE();
#pragma unroll
    for (int t = 0; t < 4; ++t)
#pragma unroll
      for (int c = 0; c < 3; ++c) {
        ag[t][c] = (f32x4){0.f, 0.f, 0.f, 0.f}; au[t][c] = (f32x4){0.f, 0.f, 0.f, 0.f};
        asm volatile("" : "+v"(ag[t][c]), "+v"(au[t][c]));
      }
    FENCE();
    if (lane < 40) kv_compute(t0, lds, task, tsi);
    sy_arrive(lds, SY_KV, lane);
    sy_wait(lds, SY_KV, 4u);
    const __amdgpu_buffer_rsrc_t rkv = make_rsrc(a.ws + WS_KVR, S * D * 2);
#pragma unroll
    for (int i = 0; i < 3; ++i) {
      const int id = tid + 256 * i;
      if (id < 640) {
        const int dl = id >> 3, jj = id & 7;
        const u32x4 val = *(LAS u32x4*)(lds + L_T + dl * 128 + ((jj ^ ((dl >> 3) & 7)) << 4));
        __builtin_amdgcn_raw_buffer_store_b128(val, rkv, (unsigned)(((80 * tdi + dl) * S + (1984 - 64 * tsi) + 8 * jj) * 2), 0, SC1);
      }
    }
  }
  const int n = lane & 15, g = lane >> 4;
  {
    const int ws_ = wave & 1, wf = wave >> 1;
    const int q = n >> 2, p = lane & 3;
    const int xb0 = XB + (64 * ws_ + n) * 128 + ((g ^ (n >> 1)) << 4);
    int wb_[3];
#pragma unroll
    for (int c = 0; c < 3; ++c) wb_[c] = GB + (8 * g + q) * 192 + 8 * p + 32 * ((3 * wf + c) ^ (g & 1));
    lptr xa[3][2], wa[3][3];
#pragma unroll
    for (int b = 0; b < 3; ++b) {
#pragma unroll
      for (int kk = 0; kk < 2; ++kk) { xa[b][kk] = lds + b * STAGE + (xb0 ^ (kk << 6)); asm volatile("" : "+v"(xa[b][kk])); }
#pragma unroll
      for (int c = 0; c < 3; ++c) { wa[b][c] = lds + b * STAGE + wb_[c]; asm volatile("" : "+v"(wa[b][c])); }
    }
    auto read_piece = [&](Frags& f, int i, int b, int kk) {
      if (i < 4) {
        f.x[i] = lds_frag(xa[b][kk] + i * 2048);
      } else {
        const int c = (i - 4) >> 2, u = ((i - 4) >> 1) & 1, t = (i - 4) & 1;
        const s16x4 v = lds_tr(wa[b][c] + kk * (32 * 192) + u * (UB - GB) + t * (4 * 192));
        if (u == 0) f.g[c][t] = v; else f.u[c][t] = v;
      }
    };
    auto mma_piece = [&](const Frags& f, int i) {
      const int c = i >> 3, t = (i >> 1) & 3;
      if ((i & 1) == 0) ag[t][c] = MFMA16(join(f.g[c][0], f.g[c][1]), f.x[t], ag[t][c]);
      else au[t][c] = MFMA16(join(f.u[c][0], f.u[c][1]), f.x[t], au[t][c]);
    };
    auto half = [&](const Frags& cur, Frags& nxt, int nb, int nkk) {
#pragma unroll
      for (int i = 0; i < 24; ++i) {
        mma_piece(cur, i); FENCE();
        if (i < 16) { read_piece(nxt, i, nb, nkk); FENCE(); }
      }
    };
    BARRIER();
    __builtin_amdgcn_s_setprio(M_PRIO);
    Frags f0, f1;
#pragma unroll
    for (int i = 0; i < 16; ++i) read_piece(f0, i, 0, 0);
    FENCE();
    auto iteration = [&](int b, bool drain) {
      FENCE();
      half(f0, f1, b, 1);
      if (drain) WAIT_VM(0);
      WAIT_LGKM0();
      BARRIER();
      FENCE();
      half(f1, f0, (b + 1) % 3, 0);
    };
#pragma unroll 1
    for (int s3 = 0; s3 < 3; ++s3) {
      iteration(0, s3 == 1);
      iteration(1, false);
      iteration(2, false);
    }
    half(f0, f1, 0, 1);
    const __amdgpu_buffer_rsrc_t rhs = make_rsrc(a.ws + WS_HT, S * F * 2);
    const lptr exch = lds + L_FREE + ws_ * 2048 + lane * 8;
    u32x2 keep[4];
    auto epilogue = [&](int t) {
      u32x2 pk[3];
#pragma unroll
      for (int c = 0; c < 3; ++c) {
        const f32x2 s01 = (f32x2){ag[t][c][0], ag[t][c][1]} * -1.4426950408889634f, s23 = (f32x2){ag[t][c][2], ag[t][c][3]} * -1.4426950408889634f;
        const f32x2 d01 = (f32x2){__builtin_amdgcn_exp2f(s01[0]), __builtin_amdgcn_exp2f(s01[1])} + 1.0f;
        const f32x2 d23 = (f32x2){__builtin_amdgcn_exp2f(s23[0]), __builtin_amdgcn_exp2f(s23[1])} + 1.0f;
        const f32x2 h01 = (f32x2){__builtin_amdgcn_rcpf(d01[0]), __builtin_amdgcn_rcpf(d01[1])} * (f32x2){au[t][c][0], au[t][c][1]};
        const f32x2 h23 = (f32x2){__builtin_amdgcn_rcpf(d23[0]), __builtin_amdgcn_rcpf(d23[1])} * (f32x2){au[t][c][2], au[t][c][3]};
        pk[c] = (u32x2){pack_bf16(h01[0], h01[1]), pack_bf16(h23[0], h23[1])};
      }
      const unsigned base = (unsigned)(((8 * mi + 4 * ws_ + t) * 48 + 3 * fi) * 1024 + lane * 16);
      if (wf == 0) {
        __builtin_amdgcn_raw_buffer_store_b128((u32x4){pk[0][0], pk[0][1], pk[1][0], pk[1][1]}, rhs, base, 0, SC1);
        keep[t] = pk[2];
      } else {
        *(LAS u32x2*)(exch + t * 512) = pk[0];
        __builtin_amdgcn_raw_buffer_store_b128((u32x4){pk[1][0], pk[1][1], pk[2][0], pk[2][1]}, rhs, base + 2048, 0, SC1);
      }
    };
#pragma unroll
    for (int t = 0; t < 4; ++t) {
      FENCE();
#pragma unroll
      for (int c = 0; c < 3; ++c) {
        ag[t][c] = MFMA16(join(f1.g[c][0], f1.g[c][1]), f1.x[t], ag[t][c]);
        au[t][c] = MFMA16(join(f1.u[c][0], f1.u[c][1]), f1.x[t], au[t][c]);
      }
      FENCE();
      if (t > 0) epilogue(t - 1);
    }
    FENCE();
    epilogue(3);
    if (wf == 0) {
      sy_wait(lds, SY_EX + ws_, 1u);
#pragma unroll
      for (int t = 0; t < 4; ++t) {
        const u32x2 o = *(LAS u32x2*)(exch + t * 512);
        __builtin_amdgcn_raw_buffer_store_b128((u32x4){keep[t][0], keep[t][1], o[0], o[1]}, rhs,
                                               (unsigned)(((8 * mi + 4 * ws_ + t) * 48 + 3 * fi + 1) * 1024 + lane * 16), 0, SC1);
      }
    } else {
      WAIT_LGKM0();
      sy_set(lds, SY_EX + ws_, 1u, lane);
    }
  }

  const __amdgpu_buffer_rsrc_t rh = make_rsrc(a.ws + WS_HT, S * F * 2);
  const __amdgpu_buffer_rsrc_t rw = make_rsrc(a.wd, F * D * 2);
  const unsigned wr0 = wave == 0 ? wring_off(0) : wave == 1 ? wring_off(1) : wave == 2 ? wring_off(2) : wring_off(3);
  const int rot = 3 * (j >> 3);
  auto kstep = [&](int b) { const int t = b + rot; return 12 * wave + (t >= 12 ? t - 12 : t); };
  unsigned vwd[5];
#pragma unroll
  for (int i = 0; i < 5; ++i) {
    const int slot = 64 * i + lane, row = slot / 10, ch = slot - 10 * row;
    vwd[i] = (unsigned)(row * (D * 2) + 160 * di + ch * 16 - 1024 * (i & 3));
  }
  auto load_w = [&](int b) {
    const unsigned dst = lds0 + wr0 + (b & 3) * WSLOT, so = (unsigned)kstep(b) * (32 * D * 2);
    dma16xN<4>(rw, dst, vwd, so);
    dma16xN<1>(rw, dst + 4096, vwd + 4, so);
  };
  auto load_w1 = [&](int b, int i) {
    dma16(rw, lds0 + wr0 + (b & 3) * WSLOT + i * 1024, vwd[i] + 1024 * (i & 3), (unsigned)kstep(b) * (32 * D * 2));
  };
  struct HS4 { u32x4 f[4]; };
  HS4 H[HD + 1];
  const unsigned voff = lane * 16;
  auto load_h = [&](HS4& h, int b) {
    const unsigned so = (unsigned)(4 * si * 48 + kstep(b)) * 1024;
    asm volatile("buffer_load_dwordx4 %0, %4, %5, %6 offen sc1\n\tbuffer_load_dwordx4 %1, %4, %5, %7 offen sc1\n\t"
                 "buffer_load_dwordx4 %2, %4, %5, %8 offen sc1\n\tbuffer_load_dwordx4 %3, %4, %5, %9 offen sc1"
                 : "=&v"(h.f[0]), "=&v"(h.f[1]), "=&v"(h.f[2]), "=&v"(h.f[3])
                 : "v"(voff), "s"(rh), "s"(so), "s"(so + 48 * 1024), "s"(so + 96 * 1024), "s"(so + 144 * 1024) : "memory");
  };
  auto load_h1 = [&](HS4& h, int b, int i) {
    const unsigned so = (unsigned)((4 * si + i) * 48 + kstep(b)) * 1024;
    asm volatile("buffer_load_dwordx4 %0, %1, %2, %3 offen sc1" : "=&v"(h.f[i]) : "v"(voff), "s"(rh), "s"(so) : "memory");
  };
  const int srow = 64 * si + 16 * wave + n;
  u32x2 qv[5];
  FENCE();
  __builtin_amdgcn_s_setprio(3);
  sy_arrive(lds, SY_EPI, lane);
  FENCE();
  WAIT_VM(0);
  if (sy_arrive(lds, SY_HDONE, lane) == 3u) {
    sy_wait(lds, SY_GATE, 1u);
    const unsigned tag = get_tag(lds);
    if (lane == 0) flag_store(a.ws, WS_FLAGH + 4 * (16 * mi + fi), tag);
  }
  sy_wait(lds, SY_EPI, 4u);
  load_w(0); load_w(1); load_w(2); load_w(3);
  f32x4 acc[4][5];
#pragma unroll
  for (int t = 0; t < 4; ++t)
#pragma unroll
    for (int c = 0; c < 5; ++c) { acc[t][c] = (f32x4){0.f, 0.f, 0.f, 0.f}; asm volatile("" : "+v"(acc[t][c])); }
  struct WF { s16x4 f[5][2]; };
  WF w0, w1;
  const lptr wrd = lds + wr0 + (4 * g + (n >> 2)) * 160 + 8 * (lane & 3);
  auto read_w = [&](WF& w, int p, int b) {
    w.f[p >> 1][p & 1] = lds_tr(wrd + (b & 3) * WSLOT + (p & 1) * (16 * 160) + (p >> 1) * 32);
  };
  WAIT_VM(10);
#pragma unroll
  for (int p = 0; p < 10; ++p) read_w(w0, p, 0);
#pragma unroll
  for (int p = 0; p < 10; ++p) read_w(w1, p, 1);
  WAIT_LGKM0();
  load_w(4);
  load_w(5);
  sy_arrive(lds, SY_WPRE, lane);
  __builtin_amdgcn_s_setprio(G_PRIO);
  sy_wait(lds, SY_HREADY, 1u);
#pragma unroll
  for (int b = 0; b < HD; ++b) load_h(H[b], b);
  const float gate = __builtin_bit_cast(float, __hip_atomic_load((GAS unsigned*)(a.ws + WS_GATE) + srow, __ATOMIC_RELAXED, __HIP_MEMORY_SCOPE_AGENT));
  FENCE();
  auto gstep = [&](const WF& cur, WF& nxt, HS4& h, HS4& hn, int b) {
    asm volatile("s_waitcnt vmcnt(%4) lgkmcnt(0)" : "+v"(h.f[0]), "+v"(h.f[1]), "+v"(h.f[2]), "+v"(h.f[3]) : "n"(g_younger(b)) : "memory");
    FENCE();
#pragma unroll
    for (int i = 0; i < 20; ++i) {
      acc[i & 3][i >> 2] = MFMA16(join(cur.f[i >> 2][0], cur.f[i >> 2][1]), __builtin_bit_cast(bf16x8, h.f[i & 3]), acc[i & 3][i >> 2]);
      FENCE();
      if (i < 8 && (i & 1) == 0 && b + HD < 12) { load_h1(hn, b + HD, i >> 1); FENCE(); }
      if (i == 7 && b == 9) {
#pragma unroll
        for (int c = 0; c < 5; ++c) qv[c] = *(const u32x2*)(a.x + (size_t)srow * D + 80 * di + 16 * c + 4 * g);
        asm volatile("" ::: "memory");
        FENCE();
      }
      if (i < 10 && b >= 1 && b + 1 < 12) { read_w(nxt, i, b + 1); FENCE(); }
      if (i >= 10 && (i & 1) == 0 && b >= 2 && b + 4 < 12) { load_w1(b + 4, (i - 10) >> 1); FENCE(); }
      if (b == YPOLL_STEP && i == 17 && wave == 0) { sy_post(lds0 + L_SYNC + 4 * SY_GSTEP, 1u); FENCE(); }
      if (b == YPOLL_STEP && i == 19 && wave == 0) { wake(); FENCE(); }
    }
  };
#pragma unroll
  for (int b = 0; b < 10; b += 2) {
    gstep(w0, w1, H[b % (HD + 1)], H[(b + HD) % (HD + 1)], b);
    gstep(w1, w0, H[(b + 1) % (HD + 1)], H[(b + 1 + HD) % (HD + 1)], b + 1);
  }
  gstep(w0, w1, H[10 % (HD + 1)], H[0], 10);
  {
    HS4& h = H[11 % (HD + 1)];
    asm volatile("s_waitcnt vmcnt(%4) lgkmcnt(0)" : "+v"(h.f[0]), "+v"(h.f[1]), "+v"(h.f[2]), "+v"(h.f[3]) : "n"(g_younger(11)) : "memory");
    auto put = [&](int t) {
      if (t != wave) {
#pragma unroll
        for (int c = 0; c < 5; ++c) *(LAS f32x4*)(lds + wr0 + (((t - wave - 1) & 3) * 5 + c) * 1024 + lane * 16) = acc[t][c];
      }
    };
#pragma unroll
    for (int t = 0; t < 4; ++t) {
      FENCE();
#pragma unroll
      for (int c = 0; c < 5; ++c) acc[t][c] = MFMA16(join(w1.f[c][0], w1.f[c][1]), __builtin_bit_cast(bf16x8, h.f[t]), acc[t][c]);
      FENCE();
      if (t > 0) put(t - 1);
    }
    FENCE();
    put(3);
  }

  f32x4 bsum[5];
  {
#pragma unroll
    for (int c = 0; c < 5; ++c) bsum[c] = wave == 0 ? acc[0][c] : wave == 1 ? acc[1][c] : wave == 2 ? acc[2][c] : acc[3][c];
    sy_arrive(lds, SY_RED, lane);
    sy_wait(lds, SY_RED, 4u);
#pragma unroll
    for (int w = 0; w < 4; ++w) {
      if (w != wave) {
#pragma unroll
        for (int c = 0; c < 5; ++c) bsum[c] += *(LAS f32x4*)(lds + wring_off(w) + (((wave - w - 1) & 3) * 5 + c) * 1024 + lane * 16);
      }
    }
  }
  sy_wait(lds, SY_YREADY, 1u);
  {
    const int q = n >> 2, p = lane & 3;
    const __amdgpu_buffer_rsrc_t ro = make_rsrc(a.out, S * D * 2);
    const lptr ystr = lds + (wave == 0 ? ystrip_off(0) : wave == 1 ? ystrip_off(1) : wave == 2 ? ystrip_off(2) : ystrip_off(3));
#pragma unroll
    for (int c = 0; c < 5; ++c) {
      const s16x4 yv = lds_tr(ystr + (16 * c + 4 * g + q) * 32 + 8 * p);
      const u32x2 y = __builtin_bit_cast(u32x2, yv);
      const float o0 = gate * (bf_lo(qv[c][0]) * bf_lo(y[0])) + (1.f - gate) * bsum[c][0];
      const float o1 = gate * (bf_hi(qv[c][0]) * bf_hi(y[0])) + (1.f - gate) * bsum[c][1];
      const float o2 = gate * (bf_lo(qv[c][1]) * bf_lo(y[1])) + (1.f - gate) * bsum[c][2];
      const float o3 = gate * (bf_hi(qv[c][1]) * bf_hi(y[1])) + (1.f - gate) * bsum[c][3];
      __builtin_amdgcn_raw_buffer_store_b64((u32x2){pack_bf16(o0, o1), pack_bf16(o2, o3)}, ro,
                                            (unsigned)((srow * D + 80 * di + 16 * c + 4 * g) * 2), 0, 0);
    }
  }
}

}

extern "C" void kernel_launch(void* const* d_in, const int* in_sizes, int n_in, void* d_out, int out_size, void* d_ws,
                              size_t ws_size, hipStream_t stream) {
  (void)in_sizes; (void)n_in; (void)out_size; (void)ws_size;
  Args a;
  a.x = (const u16*)d_in[0];
  a.k = (const u16*)d_in[1];
  a.v = (const u16*)d_in[2];
  a.shk = (const u16*)d_in[3];
  a.ssmk = (const u16*)d_in[4];
  a.ssmd = (const u16*)d_in[5];
  a.wg = (const u16*)d_in[7];
  a.wu = (const u16*)d_in[8];
  a.wd = (const u16*)d_in[9];
  a.gw = (const u16*)d_in[10];
  a.ws = (char*)d_ws;
  a.out = (u16*)d_out;
  (void)hipFuncSetAttribute((const void*)fused_kernel, hipFuncAttributeMaxDynamicSharedMemorySize, LDS_BYTES);
  fused_kernel<<<256, 512, LDS_BYTES, stream>>>(a);
}
```
